```python
import torch

def _softmax(x, axis=-1):
    return torch.softmax(x, dim=axis)
def _gelu(x):
    return torch.nn.functional.gelu(x, approximate="tanh")
def _silu(x):
    return torch.nn.functional.silu(x)
def _sigmoid(x):
    return torch.sigmoid(x)
def _layernorm(x, axis=-1, eps=1e-5):
    m = x.mean(dim=axis, keepdim=True)
    v = x.var(dim=axis, keepdim=True, unbiased=False)
    return (x - m) / torch.sqrt(v + eps)
def _scatter_add(tgt, idx, src, axis=0):
    return tgt.clone().index_add_(axis, idx, src)
def _segment_sum(src, seg, n):
    out = torch.zeros((n,) + src.shape[1:], dtype=src.dtype)
    return out.index_add_(0, seg, src)
def _pad(x, pw):
    flat = tuple(v for pair in reversed(pw) for v in pair)
    return torch.nn.functional.pad(x, flat)

kernel_name = 'standalone__gqa_softmax__near_ridge'

def setup_inputs():

    _w = (lambda a: (lambda t: t.to(torch.bfloat16) if t.is_floating_point() else t)(torch.as_tensor(a)))
    B, Hq, Hkv, Sq, Sk, D = 8, 32, 32, 432, 1024, 192
    return {
        "q": _w((lambda *s: torch.randn(*s, dtype=torch.bfloat16) * 0.8)(B, Hq, Sq, D)),
        "k": _w((lambda *s: torch.randn(*s, dtype=torch.bfloat16) * 0.8)(B, Hkv, Sk, D)),
        "v": _w((lambda *s: torch.randn(*s, dtype=torch.bfloat16) * 0.05)(B, Hkv, Sk, D)),
    }

def reference(q, k, v):
    B, Hq, Sq, D = q.shape
    Hkv = k.shape[1]
    rep = Hq // Hkv
    k = (lambda x, n, axis: torch.repeat_interleave(x, n, dim=axis))(k, rep, axis=1)
    v = (lambda x, n, axis: torch.repeat_interleave(x, n, dim=axis))(v, rep, axis=1)
    scale = D ** -0.5
    scores = torch.einsum("bhqd,bhkd->bhqk", q, k) * scale
    p = _softmax(scores, axis=-1)
    return torch.einsum("bhqk,bhkd->bhqd", p, v)
```

```cpp
#include <hip/hip_runtime.h>
#include <hip/hip_bf16.h>
constexpr int B=8,H=32,SQ=432,SK=1024,D=192;
__device__ __forceinline__ float bf2f(unsigned short x){ return __uint_as_float(((unsigned)x)<<16); }
__global__ __launch_bounds__(256) void naive_attn(const unsigned short* __restrict__ q,const unsigned short* __restrict__ k,const unsigned short* __restrict__ v,__hip_bfloat16* __restrict__ o){
  __shared__ float sq[4][D];
  __shared__ float sp[4][SK];
  int w=threadIdx.x>>6, lane=threadIdx.x&63;
  int bh=blockIdx.y; int row=blockIdx.x*4+w;
  const unsigned short* qr=q+((size_t)bh*SQ+row)*D;
  for(int d=lane;d<D;d+=64) sq[w][d]=bf2f(qr[d]);
  __syncthreads();
  float mx=-1e30f;
  const float scale=0.07216878364870322f;
  for(int j=lane;j<SK;j+=64){
    const unsigned short* kr=k+((size_t)bh*SK+j)*D;
    float s=0;
    for(int d=0;d<D;d++) s+=sq[w][d]*bf2f(kr[d]);
    s*=scale; sp[w][j]=s; mx=fmaxf(mx,s);
  }
  for(int off=32;off;off>>=1) mx=fmaxf(mx,__shfl_xor(mx,off));
  float sum=0;
  for(int j=lane;j<SK;j+=64){ float p=__expf(sp[w][j]-mx); sp[w][j]=p; sum+=p; }
  for(int off=32;off;off>>=1) sum+=__shfl_xor(sum,off);
  __syncthreads();
  for(int d=lane;d<D;d+=64){
    float acc=0;
    for(int j=0;j<SK;j++) acc+=sp[w][j]*bf2f(v[((size_t)bh*SK+j)*D+d]);
    o[((size_t)bh*SQ+row)*D+d]=__float2bfloat16(acc/sum);
  }
}
extern "C" void kernel_launch(void* const* d_in, const int* in_sizes, int n_in, void* d_out, int out_size, void* d_ws, size_t ws_size, hipStream_t stream){
  naive_attn<<<dim3(SQ/4,B*H),256,0,stream>>>((const unsigned short*)d_in[0],(const unsigned short*)d_in[1],(const unsigned short*)d_in[2],(__hip_bfloat16*)d_out);
}
```

```cpp
#include <hip/hip_runtime.h>
#include <hip/hip_bf16.h>

__global__ __launch_bounds__(256, 1) void attn_fwd_d192(const void* __restrict__ q, const void* __restrict__ k,
                                                        const void* __restrict__ v, void* __restrict__ o) {
  __shared__ __attribute__((aligned(1024))) char lds[163840];
  const unsigned wave = __builtin_amdgcn_readfirstlane(threadIdx.x >> 6);
  asm volatile(
      "L_start:\n"
      "s_load_dwordx8 s[4:11], %0, 0x0\n"
      "v_mbcnt_lo_u32_b32 v0, -1, 0\n"
      "v_mbcnt_hi_u32_b32 v0, -1, v0\n"
      "s_mov_b32 s61, %2\n"
      "s_mov_b32 s65, %3\n"
      "v_lshrrev_b32 v28, 3, v0\n"
      "v_and_b32 v29, 7, v0\n"
      "v_add_u32 v30, 0, v28\n"
      "v_mul_u32_u24 v30, 171, v30\n"
      "v_lshrrev_b32 v30, 9, v30\n"
      "v_and_b32 v30, 6, v30\n"
      "v_xor_b32 v30, v30, v29\n"
      "v_lshlrev_b32 v30, 4, v30\n"
      "v_lshl_add_u32 v1, v28, 7, v30\n"
      "v_add_u32 v30, 8, v28\n"
      "v_mul_u32_u24 v30, 171, v30\n"
      "v_lshrrev_b32 v30, 9, v30\n"
      "v_and_b32 v30, 6, v30\n"
      "v_xor_b32 v30, v30, v29\n"
      "v_lshlrev_b32 v30, 4, v30\n"
      "v_lshl_add_u32 v2, v28, 7, v30\n"
      "v_add_u32 v30, 16, v28\n"
      "v_mul_u32_u24 v30, 171, v30\n"
      "v_lshrrev_b32 v30, 9, v30\n"
      "v_and_b32 v30, 6, v30\n"
      "v_xor_b32 v30, v30, v29\n"
      "v_lshlrev_b32 v30, 4, v30\n"
      "v_lshl_add_u32 v3, v28, 7, v30\n"
      "s_and_b32 s43, %1, 7\n"
      "s_lshr_b32 s63, %1, 4\n"
      "s_lshl_b32 s63, s63, 3\n"
      "s_add_u32 s43, s43, s63\n"
      "s_mul_i32 s14, s61, 6144\n"
      "s_add_u32 s44, s14, s65\n"
      "s_add_u32 s45, s44, 73728\n"
      "s_mul_i32 s13, s43, 0\n"
      "s_add_u32 s15, s13, 0\n"
      "s_and_b32 s15, s15, 15\n"
      "s_mul_i32 s15, s15, 24576\n"
      "s_add_u32 s36, s15, s14\n"
      "s_add_u32 s15, s13, 1\n"
      "s_and_b32 s15, s15, 15\n"
      "s_mul_i32 s15, s15, 24576\n"
      "s_add_u32 s37, s15, s14\n"
      "s_add_u32 s15, s13, 2\n"
      "s_and_b32 s15, s15, 15\n"
      "s_mul_i32 s15, s15, 24576\n"
      "s_add_u32 s38, s15, s14\n"
      "s_mul_i32 s82, s43, 331776\n"
      "s_lshr_b32 s64, %1, 3\n"
      "s_and_b32 s64, s64, 1\n"
      "s_mul_i32 s63, s64, 86016\n"
      "s_add_u32 s82, s82, s63\n"
      "s_mul_i32 s63, s61, 18432\n"
      "s_add_u32 s52, s82, s63\n"
      "s_add_u32 s53, s52, 6144\n"
      "s_add_u32 s54, s52, 12288\n"
      "s_lshr_b32 s63, s61, 1\n"
      "s_add_u32 s63, s63, 12\n"
      "s_mul_i32 s63, s63, 6144\n"
      "s_add_u32 s55, s82, s63\n"
      "s_mul_i32 s83, s43, 786432\n"
      "s_mov_b32 s22, 786432\n"
      "s_mov_b32 s23, 0x20000\n"
      "s_mov_b32 s26, 786432\n"
      "s_mov_b32 s27, 0x20000\n"
      "s_mov_b32 s30, 42467328\n"
      "s_mov_b32 s31, 0x20000\n"
      "s_mov_b32 s74, 42467328\n"
      "s_mov_b32 s75, 0x20000\n"
      "s_lshr_b32 s63, %1, 4\n"
      "s_and_b32 s63, s63, 1\n"
      "L_stag:\n"
      "s_cmp_eq_u32 s63, 0\n"
      "s_cbranch_scc1 L_stag_end\n"
      "s_sleep 30\n"
      "s_sub_u32 s63, s63, 1\n"
      "s_branch L_stag\n"
      "L_stag_end:\n"
      "s_waitcnt lgkmcnt(0)\n"
      "s_add_u32 s20, s6, s83\n"
      "s_addc_u32 s21, s7, 0\n"
      "s_and_b32 s21, s21, 0xffff\n"
      "s_add_u32 s24, s8, s83\n"
      "s_addc_u32 s25, s9, 0\n"
      "s_and_b32 s25, s25, 0xffff\n"
      "s_add_u32 m0, s44, 0\n"
      "s_add_u32 s43, s36, 0\n"
      "buffer_load_dwordx4 v1, s[20:23], s43 offen offset:0 lds\n"
      "buffer_load_dwordx4 v2, s[20:23], s43 offen offset:1024 lds\n"
      "buffer_load_dwordx4 v3, s[20:23], s43 offen offset:2048 lds\n"
      "s_add_u32 m0, s44, 3072\n"
      "s_add_u32 s43, s36, 3072\n"
      "buffer_load_dwordx4 v1, s[20:23], s43 offen offset:0 lds\n"
      "buffer_load_dwordx4 v2, s[20:23], s43 offen offset:1024 lds\n"
      "buffer_load_dwordx4 v3, s[20:23], s43 offen offset:2048 lds\n"
      "s_mov_b32 s28, s4\n"
      "s_and_b32 s29, s5, 0xffff\n"
      "s_mov_b32 s72, s10\n"
      "s_and_b32 s73, s11, 0xffff\n"
      "s_add_u32 m0, s44, 49152\n"
      "s_add_u32 s43, s52, 0\n"
      "buffer_load_dwordx4 v1, s[28:31], s43 offen offset:0 nt lds\n"
      "buffer_load_dwordx4 v2, s[28:31], s43 offen offset:1024 nt lds\n"
      "buffer_load_dwordx4 v3, s[28:31], s43 offen offset:2048 nt lds\n"
      "s_add_u32 m0, s44, 52224\n"
      "s_add_u32 s43, s52, 3072\n"
      "buffer_load_dwordx4 v1, s[28:31], s43 offen offset:0 nt lds\n"
      "buffer_load_dwordx4 v2, s[28:31], s43 offen offset:1024 nt lds\n"
      "buffer_load_dwordx4 v3, s[28:31], s43 offen offset:2048 nt lds\n"
      "s_mul_i32 s43, s61, 21504\n"
      "s_add_u32 s43, s43, s82\n"
      "s_add_u32 s43, s43, 165888\n"
      "s_add_u32 s96, s4, s43\n"
      "s_addc_u32 s97, s5, 0\n"
      "s_and_b32 s97, s97, 0xffff\n"
      "s_sub_u32 s98, 42467328, s43\n"
      "s_min_u32 s98, s98, 21504\n"
      "s_mov_b32 s99, 0x20000\n"
      "s_mov_b32 s34, 0xffffb800\n"
      "v_mul_u32_u24 v254, 96, v0\n"
      "s_mov_b32 s86, 1\n"
      "s_and_b32 s88, s61, 1\n"
      "s_mul_i32 s88, s88, 3072\n"
      "s_lshr_b32 s89, s61, 1\n"
      "s_mul_i32 s89, s89, 6144\n"
      "s_add_u32 s89, s89, 149504\n"
      "s_add_u32 s87, s89, s88\n"
      "s_add_u32 s87, s87, s65\n"
      "s_mov_b32 s90, s20\n"
      "s_mov_b32 s93, s24\n"
      "s_mov_b32 s91, s21\n"
      "s_mov_b32 s94, s25\n"
      "s_mov_b32 s92, s22\n"
      "s_mov_b32 s95, s26\n"
      "v_and_b32 v31, 15, v0\n"
      "v_lshrrev_b32 v32, 4, v0\n"
      "v_mul_u32_u24 v28, 384, v31\n"
      "v_lshrrev_b32 v30, 3, v0\n"
      "v_mul_u32_u24 v250, 384, v30\n"
      "v_and_b32 v29, 3, v0\n"
      "v_lshl_add_u32 v250, v29, 5, v250\n"
      "v_bfe_u32 v29, v0, 2, 1\n"
      "v_lshl_add_u32 v250, v29, 4, v250\n"
      "v_lshlrev_b32 v29, 3, v31\n"
      "v_lshl_add_u32 v251, v32, 9, v29\n"
      "v_bfe_u32 v29, v0, 1, 1\n"
      "v_lshlrev_b32 v29, 7, v29\n"
      "v_lshl_add_u32 v17, v32, 8, v29\n"
      "v_and_b32 v29, 1, v0\n"
      "v_lshl_add_u32 v17, v29, 6, v17\n"
      "v_bfe_u32 v29, v0, 2, 2\n"
      "v_lshl_add_u32 v17, v29, 3, v17\n"
      "v_and_b32 v29, 1, v32\n"
      "v_lshlrev_b32 v29, 5, v29\n"
      "v_xor_b32 v17, v17, v29\n"
      "v_and_b32 v29, 6, v31\n"
      "v_xor_b32 v29, v29, v32\n"
      "v_lshl_add_u32 v29, v29, 4, v28\n"
      "v_add_u32 v6, s65, v29\n"
      "v_xor_b32 v7, 64, v6\n"
      "v_mov_b32 v4, v6\n"
      "v_mov_b32 v5, v7\n"
      "v_lshrrev_b32 v28, 2, v0\n"
      "v_and_b32 v29, 6, v28\n"
      "v_mul_u32_u24 v28, 384, v28\n"
      "v_lshl_add_u32 v28, v29, 4, v28\n"
      "v_and_b32 v29, 3, v0\n"
      "v_lshl_add_u32 v28, v29, 3, v28\n"
      "s_add_u32 s43, s65, 73728\n"
      "v_add_u32 v12, s43, v28\n"
      "v_xor_b32 v13, 32, v12\n"
      "v_xor_b32 v14, 64, v12\n"
      "v_xor_b32 v15, 96, v12\n"
      "v_and_b32 v29, 2, v0\n"
      "v_cmp_eq_u32 vcc, 0, v29\n"
      "v_mov_b32 v30, 0x3f803f80\n"
      "s_nop 0\n"
      "v_cndmask_b32 v24, 0, v30, vcc\n"
      "v_cndmask_b32 v25, 0, v30, vcc\n"
      "v_cndmask_b32 v26, 0, v30, vcc\n"
      "v_cndmask_b32 v27, 0, v30, vcc\n"
      "v_and_b32 v29, 1, v0\n"
      "v_mul_u32_u24 v29, 43690, v29\n"
      "v_add_u32 v29, 17476, v29\n"
      "v_lshl_or_b32 v16, v29, 16, v29\n"
      "s_mov_b32 s40, 0\n"
      "s_mov_b32 s41, 24576\n"
      "s_mov_b32 s42, 49152\n"
      "s_mov_b32 s48, 0x3dd53b95\n"
      "s_mov_b32 s62, 0xbdd53b95\n"
      "s_mov_b32 s49, 0x40004000\n"
      "s_mov_b32 s51, 0\n"
      "s_mov_b32 s35, -1\n"
      "s_getpc_b64 s[58:59]\n"
      "L_pcref:\n"
      "s_add_u32 s58, s58, L_rare_common-L_pcref\n"
      "s_addc_u32 s59, s59, 0\n"
      "v_mov_b32 v28, 0\n"
      "v_mov_b32 v29, 0\n"
      "v_mov_b32 v30, 0\n"
      "v_mov_b32 v31, 0\n"
      "s_nop 1\n"
      "v_mfma_f32_32x32x16_bf16 v[64:79], v[28:31], v[28:31], 0\n"
      "v_mfma_f32_32x32x16_bf16 v[80:95], v[28:31], v[28:31], 0\n"
      "v_mfma_f32_32x32x16_bf16 v[96:111], v[28:31], v[28:31], 0\n"
      "v_mfma_f32_32x32x16_bf16 v[112:127], v[28:31], v[28:31], 0\n"
      "v_mfma_f32_32x32x16_bf16 v[128:143], v[28:31], v[28:31], 0\n"
      "v_mfma_f32_32x32x16_bf16 v[144:159], v[28:31], v[28:31], 0\n"
      "v_mfma_f32_32x32x16_bf16 a[96:111], v[28:31], v[28:31], 0\n"
      "v_mfma_f32_32x32x16_bf16 a[112:127], v[28:31], v[28:31], 0\n"
      "v_mfma_f32_32x32x16_bf16 a[128:143], v[28:31], v[28:31], 0\n"
      "v_mfma_f32_32x32x16_bf16 a[144:159], v[28:31], v[28:31], 0\n"
      "v_mfma_f32_32x32x16_bf16 a[160:175], v[28:31], v[28:31], 0\n"
      "v_mfma_f32_32x32x16_bf16 a[176:191], v[28:31], v[28:31], 0\n"
      "v_mfma_f32_32x32x16_bf16 a[192:207], v[28:31], v[28:31], 0\n"
      "s_waitcnt vmcnt(6)\n"
      "s_add_u32 m0, s45, 24576\n"
      "s_add_u32 s43, s53, 0\n"
      "buffer_load_dwordx4 v1, s[28:31], s43 offen offset:0 nt lds\n"
      "buffer_load_dwordx4 v2, s[28:31], s43 offen offset:1024 nt lds\n"
      "buffer_load_dwordx4 v3, s[28:31], s43 offen offset:2048 nt lds\n"
      "s_add_u32 m0, s45, 27648\n"
      "s_add_u32 s43, s53, 3072\n"
      "buffer_load_dwordx4 v1, s[28:31], s43 offen offset:0 nt lds\n"
      "buffer_load_dwordx4 v2, s[28:31], s43 offen offset:1024 nt lds\n"
      "buffer_load_dwordx4 v3, s[28:31], s43 offen offset:2048 nt lds\n"
      "s_barrier\n"
      "s_lshr_b32 s43, s61, 1\n"
      "s_and_b32 s43, s43, s64\n"
      "s_cbranch_scc1 L_pro_N\n"
      "s_and_b32 s43, s61, 1\n"
      "s_cbranch_scc1 L_pro_O\n"
      "L_pro_E:\n"
      "s_mov_b32 s50, 7\n"
      "ds_read_b128 a[208:211], v4 offset:0\n"
      "ds_read_b128 a[212:215], v5 offset:0\n"
      "ds_read_b128 a[216:219], v4 offset:128\n"
      "ds_read_b128 a[220:223], v5 offset:128\n"
      "ds_read_b128 a[224:227], v4 offset:256\n"
      "ds_read_b128 a[228:231], v5 offset:256\n"
      "s_add_u32 s43, s44, 49152\n"
      "s_sub_u32 s43, s43, s65\n"
      "v_add_u32 v232, s43, v6\n"
      "v_add_u32 v233, s43, v7\n"
      "s_waitcnt vmcnt(6)\n"
      "ds_read_b128 a[0:3], v232 offset:0\n"
      "ds_read_b128 a[4:7], v233 offset:0\n"
      "ds_read_b128 a[8:11], v232 offset:128\n"
      "ds_read_b128 a[12:15], v233 offset:128\n"
      "ds_read_b128 a[16:19], v232 offset:256\n"
      "ds_read_b128 a[20:23], v233 offset:256\n"
      "s_add_u32 m0, s45, 49152\n"
      "s_add_u32 s63, s54, 0\n"
      "buffer_load_dwordx4 v1, s[28:31], s63 offen offset:0 nt lds\n"
      "buffer_load_dwordx4 v2, s[28:31], s63 offen offset:1024 nt lds\n"
      "buffer_load_dwordx4 v3, s[28:31], s63 offen offset:2048 nt lds\n"
      "s_add_u32 m0, s45, 52224\n"
      "s_add_u32 s63, s54, 3072\n"
      "buffer_load_dwordx4 v1, s[28:31], s63 offen offset:0 nt lds\n"
      "buffer_load_dwordx4 v2, s[28:31], s63 offen offset:1024 nt lds\n"
      "buffer_load_dwordx4 v3, s[28:31], s63 offen offset:2048 nt lds\n"
      "s_waitcnt lgkmcnt(0)\n"
      "v_mfma_f32_16x16x32_bf16 v[36:39], a[208:211], a[0:3], 0\n"
      "ds_read_b128 a[232:235], v4 offset:6144\n"
      "ds_read_b128 a[236:239], v5 offset:6144\n"
      "v_mfma_f32_16x16x32_bf16 v[36:39], a[212:215], a[4:7], v[36:39]\n"
      "ds_read_b128 a[240:243], v4 offset:6272\n"
      "ds_read_b128 a[244:247], v5 offset:6272\n"
      "v_mfma_f32_16x16x32_bf16 v[36:39], a[216:219], a[8:11], v[36:39]\n"
      "ds_read_b128 a[248:251], v4 offset:6400\n"
      "ds_read_b128 a[252:255], v5 offset:6400\n"
      "v_mfma_f32_16x16x32_bf16 v[36:39], a[220:223], a[12:15], v[36:39]\n"
      "v_mfma_f32_16x16x32_bf16 v[36:39], a[224:227], a[16:19], v[36:39]\n"
      "v_mfma_f32_16x16x32_bf16 v[36:39], a[228:231], a[20:23], v[36:39]\n"
      "s_waitcnt lgkmcnt(4)\n"
      "v_mfma_f32_16x16x32_bf16 v[40:43], a[232:235], a[0:3], 0\n"
      "ds_read_b128 a[208:211], v4 offset:12288\n"
      "ds_read_b128 a[212:215], v5 offset:12288\n"
      "v_mfma_f32_16x16x32_bf16 v[40:43], a[236:239], a[4:7], v[40:43]\n"
      "s_waitcnt lgkmcnt(4)\n"
      "ds_read_b128 a[216:219], v4 offset:12416\n"
      "ds_read_b128 a[220:223], v5 offset:12416\n"
      "v_mfma_f32_16x16x32_bf16 v[40:43], a[240:243], a[8:11], v[40:43]\n"
      "ds_read_b128 a[224:227], v4 offset:12544\n"
      "ds_read_b128 a[228:231], v5 offset:12544\n"
      "v_mfma_f32_16x16x32_bf16 v[40:43], a[244:247], a[12:15], v[40:43]\n"
      "s_waitcnt lgkmcnt(6)\n"
      "v_max3_f32 v52, v36, v37, v38\n"
      "v_max_f32 v52, v52, v39\n"
      "v_mfma_f32_16x16x32_bf16 v[40:43], a[248:251], a[16:19], v[40:43]\n"
      "v_mfma_f32_16x16x32_bf16 v[40:43], a[252:255], a[20:23], v[40:43]\n"
      "s_waitcnt lgkmcnt(4)\n"
      "v_mfma_f32_16x16x32_bf16 v[44:47], a[208:211], a[0:3], 0\n"
      "ds_read_b128 a[232:235], v4 offset:18432\n"
      "ds_read_b128 a[236:239], v5 offset:18432\n"
      "v_mfma_f32_16x16x32_bf16 v[44:47], a[212:215], a[4:7], v[44:47]\n"
      "s_waitcnt lgkmcnt(4)\n"
      "ds_read_b128 a[240:243], v4 offset:18560\n"
      "ds_read_b128 a[244:247], v5 offset:18560\n"
      "v_mfma_f32_16x16x32_bf16 v[44:47], a[216:219], a[8:11], v[44:47]\n"
      "ds_read_b128 a[248:251], v4 offset:18688\n"
      "ds_read_b128 a[252:255], v5 offset:18688\n"
      "v_mfma_f32_16x16x32_bf16 v[44:47], a[220:223], a[12:15], v[44:47]\n"
      "s_waitcnt lgkmcnt(6)\n"
      "v_max3_f32 v52, v52, v40, v41\n"
      "v_max3_f32 v52, v52, v42, v43\n"
      "v_mfma_f32_16x16x32_bf16 v[44:47], a[224:227], a[16:19], v[44:47]\n"
      "v_mfma_f32_16x16x32_bf16 v[44:47], a[228:231], a[20:23], v[44:47]\n"
      "s_waitcnt lgkmcnt(4)\n"
      "v_mfma_f32_16x16x32_bf16 v[48:51], a[232:235], a[0:3], 0\n"
      "ds_read_b128 a[208:211], v4 offset:0\n"
      "ds_read_b128 a[212:215], v5 offset:0\n"
      "v_mfma_f32_16x16x32_bf16 v[48:51], a[236:239], a[4:7], v[48:51]\n"
      "s_waitcnt lgkmcnt(4)\n"
      "ds_read_b128 a[216:219], v4 offset:128\n"
      "ds_read_b128 a[220:223], v5 offset:128\n"
      "v_mfma_f32_16x16x32_bf16 v[48:51], a[240:243], a[8:11], v[48:51]\n"
      "ds_read_b128 a[224:227], v4 offset:256\n"
      "ds_read_b128 a[228:231], v5 offset:256\n"
      "v_mfma_f32_16x16x32_bf16 v[48:51], a[244:247], a[12:15], v[48:51]\n"
      "s_waitcnt lgkmcnt(6)\n"
      "v_max3_f32 v52, v52, v44, v45\n"
      "v_max3_f32 v52, v52, v46, v47\n"
      "v_mfma_f32_16x16x32_bf16 v[48:51], a[248:251], a[16:19], v[48:51]\n"
      "v_mfma_f32_16x16x32_bf16 v[48:51], a[252:255], a[20:23], v[48:51]\n"
      "s_nop 7\n"
      "v_max3_f32 v52, v52, v48, v49\n"
      "v_max3_f32 v52, v52, v50, v51\n"
      "v_mov_b32 v53, v52\n"
      "s_nop 1\n"
      "v_permlane32_swap_b32 v52, v53\n"
      "v_max_f32 v52, v52, v53\n"
      "v_mov_b32 v53, v52\n"
      "s_nop 1\n"
      "v_permlane16_swap_b32 v52, v53\n"
      "v_max_f32 v52, v52, v53\n"
      "v_mul_f32 v52, s62, v52\n"
      "v_add_f32 v18, 0xc1800000, v52\n"
      "v_fma_f32 v36, v36, s48, v18\n"
      "v_fma_f32 v37, v37, s48, v18\n"
      "v_fma_f32 v38, v38, s48, v18\n"
      "v_fma_f32 v39, v39, s48, v18\n"
      "v_exp_f32 v28, v36\n"
      "v_exp_f32 v29, v37\n"
      "v_exp_f32 v30, v38\n"
      "v_exp_f32 v31, v39\n"
      "v_cvt_pk_bf16_f32 v160, v28, v29\n"
      "v_cvt_pk_bf16_f32 v161, v30, v31\n"
      "v_fma_f32 v40, v40, s48, v18\n"
      "v_fma_f32 v41, v41, s48, v18\n"
      "v_fma_f32 v42, v42, s48, v18\n"
      "v_fma_f32 v43, v43, s48, v18\n"
      "v_exp_f32 v32, v40\n"
      "v_exp_f32 v33, v41\n"
      "v_exp_f32 v34, v42\n"
      "v_exp_f32 v35, v43\n"
      "v_cvt_pk_bf16_f32 v162, v32, v33\n"
      "v_cvt_pk_bf16_f32 v163, v34, v35\n"
      "v_fma_f32 v44, v44, s48, v18\n"
      "v_fma_f32 v45, v45, s48, v18\n"
      "v_fma_f32 v46, v46, s48, v18\n"
      "v_fma_f32 v47, v47, s48, v18\n"
      "v_exp_f32 v28, v44\n"
      "v_exp_f32 v29, v45\n"
      "v_exp_f32 v30, v46\n"
      "v_exp_f32 v31, v47\n"
      "v_cvt_pk_bf16_f32 v164, v28, v29\n"
      "v_cvt_pk_bf16_f32 v165, v30, v31\n"
      "v_fma_f32 v48, v48, s48, v18\n"
      "v_fma_f32 v49, v49, s48, v18\n"
      "v_fma_f32 v50, v50, s48, v18\n"
      "v_fma_f32 v51, v51, s48, v18\n"
      "v_exp_f32 v32, v48\n"
      "v_exp_f32 v33, v49\n"
      "v_exp_f32 v34, v50\n"
      "v_exp_f32 v35, v51\n"
      "v_cvt_pk_bf16_f32 v166, v32, v33\n"
      "v_cvt_pk_bf16_f32 v167, v34, v35\n"
      "s_add_u32 s43, s45, 24576\n"
      "s_sub_u32 s43, s43, s65\n"
      "v_add_u32 v232, s43, v6\n"
      "v_add_u32 v233, s43, v7\n"
      "s_waitcnt vmcnt(6)\n"
      "ds_read_b128 a[24:27], v232 offset:0\n"
      "ds_read_b128 a[28:31], v233 offset:0\n"
      "ds_read_b128 a[32:35], v232 offset:128\n"
      "ds_read_b128 a[36:39], v233 offset:128\n"
      "ds_read_b128 a[40:43], v232 offset:256\n"
      "ds_read_b128 a[44:47], v233 offset:256\n"
      "s_mov_b32 m0, s87\n"
      "s_add_u32 s63, s55, s88\n"
      "buffer_load_dwordx4 v1, s[28:31], s63 offen offset:0 nt lds\n"
      "buffer_load_dwordx4 v2, s[28:31], s63 offen offset:1024 nt lds\n"
      "buffer_load_dwordx4 v3, s[28:31], s63 offen offset:2048 nt lds\n"
      "s_add_u32 m0, s44, 24576\n"
      "s_add_u32 s63, s37, 0\n"
      "buffer_load_dwordx4 v1, s[20:23], s63 offen offset:0 lds\n"
      "buffer_load_dwordx4 v2, s[20:23], s63 offen offset:1024 lds\n"
      "buffer_load_dwordx4 v3, s[20:23], s63 offen offset:2048 lds\n"
      "s_waitcnt lgkmcnt(0)\n"
      "v_mfma_f32_16x16x32_bf16 v[36:39], a[208:211], a[24:27], 0\n"
      "s_add_u32 m0, s44, 27648\n"
      "ds_read_b128 a[232:235], v4 offset:6144\n"
      "ds_read_b128 a[236:239], v5 offset:6144\n"
      "v_mfma_f32_16x16x32_bf16 v[36:39], a[212:215], a[28:31], v[36:39]\n"
      "s_add_u32 s63, s37, 3072\n"
      "ds_read_b128 a[240:243], v4 offset:6272\n"
      "ds_read_b128 a[244:247], v5 offset:6272\n"
      "v_mfma_f32_16x16x32_bf16 v[36:39], a[216:219], a[32:35], v[36:39]\n"
      "ds_read_b128 a[248:251], v4 offset:6400\n"
      "ds_read_b128 a[252:255], v5 offset:6400\n"
      "v_mfma_f32_16x16x32_bf16 v[36:39], a[220:223], a[36:39], v[36:39]\n"
      "buffer_load_dwordx4 v1, s[20:23], s63 offen offset:0 lds\n"
      "v_mfma_f32_16x16x32_bf16 v[36:39], a[224:227], a[40:43], v[36:39]\n"
      "v_mfma_f32_16x16x32_bf16 v[36:39], a[228:231], a[44:47], v[36:39]\n"
      "s_waitcnt lgkmcnt(4)\n"
      "buffer_load_dwordx4 v2, s[20:23], s63 offen offset:1024 lds\n"
      "v_mfma_f32_16x16x32_bf16 v[40:43], a[232:235], a[24:27], 0\n"
      "ds_read_b128 a[208:211], v4 offset:12288\n"
      "ds_read_b128 a[212:215], v5 offset:12288\n"
      "v_mfma_f32_16x16x32_bf16 v[40:43], a[236:239], a[28:31], v[40:43]\n"
      "s_waitcnt lgkmcnt(4)\n"
      "buffer_load_dwordx4 v3, s[20:23], s63 offen offset:2048 lds\n"
      "v_mfma_f32_16x16x32_bf16 v[40:43], a[240:243], a[32:35], v[40:43]\n"
      "ds_read_b128 a[216:219], v4 offset:12416\n"
      "ds_read_b128 a[220:223], v5 offset:12416\n"
      "v_mfma_f32_16x16x32_bf16 v[40:43], a[244:247], a[36:39], v[40:43]\n"
      "s_waitcnt lgkmcnt(4)\n"
      "ds_read_b128 a[224:227], v4 offset:12544\n"
      "ds_read_b128 a[228:231], v5 offset:12544\n"
      "v_mfma_f32_16x16x32_bf16 v[40:43], a[248:251], a[40:43], v[40:43]\n"
      "v_max3_f32 v52, v36, v37, v38\n"
      "v_max_f32 v52, v52, v39\n"
      "v_mfma_f32_16x16x32_bf16 v[40:43], a[252:255], a[44:47], v[40:43]\n"
      "s_waitcnt lgkmcnt(4)\n"
      "v_mfma_f32_16x16x32_bf16 v[44:47], a[208:211], a[24:27], 0\n"
      "ds_read_b128 a[232:235], v4 offset:18432\n"
      "ds_read_b128 a[236:239], v5 offset:18432\n"
      "v_mfma_f32_16x16x32_bf16 v[44:47], a[212:215], a[28:31], v[44:47]\n"
      "s_waitcnt lgkmcnt(4)\n"
      "ds_read_b128 a[240:243], v4 offset:18560\n"
      "ds_read_b128 a[244:247], v5 offset:18560\n"
      "v_mfma_f32_16x16x32_bf16 v[44:47], a[216:219], a[32:35], v[44:47]\n"
      "ds_read_b128 a[248:251], v4 offset:18688\n"
      "ds_read_b128 a[252:255], v5 offset:18688\n"
      "v_mfma_f32_16x16x32_bf16 v[44:47], a[220:223], a[36:39], v[44:47]\n"
      "s_waitcnt lgkmcnt(6)\n"
      "v_max3_f32 v52, v52, v40, v41\n"
      "v_max3_f32 v52, v52, v42, v43\n"
      "v_mfma_f32_16x16x32_bf16 v[44:47], a[224:227], a[40:43], v[44:47]\n"
      "v_mfma_f32_16x16x32_bf16 v[44:47], a[228:231], a[44:47], v[44:47]\n"
      "s_waitcnt lgkmcnt(4)\n"
      "v_mfma_f32_16x16x32_bf16 v[48:51], a[232:235], a[24:27], 0\n"
      "ds_read_b128 a[208:211], v4 offset:0\n"
      "ds_read_b128 a[212:215], v5 offset:0\n"
      "v_mfma_f32_16x16x32_bf16 v[48:51], a[236:239], a[28:31], v[48:51]\n"
      "s_waitcnt lgkmcnt(4)\n"
      "ds_read_b128 a[216:219], v4 offset:128\n"
      "ds_read_b128 a[220:223], v5 offset:128\n"
      "v_mfma_f32_16x16x32_bf16 v[48:51], a[240:243], a[32:35], v[48:51]\n"
      "ds_read_b128 a[224:227], v4 offset:256\n"
      "ds_read_b128 a[228:231], v5 offset:256\n"
      "v_mfma_f32_16x16x32_bf16 v[48:51], a[244:247], a[36:39], v[48:51]\n"
      "s_waitcnt lgkmcnt(6)\n"
      "v_max3_f32 v52, v52, v44, v45\n"
      "v_max3_f32 v52, v52, v46, v47\n"
      "v_mfma_f32_16x16x32_bf16 v[48:51], a[248:251], a[40:43], v[48:51]\n"
      "v_mfma_f32_16x16x32_bf16 v[48:51], a[252:255], a[44:47], v[48:51]\n"
      "s_nop 7\n"
      "v_max3_f32 v52, v52, v48, v49\n"
      "v_max3_f32 v52, v52, v50, v51\n"
      "v_mov_b32 v53, v52\n"
      "s_nop 1\n"
      "v_permlane32_swap_b32 v52, v53\n"
      "v_max_f32 v52, v52, v53\n"
      "v_mov_b32 v53, v52\n"
      "s_nop 1\n"
      "v_permlane16_swap_b32 v52, v53\n"
      "v_max_f32 v52, v52, v53\n"
      "v_mul_f32 v52, s62, v52\n"
      "v_add_f32 v19, 0xc1800000, v52\n"
      "v_fma_f32 v36, v36, s48, v19\n"
      "v_fma_f32 v37, v37, s48, v19\n"
      "v_fma_f32 v38, v38, s48, v19\n"
      "v_fma_f32 v39, v39, s48, v19\n"
      "v_exp_f32 v28, v36\n"
      "v_exp_f32 v29, v37\n"
      "v_exp_f32 v30, v38\n"
      "v_exp_f32 v31, v39\n"
      "v_cvt_pk_bf16_f32 v168, v28, v29\n"
      "v_cvt_pk_bf16_f32 v169, v30, v31\n"
      "v_fma_f32 v40, v40, s48, v19\n"
      "v_fma_f32 v41, v41, s48, v19\n"
      "v_fma_f32 v42, v42, s48, v19\n"
      "v_fma_f32 v43, v43, s48, v19\n"
      "v_exp_f32 v32, v40\n"
      "v_exp_f32 v33, v41\n"
      "v_exp_f32 v34, v42\n"
      "v_exp_f32 v35, v43\n"
      "v_cvt_pk_bf16_f32 v170, v32, v33\n"
      "v_cvt_pk_bf16_f32 v171, v34, v35\n"
      "v_fma_f32 v44, v44, s48, v19\n"
      "v_fma_f32 v45, v45, s48, v19\n"
      "v_fma_f32 v46, v46, s48, v19\n"
      "v_fma_f32 v47, v47, s48, v19\n"
      "v_exp_f32 v28, v44\n"
      "v_exp_f32 v29, v45\n"
      "v_exp_f32 v30, v46\n"
      "v_exp_f32 v31, v47\n"
      "v_cvt_pk_bf16_f32 v172, v28, v29\n"
      "v_cvt_pk_bf16_f32 v173, v30, v31\n"
      "v_fma_f32 v48, v48, s48, v19\n"
      "v_fma_f32 v49, v49, s48, v19\n"
      "v_fma_f32 v50, v50, s48, v19\n"
      "v_fma_f32 v51, v51, s48, v19\n"
      "v_exp_f32 v32, v48\n"
      "v_exp_f32 v33, v49\n"
      "v_exp_f32 v34, v50\n"
      "v_exp_f32 v35, v51\n"
      "v_cvt_pk_bf16_f32 v174, v32, v33\n"
      "v_cvt_pk_bf16_f32 v175, v34, v35\n"
      "s_add_u32 s43, s45, 49152\n"
      "s_sub_u32 s43, s43, s65\n"
      "v_add_u32 v232, s43, v6\n"
      "v_add_u32 v233, s43, v7\n"
      "s_waitcnt vmcnt(9)\n"
      "ds_read_b128 a[48:51], v232 offset:0\n"
      "ds_read_b128 a[52:55], v233 offset:0\n"
      "ds_read_b128 a[56:59], v232 offset:128\n"
      "ds_read_b128 a[60:63], v233 offset:128\n"
      "ds_read_b128 a[64:67], v232 offset:256\n"
      "ds_read_b128 a[68:71], v233 offset:256\n"
      "s_add_u32 m0, s45, 0\n"
      "s_add_u32 s63, s36, 0\n"
      "buffer_load_dwordx4 v1, s[24:27], s63 offen offset:0 lds\n"
      "buffer_load_dwordx4 v2, s[24:27], s63 offen offset:1024 lds\n"
      "buffer_load_dwordx4 v3, s[24:27], s63 offen offset:2048 lds\n"
      "s_add_u32 m0, s45, 3072\n"
      "s_add_u32 s63, s36, 3072\n"
      "buffer_load_dwordx4 v1, s[24:27], s63 offen offset:0 lds\n"
      "buffer_load_dwordx4 v2, s[24:27], s63 offen offset:1024 lds\n"
      "buffer_load_dwordx4 v3, s[24:27], s63 offen offset:2048 lds\n"
      "s_waitcnt lgkmcnt(0)\n"
      "v_mfma_f32_16x16x32_bf16 v[36:39], a[208:211], a[48:51], 0\n"
      "ds_read_b128 a[232:235], v4 offset:6144\n"
      "ds_read_b128 a[236:239], v5 offset:6144\n"
      "v_mfma_f32_16x16x32_bf16 v[36:39], a[212:215], a[52:55], v[36:39]\n"
      "ds_read_b128 a[240:243], v4 offset:6272\n"
      "ds_read_b128 a[244:247], v5 offset:6272\n"
      "v_mfma_f32_16x16x32_bf16 v[36:39], a[216:219], a[56:59], v[36:39]\n"
      "ds_read_b128 a[248:251], v4 offset:6400\n"
      "ds_read_b128 a[252:255], v5 offset:6400\n"
      "v_mfma_f32_16x16x32_bf16 v[36:39], a[220:223], a[60:63], v[36:39]\n"
      "v_mfma_f32_16x16x32_bf16 v[36:39], a[224:227], a[64:67], v[36:39]\n"
      "v_mfma_f32_16x16x32_bf16 v[36:39], a[228:231], a[68:71], v[36:39]\n"
      "s_waitcnt lgkmcnt(4)\n"
      "v_mfma_f32_16x16x32_bf16 v[40:43], a[232:235], a[48:51], 0\n"
      "ds_read_b128 a[208:211], v4 offset:12288\n"
      "ds_read_b128 a[212:215], v5 offset:12288\n"
      "v_mfma_f32_16x16x32_bf16 v[40:43], a[236:239], a[52:55], v[40:43]\n"
      "s_waitcnt lgkmcnt(4)\n"
      "ds_read_b128 a[216:219], v4 offset:12416\n"
      "ds_read_b128 a[220:223], v5 offset:12416\n"
      "v_mfma_f32_16x16x32_bf16 v[40:43], a[240:243], a[56:59], v[40:43]\n"
      "ds_read_b128 a[224:227], v4 offset:12544\n"
      "ds_read_b128 a[228:231], v5 offset:12544\n"
      "v_mfma_f32_16x16x32_bf16 v[40:43], a[244:247], a[60:63], v[40:43]\n"
      "s_waitcnt lgkmcnt(6)\n"
      "v_max3_f32 v52, v36, v37, v38\n"
      "v_max_f32 v52, v52, v39\n"
      "v_mfma_f32_16x16x32_bf16 v[40:43], a[248:251], a[64:67], v[40:43]\n"
      "v_mfma_f32_16x16x32_bf16 v[40:43], a[252:255], a[68:71], v[40:43]\n"
      "s_waitcnt lgkmcnt(4)\n"
      "v_mfma_f32_16x16x32_bf16 v[44:47], a[208:211], a[48:51], 0\n"
      "ds_read_b128 a[232:235], v4 offset:18432\n"
      "ds_read_b128 a[236:239], v5 offset:18432\n"
      "v_mfma_f32_16x16x32_bf16 v[44:47], a[212:215], a[52:55], v[44:47]\n"
      "s_waitcnt lgkmcnt(4)\n"
      "ds_read_b128 a[240:243], v4 offset:18560\n"
      "ds_read_b128 a[244:247], v5 offset:18560\n"
      "v_mfma_f32_16x16x32_bf16 v[44:47], a[216:219], a[56:59], v[44:47]\n"
      "ds_read_b128 a[248:251], v4 offset:18688\n"
      "ds_read_b128 a[252:255], v5 offset:18688\n"
      "v_mfma_f32_16x16x32_bf16 v[44:47], a[220:223], a[60:63], v[44:47]\n"
      "s_waitcnt lgkmcnt(6)\n"
      "v_max3_f32 v52, v52, v40, v41\n"
      "v_max3_f32 v52, v52, v42, v43\n"
      "v_mfma_f32_16x16x32_bf16 v[44:47], a[224:227], a[64:67], v[44:47]\n"
      "v_mfma_f32_16x16x32_bf16 v[44:47], a[228:231], a[68:71], v[44:47]\n"
      "s_waitcnt lgkmcnt(4)\n"
      "v_mfma_f32_16x16x32_bf16 v[48:51], a[232:235], a[48:51], 0\n"
      "ds_read_b128 a[208:211], v4 offset:0\n"
      "ds_read_b128 a[212:215], v5 offset:0\n"
      "v_mfma_f32_16x16x32_bf16 v[48:51], a[236:239], a[52:55], v[48:51]\n"
      "s_waitcnt lgkmcnt(4)\n"
      "ds_read_b128 a[216:219], v4 offset:128\n"
      "ds_read_b128 a[220:223], v5 offset:128\n"
      "v_mfma_f32_16x16x32_bf16 v[48:51], a[240:243], a[56:59], v[48:51]\n"
      "ds_read_b128 a[224:227], v4 offset:256\n"
      "ds_read_b128 a[228:231], v5 offset:256\n"
      "v_mfma_f32_16x16x32_bf16 v[48:51], a[244:247], a[60:63], v[48:51]\n"
      "s_waitcnt lgkmcnt(6)\n"
      "v_max3_f32 v52, v52, v44, v45\n"
      "v_max3_f32 v52, v52, v46, v47\n"
      "v_mfma_f32_16x16x32_bf16 v[48:51], a[248:251], a[64:67], v[48:51]\n"
      "v_mfma_f32_16x16x32_bf16 v[48:51], a[252:255], a[68:71], v[48:51]\n"
      "s_nop 7\n"
      "v_max3_f32 v52, v52, v48, v49\n"
      "v_max3_f32 v52, v52, v50, v51\n"
      "v_mov_b32 v53, v52\n"
      "s_nop 1\n"
      "v_permlane32_swap_b32 v52, v53\n"
      "v_max_f32 v52, v52, v53\n"
      "v_mov_b32 v53, v52\n"
      "s_nop 1\n"
      "v_permlane16_swap_b32 v52, v53\n"
      "v_max_f32 v52, v52, v53\n"
      "v_mul_f32 v52, s62, v52\n"
      "v_add_f32 v20, 0xc1800000, v52\n"
      "v_fma_f32 v36, v36, s48, v20\n"
      "v_fma_f32 v37, v37, s48, v20\n"
      "v_fma_f32 v38, v38, s48, v20\n"
      "v_fma_f32 v39, v39, s48, v20\n"
      "v_exp_f32 v28, v36\n"
      "v_exp_f32 v29, v37\n"
      "v_exp_f32 v30, v38\n"
      "v_exp_f32 v31, v39\n"
      "v_cvt_pk_bf16_f32 v176, v28, v29\n"
      "v_cvt_pk_bf16_f32 v177, v30, v31\n"
      "v_fma_f32 v40, v40, s48, v20\n"
      "v_fma_f32 v41, v41, s48, v20\n"
      "v_fma_f32 v42, v42, s48, v20\n"
      "v_fma_f32 v43, v43, s48, v20\n"
      "v_exp_f32 v32, v40\n"
      "v_exp_f32 v33, v41\n"
      "v_exp_f32 v34, v42\n"
      "v_exp_f32 v35, v43\n"
      "v_cvt_pk_bf16_f32 v178, v32, v33\n"
      "v_cvt_pk_bf16_f32 v179, v34, v35\n"
      "v_fma_f32 v44, v44, s48, v20\n"
      "v_fma_f32 v45, v45, s48, v20\n"
      "v_fma_f32 v46, v46, s48, v20\n"
      "v_fma_f32 v47, v47, s48, v20\n"
      "v_exp_f32 v28, v44\n"
      "v_exp_f32 v29, v45\n"
      "v_exp_f32 v30, v46\n"
      "v_exp_f32 v31, v47\n"
      "v_cvt_pk_bf16_f32 v180, v28, v29\n"
      "v_cvt_pk_bf16_f32 v181, v30, v31\n"
      "v_fma_f32 v48, v48, s48, v20\n"
      "v_fma_f32 v49, v49, s48, v20\n"
      "v_fma_f32 v50, v50, s48, v20\n"
      "v_fma_f32 v51, v51, s48, v20\n"
      "v_exp_f32 v32, v48\n"
      "v_exp_f32 v33, v49\n"
      "v_exp_f32 v34, v50\n"
      "v_exp_f32 v35, v51\n"
      "v_cvt_pk_bf16_f32 v182, v32, v33\n"
      "v_cvt_pk_bf16_f32 v183, v34, v35\n"
      "v_add_u32 v232, s89, v6\n"
      "v_add_u32 v233, s89, v7\n"
      "s_waitcnt vmcnt(12)\n"
      "s_barrier\n"
      "ds_read_b128 a[72:75], v232 offset:0\n"
      "ds_read_b128 a[76:79], v233 offset:0\n"
      "ds_read_b128 a[80:83], v232 offset:128\n"
      "ds_read_b128 a[84:87], v233 offset:128\n"
      "ds_read_b128 a[88:91], v232 offset:256\n"
      "ds_read_b128 a[92:95], v233 offset:256\n"
      "s_waitcnt lgkmcnt(0)\n"
      "v_mfma_f32_16x16x32_bf16 v[36:39], a[208:211], a[72:75], 0\n"
      "ds_read_b128 a[232:235], v4 offset:6144\n"
      "ds_read_b128 a[236:239], v5 offset:6144\n"
      "v_mfma_f32_16x16x32_bf16 v[36:39], a[212:215], a[76:79], v[36:39]\n"
      "ds_read_b128 a[240:243], v4 offset:6272\n"
      "ds_read_b128 a[244:247], v5 offset:6272\n"
      "v_mfma_f32_16x16x32_bf16 v[36:39], a[216:219], a[80:83], v[36:39]\n"
      "ds_read_b128 a[248:251], v4 offset:6400\n"
      "ds_read_b128 a[252:255], v5 offset:6400\n"
      "v_mfma_f32_16x16x32_bf16 v[36:39], a[220:223], a[84:87], v[36:39]\n"
      "v_mfma_f32_16x16x32_bf16 v[36:39], a[224:227], a[88:91], v[36:39]\n"
      "v_mfma_f32_16x16x32_bf16 v[36:39], a[228:231], a[92:95], v[36:39]\n"
      "s_waitcnt lgkmcnt(4)\n"
      "v_mfma_f32_16x16x32_bf16 v[40:43], a[232:235], a[72:75], 0\n"
      "s_nop 2\n"
      "v_mfma_f32_16x16x32_bf16 v[40:43], a[236:239], a[76:79], v[40:43]\n"
      "s_waitcnt lgkmcnt(2)\n"
      "v_mfma_f32_16x16x32_bf16 v[40:43], a[240:243], a[80:83], v[40:43]\n"
      "v_max3_f32 v52, v36, v37, v38\n"
      "v_max_f32 v52, v52, v39\n"
      "v_mfma_f32_16x16x32_bf16 v[40:43], a[244:247], a[84:87], v[40:43]\n"
      "s_waitcnt lgkmcnt(0)\n"
      "v_mfma_f32_16x16x32_bf16 v[40:43], a[248:251], a[88:91], v[40:43]\n"
      "v_mfma_f32_16x16x32_bf16 v[40:43], a[252:255], a[92:95], v[40:43]\n"
      "s_nop 7\n"
      "v_max3_f32 v52, v52, v40, v41\n"
      "v_max3_f32 v52, v52, v42, v43\n"
      "v_mov_b32 v53, v52\n"
      "s_nop 1\n"
      "v_permlane32_swap_b32 v52, v53\n"
      "v_max_f32 v52, v52, v53\n"
      "v_mov_b32 v53, v52\n"
      "s_nop 1\n"
      "v_permlane16_swap_b32 v52, v53\n"
      "v_max_f32 v52, v52, v53\n"
      "v_mul_f32 v52, s62, v52\n"
      "v_add_f32 v21, 0xc1800000, v52\n"
      "v_fma_f32 v36, v36, s48, v21\n"
      "v_fma_f32 v37, v37, s48, v21\n"
      "v_fma_f32 v38, v38, s48, v21\n"
      "v_fma_f32 v39, v39, s48, v21\n"
      "v_exp_f32 v28, v36\n"
      "v_exp_f32 v29, v37\n"
      "v_exp_f32 v30, v38\n"
      "v_exp_f32 v31, v39\n"
      "v_cvt_pk_bf16_f32 v184, v28, v29\n"
      "v_cvt_pk_bf16_f32 v185, v30, v31\n"
      "v_fma_f32 v40, v40, s48, v21\n"
      "v_fma_f32 v41, v41, s48, v21\n"
      "v_fma_f32 v42, v42, s48, v21\n"
      "v_fma_f32 v43, v43, s48, v21\n"
      "v_exp_f32 v32, v40\n"
      "v_exp_f32 v33, v41\n"
      "v_exp_f32 v34, v42\n"
      "v_exp_f32 v35, v43\n"
      "v_cvt_pk_bf16_f32 v186, v32, v33\n"
      "v_cvt_pk_bf16_f32 v187, v34, v35\n"
      "s_add_u32 m0, s44, 49152\n"
      "s_add_u32 s63, s38, 0\n"
      "buffer_load_dwordx4 v1, s[20:23], s63 offen offset:0 lds\n"
      "buffer_load_dwordx4 v2, s[20:23], s63 offen offset:1024 lds\n"
      "buffer_load_dwordx4 v3, s[20:23], s63 offen offset:2048 lds\n"
      "s_add_u32 m0, s44, 52224\n"
      "s_add_u32 s63, s38, 3072\n"
      "buffer_load_dwordx4 v1, s[20:23], s63 offen offset:0 lds\n"
      "buffer_load_dwordx4 v2, s[20:23], s63 offen offset:1024 lds\n"
      "buffer_load_dwordx4 v3, s[20:23], s63 offen offset:2048 lds\n"
      "s_add_u32 m0, s45, 24576\n"
      "s_add_u32 s63, s37, 0\n"
      "buffer_load_dwordx4 v1, s[24:27], s63 offen offset:0 lds\n"
      "buffer_load_dwordx4 v2, s[24:27], s63 offen offset:1024 lds\n"
      "buffer_load_dwordx4 v3, s[24:27], s63 offen offset:2048 lds\n"
      "s_add_u32 m0, s45, 27648\n"
      "s_add_u32 s63, s37, 3072\n"
      "buffer_load_dwordx4 v1, s[24:27], s63 offen offset:0 lds\n"
      "buffer_load_dwordx4 v2, s[24:27], s63 offen offset:1024 lds\n"
      "buffer_load_dwordx4 v3, s[24:27], s63 offen offset:2048 lds\n"
      "v_add_u32 v232, 24576, v6\n"
      "v_add_u32 v233, 24576, v7\n"
      "v_mov_b32 v234, v12\n"
      "v_mov_b32 v235, v13\n"
      "v_mov_b32 v236, v14\n"
      "v_mov_b32 v237, v15\n"
      "s_add_u32 s39, s38, 3072\n"
      "s_mov_b32 s12, 3\n"
      "s_add_u32 s15, s13, 3\n"
      "s_and_b32 s15, s15, 15\n"
      "s_mul_i32 s15, s15, 24576\n"
      "s_add_u32 s36, s15, s14\n"
      "s_add_u32 s37, s36, 3072\n"
      "s_mov_b32 s40, 24576\n"
      "s_mov_b32 s41, 49152\n"
      "s_mov_b32 s42, 0\n"
      "s_waitcnt vmcnt(12)\n"
      "s_barrier\n"
      "ds_read_b128 a[208:211], v232 offset:0\n"
      "ds_read_b128 a[212:215], v233 offset:0\n"
      "ds_read_b128 a[216:219], v232 offset:128\n"
      "ds_read_b128 a[220:223], v233 offset:128\n"
      "ds_read_b128 a[224:227], v232 offset:256\n"
      "ds_read_b128 a[228:231], v233 offset:256\n"
      "s_waitcnt lgkmcnt(0)\n"
      "s_branch L_loop_E\n"
      ".p2align 6\n"
      "L_loop_E:\n"
      "v_mfma_f32_16x16x32_bf16 v[36:39], a[208:211], a[0:3], 0\n"
      "s_add_u32 s46, s44, s42\n"
      "ds_read_b64_tr_b16 a[232:233], v234 offset:0\n"
      "v_mfma_f32_16x16x32_bf16 v[36:39], a[212:215], a[4:7], v[36:39]\n"
      "s_add_u32 s47, s45, s41\n"
      "ds_read_b64_tr_b16 a[234:235], v234 offset:6144\n"
      "v_mfma_f32_16x16x32_bf16 v[36:39], a[216:219], a[8:11], v[36:39]\n"
      "ds_read_b64_tr_b16 a[236:237], v234 offset:12288\n"
      "v_mfma_f32_16x16x32_bf16 v[36:39], a[220:223], a[12:15], v[36:39]\n"
      "s_mov_b32 m0, s46\n"
      "ds_read_b64_tr_b16 a[238:239], v234 offset:18432\n"
      "v_mfma_f32_16x16x32_bf16 v[36:39], a[224:227], a[16:19], v[36:39]\n"
      "buffer_load_dwordx4 v1, s[20:23], s36 offen offset:0 lds\n"
      "v_mfma_f32_16x16x32_bf16 v[36:39], a[228:231], a[20:23], v[36:39]\n"
      "buffer_load_dwordx4 v2, s[20:23], s36 offen offset:1024 lds\n"
      "v_mfma_f32_16x16x32_bf16 v[40:43], a[228:231], a[44:47], 0\n"
      "buffer_load_dwordx4 v3, s[20:23], s36 offen offset:2048 lds\n"
      "v_mfma_f32_16x16x32_bf16 v[40:43], a[224:227], a[40:43], v[40:43]\n"
      "s_nop 0\n"
      "ds_read_b64_tr_b16 a[240:241], v235 offset:0\n"
      "v_mfma_f32_16x16x32_bf16 v[40:43], a[220:223], a[36:39], v[40:43]\n"
      "ds_read_b64_tr_b16 a[242:243], v235 offset:6144\n"
      "v_fma_f32 v36, v36, s48, v18\n"
      "v_mfma_f32_16x16x32_bf16 v[40:43], a[216:219], a[32:35], v[40:43]\n"
      "ds_read_b64_tr_b16 a[244:245], v235 offset:12288\n"
      "v_fma_f32 v37, v37, s48, v18\n"
      "v_mfma_f32_16x16x32_bf16 v[40:43], a[212:215], a[28:31], v[40:43]\n"
      "ds_read_b64_tr_b16 a[246:247], v235 offset:18432\n"
      "v_fma_f32 v38, v38, s48, v18\n"
      "v_mfma_f32_16x16x32_bf16 v[40:43], a[208:211], a[24:27], v[40:43]\n"
      "ds_read_b64_tr_b16 a[248:249], v236 offset:0\n"
      "v_fma_f32 v39, v39, s48, v18\n"
      "v_mfma_f32_16x16x32_bf16 v[44:47], a[208:211], a[48:51], 0\n"
      "v_exp_f32 v28, v36\n"
      "v_mfma_f32_16x16x32_bf16 v[44:47], a[212:215], a[52:55], v[44:47]\n"
      "v_exp_f32 v29, v37\n"
      "v_mfma_f32_16x16x32_bf16 v[44:47], a[216:219], a[56:59], v[44:47]\n"
      "ds_read_b64_tr_b16 a[250:251], v236 offset:6144\n"
      "v_cvt_pk_bf16_f32 v188, v28, v29\n"
      "v_mfma_f32_16x16x32_bf16 v[44:47], a[220:223], a[60:63], v[44:47]\n"
      "ds_read_b64_tr_b16 a[252:253], v236 offset:12288\n"
      "v_fma_f32 v40, v40, s48, v19\n"
      "v_mfma_f32_16x16x32_bf16 v[44:47], a[224:227], a[64:67], v[44:47]\n"
      "ds_read_b64_tr_b16 a[254:255], v236 offset:18432\n"
      "v_fma_f32 v41, v41, s48, v19\n"
      "v_mfma_f32_16x16x32_bf16 v[44:47], a[228:231], a[68:71], v[44:47]\n"
      "v_exp_f32 v30, v38\n"
      "v_mfma_f32_16x16x32_bf16 v[48:51], a[228:231], a[92:95], 0\n"
      "v_exp_f32 v31, v39\n"
      "v_mfma_f32_16x16x32_bf16 v[48:51], a[224:227], a[88:91], v[48:51]\n"
      "v_exp_f32 v32, v40\n"
      "v_mfma_f32_16x16x32_bf16 v[48:51], a[220:223], a[84:87], v[48:51]\n"
      "v_exp_f32 v33, v41\n"
      "v_mfma_f32_16x16x32_bf16 v[48:51], a[216:219], a[80:83], v[48:51]\n"
      "v_cvt_pk_bf16_f32 v189, v30, v31\n"
      "v_fma_f32 v42, v42, s48, v19\n"
      "v_mfma_f32_16x16x32_bf16 v[48:51], a[212:215], a[76:79], v[48:51]\n"
      "v_exp_f32 v34, v42\n"
      "v_mfma_f32_16x16x32_bf16 v[48:51], a[208:211], a[72:75], v[48:51]\n"
      "s_waitcnt lgkmcnt(0)\n"
      "v_fma_f32 v43, v43, s48, v19\n"
      "v_cvt_pk_bf16_f32 v196, v32, v33\n"
      "v_smfmac_f32_16x16x64_bf16 a[192:195], v[24:27], v[160:167], v16\n"
      "ds_read_b128 a[208:211], v232 offset:6144\n"
      "v_or3_b32 v22, v188, v189, v196\n"
      "v_mfma_f32_16x16x32_bf16 v[64:67], v[164:167], a[236:239], v[64:67]\n"
      "v_exp_f32 v35, v43\n"
      "v_mfma_f32_16x16x32_bf16 v[64:67], v[160:163], a[232:235], v[64:67]\n"
      "ds_read_b128 a[212:215], v233 offset:6144\n"
      "v_cvt_pk_bf16_f32 v197, v34, v35\n"
      "v_mfma_f32_16x16x32_bf16 v[112:115], v[168:171], a[232:235], v[112:115]\n"
      "v_fma_f32 v44, v44, s48, v20\n"
      "v_fma_f32 v45, v45, s48, v20\n"
      "v_mfma_f32_16x16x32_bf16 v[112:115], v[172:175], a[236:239], v[112:115]\n"
      "ds_read_b128 a[216:219], v232 offset:6272\n"
      "v_fma_f32 v46, v46, s48, v20\n"
      "v_mfma_f32_16x16x32_bf16 a[96:99], v[180:183], a[236:239], a[96:99]\n"
      "v_exp_f32 v28, v44\n"
      "v_mfma_f32_16x16x32_bf16 a[96:99], v[176:179], a[232:235], a[96:99]\n"
      "ds_read_b128 a[220:223], v233 offset:6272\n"
      "v_fma_f32 v47, v47, s48, v20\n"
      "v_mfma_f32_16x16x32_bf16 a[144:147], v[184:187], a[232:235], a[144:147]\n"
      "v_exp_f32 v29, v45\n"
      "v_mfma_f32_16x16x32_bf16 v[68:71], v[164:167], a[244:247], v[68:71]\n"
      "ds_read_b128 a[224:227], v232 offset:6400\n"
      "v_cvt_pk_bf16_f32 v204, v28, v29\n"
      "v_mfma_f32_16x16x32_bf16 v[68:71], v[160:163], a[240:243], v[68:71]\n"
      "ds_read_b64_tr_b16 a[232:233], v237 offset:0\n"
      "v_or3_b32 v22, v22, v197, v204\n"
      "v_mfma_f32_16x16x32_bf16 v[116:119], v[168:171], a[240:243], v[116:119]\n"
      "ds_read_b128 a[228:231], v233 offset:6400\n"
      "v_fma_f32 v48, v48, s48, v21\n"
      "v_mfma_f32_16x16x32_bf16 v[116:119], v[172:175], a[244:247], v[116:119]\n"
      "ds_read_b64_tr_b16 a[234:235], v237 offset:6144\n"
      "v_fma_f32 v49, v49, s48, v21\n"
      "v_mfma_f32_16x16x32_bf16 a[100:103], v[180:183], a[244:247], a[100:103]\n"
      "ds_read_b64_tr_b16 a[236:237], v237 offset:12288\n"
      "v_fma_f32 v50, v50, s48, v21\n"
      "v_mfma_f32_16x16x32_bf16 a[100:103], v[176:179], a[240:243], a[100:103]\n"
      "ds_read_b64_tr_b16 a[238:239], v237 offset:18432\n"
      "v_fma_f32 v51, v51, s48, v21\n"
      "v_mfma_f32_16x16x32_bf16 a[148:151], v[184:187], a[240:243], a[148:151]\n"
      "v_exp_f32 v30, v46\n"
      "v_mfma_f32_16x16x32_bf16 v[72:75], v[164:167], a[252:255], v[72:75]\n"
      "v_exp_f32 v31, v47\n"
      "v_mfma_f32_16x16x32_bf16 v[72:75], v[160:163], a[248:251], v[72:75]\n"
      "ds_read_b64_tr_b16 a[240:241], v234 offset:128\n"
      "v_cvt_pk_bf16_f32 v205, v30, v31\n"
      "v_mfma_f32_16x16x32_bf16 v[120:123], v[168:171], a[248:251], v[120:123]\n"
      "v_exp_f32 v32, v48\n"
      "v_mfma_f32_16x16x32_bf16 v[120:123], v[172:175], a[252:255], v[120:123]\n"
      "v_exp_f32 v33, v49\n"
      "v_mfma_f32_16x16x32_bf16 a[104:107], v[180:183], a[252:255], a[104:107]\n"
      "ds_read_b64_tr_b16 a[242:243], v234 offset:6272\n"
      "v_cvt_pk_bf16_f32 v212, v32, v33\n"
      "v_mfma_f32_16x16x32_bf16 a[104:107], v[176:179], a[248:251], a[104:107]\n"
      "ds_read_b64_tr_b16 a[244:245], v234 offset:12416\n"
      "v_or3_b32 v22, v22, v205, v212\n"
      "v_mfma_f32_16x16x32_bf16 a[152:155], v[184:187], a[248:251], a[152:155]\n"
      "s_waitcnt lgkmcnt(6)\n"
      "v_exp_f32 v34, v50\n"
      "v_mfma_f32_16x16x32_bf16 v[216:219], a[208:211], a[0:3], 0\n"
      "v_exp_f32 v35, v51\n"
      "v_mfma_f32_16x16x32_bf16 v[216:219], a[212:215], a[4:7], v[216:219]\n"
      "ds_read_b64_tr_b16 a[246:247], v234 offset:18560\n"
      "v_cvt_pk_bf16_f32 v213, v34, v35\n"
      "v_mfma_f32_16x16x32_bf16 v[216:219], a[216:219], a[8:11], v[216:219]\n"
      "ds_read_b64_tr_b16 a[248:249], v235 offset:128\n"
      "v_mfma_f32_16x16x32_bf16 v[216:219], a[220:223], a[12:15], v[216:219]\n"
      "s_add_u32 m0, s46, 3072\n"
      "ds_read_b64_tr_b16 a[250:251], v235 offset:6272\n"
      "v_mfma_f32_16x16x32_bf16 v[216:219], a[224:227], a[16:19], v[216:219]\n"
      "buffer_load_dwordx4 v1, s[20:23], s37 offen offset:0 lds\n"
      "v_mfma_f32_16x16x32_bf16 v[216:219], a[228:231], a[20:23], v[216:219]\n"
      "buffer_load_dwordx4 v2, s[20:23], s37 offen offset:1024 lds\n"
      "v_mfma_f32_16x16x32_bf16 v[220:223], a[228:231], a[44:47], 0\n"
      "buffer_load_dwordx4 v3, s[20:23], s37 offen offset:2048 lds\n"
      "v_mfma_f32_16x16x32_bf16 v[220:223], a[224:227], a[40:43], v[220:223]\n"
      "s_nop 0\n"
      "ds_read_b64_tr_b16 a[252:253], v235 offset:12416\n"
      "v_mfma_f32_16x16x32_bf16 v[220:223], a[220:223], a[36:39], v[220:223]\n"
      "ds_read_b64_tr_b16 a[254:255], v235 offset:18560\n"
      "v_fma_f32 v216, v216, s48, v18\n"
      "v_mfma_f32_16x16x32_bf16 v[220:223], a[216:219], a[32:35], v[220:223]\n"
      "v_exp_f32 v28, v216\n"
      "v_mfma_f32_16x16x32_bf16 v[220:223], a[212:215], a[28:31], v[220:223]\n"
      "v_fma_f32 v217, v217, s48, v18\n"
      "v_fma_f32 v218, v218, s48, v18\n"
      "v_mfma_f32_16x16x32_bf16 v[220:223], a[208:211], a[24:27], v[220:223]\n"
      "v_exp_f32 v29, v217\n"
      "v_mfma_f32_16x16x32_bf16 v[224:227], a[208:211], a[48:51], 0\n"
      "v_exp_f32 v30, v218\n"
      "v_mfma_f32_16x16x32_bf16 v[224:227], a[212:215], a[52:55], v[224:227]\n"
      "v_fma_f32 v219, v219, s48, v18\n"
      "v_cvt_pk_bf16_f32 v190, v28, v29\n"
      "v_mfma_f32_16x16x32_bf16 v[224:227], a[216:219], a[56:59], v[224:227]\n"
      "v_exp_f32 v31, v219\n"
      "v_mfma_f32_16x16x32_bf16 v[224:227], a[220:223], a[60:63], v[224:227]\n"
      "v_or3_b32 v22, v22, v213, v190\n"
      "v_cvt_pk_bf16_f32 v191, v30, v31\n"
      "v_mfma_f32_16x16x32_bf16 v[224:227], a[224:227], a[64:67], v[224:227]\n"
      "v_fma_f32 v220, v220, s48, v19\n"
      "v_fma_f32 v221, v221, s48, v19\n"
      "v_mfma_f32_16x16x32_bf16 v[224:227], a[228:231], a[68:71], v[224:227]\n"
      "v_exp_f32 v32, v220\n"
      "v_mfma_f32_16x16x32_bf16 v[228:231], a[228:231], a[92:95], 0\n"
      "v_exp_f32 v33, v221\n"
      "v_mfma_f32_16x16x32_bf16 v[228:231], a[224:227], a[88:91], v[228:231]\n"
      "v_fma_f32 v222, v222, s48, v19\n"
      "v_fma_f32 v223, v223, s48, v19\n"
      "v_mfma_f32_16x16x32_bf16 v[228:231], a[220:223], a[84:87], v[228:231]\n"
      "v_exp_f32 v34, v222\n"
      "v_mfma_f32_16x16x32_bf16 v[228:231], a[216:219], a[80:83], v[228:231]\n"
      "v_exp_f32 v35, v223\n"
      "v_mfma_f32_16x16x32_bf16 v[228:231], a[212:215], a[76:79], v[228:231]\n"
      "v_cvt_pk_bf16_f32 v198, v32, v33\n"
      "v_or3_b32 v22, v22, v191, v198\n"
      "v_mfma_f32_16x16x32_bf16 v[228:231], a[208:211], a[72:75], v[228:231]\n"
      "s_waitcnt lgkmcnt(0)\n"
      "v_cvt_pk_bf16_f32 v199, v34, v35\n"
      "v_fma_f32 v224, v224, s48, v20\n"
      "v_smfmac_f32_16x16x64_bf16 a[196:199], v[24:27], v[168:175], v16\n"
      "ds_read_b128 a[208:211], v232 offset:12288\n"
      "v_fma_f32 v225, v225, s48, v20\n"
      "v_mfma_f32_16x16x32_bf16 v[76:79], v[164:167], a[236:239], v[76:79]\n"
      "v_exp_f32 v28, v224\n"
      "v_mfma_f32_16x16x32_bf16 v[76:79], v[160:163], a[232:235], v[76:79]\n"
      "ds_read_b128 a[212:215], v233 offset:12288\n"
      "v_fma_f32 v226, v226, s48, v20\n"
      "v_mfma_f32_16x16x32_bf16 v[124:127], v[168:171], a[232:235], v[124:127]\n"
      "v_exp_f32 v29, v225\n"
      "v_mfma_f32_16x16x32_bf16 v[124:127], v[172:175], a[236:239], v[124:127]\n"
      "ds_read_b128 a[216:219], v232 offset:12416\n"
      "v_fma_f32 v227, v227, s48, v20\n"
      "v_mfma_f32_16x16x32_bf16 a[108:111], v[180:183], a[236:239], a[108:111]\n"
      "v_exp_f32 v30, v226\n"
      "v_mfma_f32_16x16x32_bf16 a[108:111], v[176:179], a[232:235], a[108:111]\n"
      "ds_read_b128 a[220:223], v233 offset:12416\n"
      "v_cvt_pk_bf16_f32 v206, v28, v29\n"
      "v_mfma_f32_16x16x32_bf16 a[156:159], v[184:187], a[232:235], a[156:159]\n"
      "v_exp_f32 v31, v227\n"
      "v_mfma_f32_16x16x32_bf16 v[80:83], v[164:167], a[244:247], v[80:83]\n"
      "ds_read_b128 a[224:227], v232 offset:12544\n"
      "v_or3_b32 v22, v22, v199, v206\n"
      "v_mfma_f32_16x16x32_bf16 v[80:83], v[160:163], a[240:243], v[80:83]\n"
      "v_cvt_pk_bf16_f32 v207, v30, v31\n"
      "v_fma_f32 v228, v228, s48, v21\n"
      "v_mfma_f32_16x16x32_bf16 v[128:131], v[168:171], a[240:243], v[128:131]\n"
      "ds_read_b128 a[228:231], v233 offset:12544\n"
      "v_fma_f32 v229, v229, s48, v21\n"
      "v_mfma_f32_16x16x32_bf16 v[128:131], v[172:175], a[244:247], v[128:131]\n"
      "v_exp_f32 v32, v228\n"
      "v_mfma_f32_16x16x32_bf16 a[112:115], v[180:183], a[244:247], a[112:115]\n"
      "v_exp_f32 v33, v229\n"
      "v_mfma_f32_16x16x32_bf16 a[112:115], v[176:179], a[240:243], a[112:115]\n"
      "v_fma_f32 v230, v230, s48, v21\n"
      "v_fma_f32 v231, v231, s48, v21\n"
      "v_mfma_f32_16x16x32_bf16 a[160:163], v[184:187], a[240:243], a[160:163]\n"
      "v_exp_f32 v34, v230\n"
      "v_mfma_f32_16x16x32_bf16 v[84:87], v[164:167], a[252:255], v[84:87]\n"
      "v_exp_f32 v35, v231\n"
      "v_mfma_f32_16x16x32_bf16 v[84:87], v[160:163], a[248:251], v[84:87]\n"
      "v_cvt_pk_bf16_f32 v214, v32, v33\n"
      "v_or3_b32 v22, v22, v207, v214\n"
      "v_mfma_f32_16x16x32_bf16 v[132:135], v[168:171], a[248:251], v[132:135]\n"
      "v_cvt_pk_bf16_f32 v215, v34, v35\n"
      "v_bitop3_b32 v22, v22, v215, s49 bitop3:0xa8\n"
      "v_mfma_f32_16x16x32_bf16 v[132:135], v[172:175], a[252:255], v[132:135]\n"
      "v_cmp_ne_u32 vcc, 0, v22\n"
      "v_mfma_f32_16x16x32_bf16 a[116:119], v[180:183], a[252:255], a[116:119]\n"
      "v_mfma_f32_16x16x32_bf16 a[116:119], v[176:179], a[248:251], a[116:119]\n"
      "v_mfma_f32_16x16x32_bf16 a[164:167], v[184:187], a[248:251], a[164:167]\n"
      "s_waitcnt lgkmcnt(0)\n"
      "s_cbranch_vccnz L_rare_Ef1_1\n"
      "L_rare_Ef1_1_ret:\n"
      "v_mfma_f32_16x16x32_bf16 v[36:39], a[208:211], a[0:3], 0\n"
      "ds_read_b64_tr_b16 a[232:233], v236 offset:128\n"
      "v_add_u32 v4, s41, v6\n"
      "v_mfma_f32_16x16x32_bf16 v[36:39], a[212:215], a[4:7], v[36:39]\n"
      "ds_read_b64_tr_b16 a[234:235], v236 offset:6272\n"
      "v_add_u32 v5, s41, v7\n"
      "v_mfma_f32_16x16x32_bf16 v[36:39], a[216:219], a[8:11], v[36:39]\n"
      "ds_read_b64_tr_b16 a[236:237], v236 offset:12416\n"
      "v_add_u32 v8, s40, v12\n"
      "v_mfma_f32_16x16x32_bf16 v[36:39], a[220:223], a[12:15], v[36:39]\n"
      "s_mov_b32 m0, s47\n"
      "ds_read_b64_tr_b16 a[238:239], v236 offset:18560\n"
      "v_add_u32 v9, s40, v13\n"
      "v_mfma_f32_16x16x32_bf16 v[36:39], a[224:227], a[16:19], v[36:39]\n"
      "buffer_load_dwordx4 v1, s[24:27], s38 offen offset:0 lds\n"
      "v_mfma_f32_16x16x32_bf16 v[36:39], a[228:231], a[20:23], v[36:39]\n"
      "ds_read_b64_tr_b16 a[240:241], v237 offset:128\n"
      "v_add_u32 v10, s40, v14\n"
      "v_mfma_f32_16x16x32_bf16 v[40:43], a[228:231], a[44:47], 0\n"
      "ds_read_b64_tr_b16 a[242:243], v237 offset:6272\n"
      "v_add_u32 v11, s40, v15\n"
      "v_mfma_f32_16x16x32_bf16 v[40:43], a[224:227], a[40:43], v[40:43]\n"
      "s_mov_b32 s43, s40\n"
      "ds_read_b64_tr_b16 a[244:245], v237 offset:12416\n"
      "v_mfma_f32_16x16x32_bf16 v[40:43], a[220:223], a[36:39], v[40:43]\n"
      "s_mov_b32 s40, s41\n"
      "ds_read_b64_tr_b16 a[246:247], v237 offset:18560\n"
      "v_fma_f32 v36, v36, s48, v18\n"
      "v_mfma_f32_16x16x32_bf16 v[40:43], a[216:219], a[32:35], v[40:43]\n"
      "s_mov_b32 s41, s42\n"
      "ds_read_b64_tr_b16 a[248:249], v234 offset:256\n"
      "v_fma_f32 v37, v37, s48, v18\n"
      "v_mfma_f32_16x16x32_bf16 v[40:43], a[212:215], a[28:31], v[40:43]\n"
      "s_mov_b32 s42, s43\n"
      "ds_read_b64_tr_b16 a[250:251], v234 offset:6400\n"
      "v_fma_f32 v38, v38, s48, v18\n"
      "v_mfma_f32_16x16x32_bf16 v[40:43], a[208:211], a[24:27], v[40:43]\n"
      "ds_read_b64_tr_b16 a[252:253], v234 offset:12544\n"
      "v_fma_f32 v39, v39, s48, v18\n"
      "v_mfma_f32_16x16x32_bf16 v[44:47], a[208:211], a[48:51], 0\n"
      "ds_read_b64_tr_b16 a[254:255], v234 offset:18688\n"
      "v_mfma_f32_16x16x32_bf16 v[44:47], a[212:215], a[52:55], v[44:47]\n"
      "buffer_load_dwordx4 v2, s[24:27], s38 offen offset:1024 lds\n"
      "v_mfma_f32_16x16x32_bf16 v[44:47], a[216:219], a[56:59], v[44:47]\n"
      "buffer_load_dwordx4 v3, s[24:27], s38 offen offset:2048 lds\n"
      "v_mfma_f32_16x16x32_bf16 v[44:47], a[220:223], a[60:63], v[44:47]\n"
      "v_exp_f32 v28, v36\n"
      "v_mfma_f32_16x16x32_bf16 v[44:47], a[224:227], a[64:67], v[44:47]\n"
      "v_exp_f32 v29, v37\n"
      "v_mfma_f32_16x16x32_bf16 v[44:47], a[228:231], a[68:71], v[44:47]\n"
      "s_waitcnt lgkmcnt(0)\n"
      "v_exp_f32 v30, v38\n"
      "v_smfmac_f32_16x16x64_bf16 a[200:203], v[24:27], v[176:183], v16\n"
      "ds_read_b128 a[208:211], v232 offset:18432\n"
      "v_cvt_pk_bf16_f32 v192, v28, v29\n"
      "v_mfma_f32_16x16x32_bf16 v[88:91], v[164:167], a[236:239], v[88:91]\n"
      "v_exp_f32 v31, v39\n"
      "v_mfma_f32_16x16x32_bf16 v[88:91], v[160:163], a[232:235], v[88:91]\n"
      "ds_read_b128 a[212:215], v233 offset:18432\n"
      "v_cvt_pk_bf16_f32 v193, v30, v31\n"
      "v_mfma_f32_16x16x32_bf16 v[136:139], v[168:171], a[232:235], v[136:139]\n"
      "v_fma_f32 v40, v40, s48, v19\n"
      "v_fma_f32 v41, v41, s48, v19\n"
      "v_mfma_f32_16x16x32_bf16 v[136:139], v[172:175], a[236:239], v[136:139]\n"
      "ds_read_b128 a[216:219], v232 offset:18560\n"
      "v_fma_f32 v42, v42, s48, v19\n"
      "v_mfma_f32_16x16x32_bf16 a[120:123], v[180:183], a[236:239], a[120:123]\n"
      "v_exp_f32 v32, v40\n"
      "v_mfma_f32_16x16x32_bf16 a[120:123], v[176:179], a[232:235], a[120:123]\n"
      "ds_read_b128 a[220:223], v233 offset:18560\n"
      "v_fma_f32 v43, v43, s48, v19\n"
      "v_mfma_f32_16x16x32_bf16 a[168:171], v[184:187], a[232:235], a[168:171]\n"
      "v_exp_f32 v33, v41\n"
      "v_mfma_f32_16x16x32_bf16 v[92:95], v[164:167], a[244:247], v[92:95]\n"
      "ds_read_b128 a[224:227], v232 offset:18688\n"
      "v_cvt_pk_bf16_f32 v200, v32, v33\n"
      "v_mfma_f32_16x16x32_bf16 v[92:95], v[160:163], a[240:243], v[92:95]\n"
      "ds_read_b64_tr_b16 a[232:233], v235 offset:256\n"
      "v_or3_b32 v22, v192, v193, v200\n"
      "v_mfma_f32_16x16x32_bf16 v[140:143], v[168:171], a[240:243], v[140:143]\n"
      "ds_read_b128 a[228:231], v233 offset:18688\n"
      "v_fma_f32 v44, v44, s48, v20\n"
      "v_mfma_f32_16x16x32_bf16 v[140:143], v[172:175], a[244:247], v[140:143]\n"
      "ds_read_b64_tr_b16 a[234:235], v235 offset:6400\n"
      "v_fma_f32 v45, v45, s48, v20\n"
      "v_mfma_f32_16x16x32_bf16 a[124:127], v[180:183], a[244:247], a[124:127]\n"
      "ds_read_b64_tr_b16 a[236:237], v235 offset:12544\n"
      "v_fma_f32 v46, v46, s48, v20\n"
      "v_mfma_f32_16x16x32_bf16 a[124:127], v[176:179], a[240:243], a[124:127]\n"
      "ds_read_b64_tr_b16 a[238:239], v235 offset:18688\n"
      "v_fma_f32 v47, v47, s48, v20\n"
      "v_mfma_f32_16x16x32_bf16 a[172:175], v[184:187], a[240:243], a[172:175]\n"
      "v_exp_f32 v34, v42\n"
      "v_mfma_f32_16x16x32_bf16 v[96:99], v[164:167], a[252:255], v[96:99]\n"
      "v_exp_f32 v35, v43\n"
      "v_mfma_f32_16x16x32_bf16 v[96:99], v[160:163], a[248:251], v[96:99]\n"
      "ds_read_b64_tr_b16 a[240:241], v236 offset:256\n"
      "v_cvt_pk_bf16_f32 v201, v34, v35\n"
      "v_mfma_f32_16x16x32_bf16 v[144:147], v[168:171], a[248:251], v[144:147]\n"
      "v_exp_f32 v28, v44\n"
      "v_mfma_f32_16x16x32_bf16 v[144:147], v[172:175], a[252:255], v[144:147]\n"
      "v_exp_f32 v29, v45\n"
      "v_mfma_f32_16x16x32_bf16 a[128:131], v[180:183], a[252:255], a[128:131]\n"
      "ds_read_b64_tr_b16 a[242:243], v236 offset:6400\n"
      "v_cvt_pk_bf16_f32 v208, v28, v29\n"
      "v_mfma_f32_16x16x32_bf16 a[128:131], v[176:179], a[248:251], a[128:131]\n"
      "ds_read_b64_tr_b16 a[244:245], v236 offset:12544\n"
      "v_or3_b32 v22, v22, v201, v208\n"
      "v_mfma_f32_16x16x32_bf16 a[176:179], v[184:187], a[248:251], a[176:179]\n"
      "s_waitcnt lgkmcnt(6)\n"
      "v_exp_f32 v30, v46\n"
      "v_mfma_f32_16x16x32_bf16 v[216:219], a[208:211], a[0:3], 0\n"
      "s_add_u32 s34, s34, 6144\n"
      "v_exp_f32 v31, v47\n"
      "v_mfma_f32_16x16x32_bf16 v[216:219], a[212:215], a[4:7], v[216:219]\n"
      "ds_read_b64_tr_b16 a[246:247], v236 offset:18688\n"
      "v_cvt_pk_bf16_f32 v209, v30, v31\n"
      "v_mfma_f32_16x16x32_bf16 v[216:219], a[216:219], a[8:11], v[216:219]\n"
      "ds_read_b64_tr_b16 a[248:249], v237 offset:256\n"
      "v_mfma_f32_16x16x32_bf16 v[216:219], a[220:223], a[12:15], v[216:219]\n"
      "s_add_u32 m0, s47, 3072\n"
      "ds_read_b64_tr_b16 a[250:251], v237 offset:6400\n"
      "v_mfma_f32_16x16x32_bf16 v[216:219], a[224:227], a[16:19], v[216:219]\n"
      "buffer_load_dwordx4 v1, s[24:27], s39 offen offset:0 lds\n"
      "v_mfma_f32_16x16x32_bf16 v[216:219], a[228:231], a[20:23], v[216:219]\n"
      "buffer_load_dwordx4 v2, s[24:27], s39 offen offset:1024 lds\n"
      "v_mfma_f32_16x16x32_bf16 v[220:223], a[228:231], a[44:47], 0\n"
      "buffer_load_dwordx4 v3, s[24:27], s39 offen offset:2048 lds\n"
      "v_mfma_f32_16x16x32_bf16 v[220:223], a[224:227], a[40:43], v[220:223]\n"
      "s_nop 0\n"
      "ds_read_b64_tr_b16 a[252:253], v237 offset:12544\n"
      "v_mfma_f32_16x16x32_bf16 v[220:223], a[220:223], a[36:39], v[220:223]\n"
      "ds_read_b64_tr_b16 a[254:255], v237 offset:18688\n"
      "v_fma_f32 v216, v216, s48, v18\n"
      "v_mfma_f32_16x16x32_bf16 v[220:223], a[216:219], a[32:35], v[220:223]\n"
      "v_exp_f32 v32, v216\n"
      "v_mfma_f32_16x16x32_bf16 v[220:223], a[212:215], a[28:31], v[220:223]\n"
      "v_fma_f32 v217, v217, s48, v18\n"
      "v_fma_f32 v218, v218, s48, v18\n"
      "v_mfma_f32_16x16x32_bf16 v[220:223], a[208:211], a[24:27], v[220:223]\n"
      "s_mov_b32 s38, s36\n"
      "v_exp_f32 v33, v217\n"
      "v_mfma_f32_16x16x32_bf16 v[224:227], a[208:211], a[48:51], 0\n"
      "s_mov_b32 s39, s37\n"
      "v_exp_f32 v34, v218\n"
      "v_mfma_f32_16x16x32_bf16 v[224:227], a[212:215], a[52:55], v[224:227]\n"
      "s_add_u32 s12, s12, 1\n"
      "v_fma_f32 v219, v219, s48, v18\n"
      "v_cvt_pk_bf16_f32 v194, v32, v33\n"
      "v_mfma_f32_16x16x32_bf16 v[224:227], a[216:219], a[56:59], v[224:227]\n"
      "s_add_u32 s15, s13, s12\n"
      "v_exp_f32 v35, v219\n"
      "v_mfma_f32_16x16x32_bf16 v[224:227], a[220:223], a[60:63], v[224:227]\n"
      "s_and_b32 s15, s15, 15\n"
      "v_or3_b32 v22, v22, v209, v194\n"
      "v_cvt_pk_bf16_f32 v195, v34, v35\n"
      "v_mfma_f32_16x16x32_bf16 v[224:227], a[224:227], a[64:67], v[224:227]\n"
      "s_waitcnt vmcnt(12) lgkmcnt(0)\n"
      "v_fma_f32 v220, v220, s48, v19\n"
      "v_fma_f32 v221, v221, s48, v19\n"
      "v_mfma_f32_16x16x32_bf16 v[224:227], a[228:231], a[68:71], v[224:227]\n"
      "s_barrier\n"
      "v_exp_f32 v28, v220\n"
      "v_mfma_f32_16x16x32_bf16 a[204:207], v[24:27], v[184:187], a[204:207]\n"
      "s_mul_i32 s15, s15, 24576\n"
      "ds_read_b128 a[208:211], v4 offset:0\n"
      "v_fma_f32 v222, v222, s48, v19\n"
      "v_mfma_f32_16x16x32_bf16 v[100:103], v[164:167], a[236:239], v[100:103]\n"
      "s_lshr_b32 s16, s12, 4\n"
      "s_cmp_lt_u32 s34, 21504\n"
      "s_cbranch_scc0 L_not_E\n"
      "buffer_load_dword v255, v254, s[96:99], s34 offen\n"
      "L_not_E:\n"
      "v_mfma_f32_16x16x32_bf16 v[100:103], v[160:163], a[232:235], v[100:103]\n"
      "s_mul_i32 s16, s16, 393216\n"
      "ds_read_b128 a[212:215], v5 offset:0\n"
      "v_fma_f32 v223, v223, s48, v19\n"
      "v_mfma_f32_16x16x32_bf16 v[148:151], v[168:171], a[232:235], v[148:151]\n"
      "s_add_u32 s15, s15, s16\n"
      "v_exp_f32 v29, v221\n"
      "v_mfma_f32_16x16x32_bf16 v[148:151], v[172:175], a[236:239], v[148:151]\n"
      "s_add_u32 s36, s15, s14\n"
      "ds_read_b128 a[216:219], v4 offset:128\n"
      "v_cvt_pk_bf16_f32 v202, v28, v29\n"
      "v_mfma_f32_16x16x32_bf16 a[132:135], v[180:183], a[236:239], a[132:135]\n"
      "s_add_u32 s37, s36, 3072\n"
      "v_exp_f32 v30, v222\n"
      "v_mfma_f32_16x16x32_bf16 a[132:135], v[176:179], a[232:235], a[132:135]\n"
      "ds_read_b128 a[220:223], v5 offset:128\n"
      "v_or3_b32 v22, v22, v195, v202\n"
      "v_mfma_f32_16x16x32_bf16 a[180:183], v[184:187], a[232:235], a[180:183]\n"
      "v_exp_f32 v31, v223\n"
      "v_mfma_f32_16x16x32_bf16 v[104:107], v[164:167], a[244:247], v[104:107]\n"
      "ds_read_b128 a[224:227], v4 offset:256\n"
      "v_cvt_pk_bf16_f32 v203, v30, v31\n"
      "v_mfma_f32_16x16x32_bf16 v[104:107], v[160:163], a[240:243], v[104:107]\n"
      "v_fma_f32 v224, v224, s48, v20\n"
      "v_fma_f32 v225, v225, s48, v20\n"
      "v_mfma_f32_16x16x32_bf16 v[152:155], v[168:171], a[240:243], v[152:155]\n"
      "ds_read_b128 a[228:231], v5 offset:256\n"
      "v_fma_f32 v226, v226, s48, v20\n"
      "v_mfma_f32_16x16x32_bf16 v[152:155], v[172:175], a[244:247], v[152:155]\n"
      "v_exp_f32 v32, v224\n"
      "v_mfma_f32_16x16x32_bf16 a[136:139], v[180:183], a[244:247], a[136:139]\n"
      "v_exp_f32 v33, v225\n"
      "v_mfma_f32_16x16x32_bf16 a[136:139], v[176:179], a[240:243], a[136:139]\n"
      "v_exp_f32 v34, v226\n"
      "v_mfma_f32_16x16x32_bf16 a[184:187], v[184:187], a[240:243], a[184:187]\n"
      "v_fma_f32 v227, v227, s48, v20\n"
      "v_cvt_pk_bf16_f32 v210, v32, v33\n"
      "v_mfma_f32_16x16x32_bf16 v[108:111], v[164:167], a[252:255], v[108:111]\n"
      "v_exp_f32 v35, v227\n"
      "v_mfma_f32_16x16x32_bf16 v[108:111], v[160:163], a[248:251], v[108:111]\n"
      "v_or3_b32 v22, v22, v203, v210\n"
      "v_cvt_pk_bf16_f32 v211, v34, v35\n"
      "v_mfma_f32_16x16x32_bf16 v[156:159], v[168:171], a[248:251], v[156:159]\n"
      "v_bitop3_b32 v22, v22, v211, s49 bitop3:0xa8\n"
      "v_cmp_ne_u32 vcc, 0, v22\n"
      "v_mfma_f32_16x16x32_bf16 v[156:159], v[172:175], a[252:255], v[156:159]\n"
      "s_cmp_eq_u32 s50, 0\n"
      "v_mfma_f32_16x16x32_bf16 a[140:143], v[180:183], a[252:255], a[140:143]\n"
      "v_mfma_f32_16x16x32_bf16 a[140:143], v[176:179], a[248:251], a[140:143]\n"
      "s_cbranch_vccnz L_rare_Ef1_3\n"
      "L_rare_Ef1_3_ret:\n"
      "v_mfma_f32_16x16x32_bf16 a[188:191], v[184:187], a[248:251], a[188:191]\n"
      "s_waitcnt lgkmcnt(0)\n"
      "s_cbranch_scc1 L_last_E\n"
      "L_mid_E:\n"
      "v_mfma_f32_16x16x32_bf16 v[36:39], a[208:211], a[0:3], 0\n"
      "s_add_u32 s46, s44, s42\n"
      "ds_read_b64_tr_b16 a[232:233], v8 offset:0\n"
      "v_mfma_f32_16x16x32_bf16 v[36:39], a[212:215], a[4:7], v[36:39]\n"
      "s_add_u32 s47, s45, s41\n"
      "ds_read_b64_tr_b16 a[234:235], v8 offset:6144\n"
      "v_mfma_f32_16x16x32_bf16 v[36:39], a[216:219], a[8:11], v[36:39]\n"
      "ds_read_b64_tr_b16 a[236:237], v8 offset:12288\n"
      "v_mfma_f32_16x16x32_bf16 v[36:39], a[220:223], a[12:15], v[36:39]\n"
      "s_mov_b32 m0, s46\n"
      "ds_read_b64_tr_b16 a[238:239], v8 offset:18432\n"
      "v_mfma_f32_16x16x32_bf16 v[36:39], a[224:227], a[16:19], v[36:39]\n"
      "buffer_load_dwordx4 v1, s[20:23], s36 offen offset:0 lds\n"
      "v_mfma_f32_16x16x32_bf16 v[36:39], a[228:231], a[20:23], v[36:39]\n"
      "buffer_load_dwordx4 v2, s[20:23], s36 offen offset:1024 lds\n"
      "v_mfma_f32_16x16x32_bf16 v[40:43], a[228:231], a[44:47], 0\n"
      "buffer_load_dwordx4 v3, s[20:23], s36 offen offset:2048 lds\n"
      "v_mfma_f32_16x16x32_bf16 v[40:43], a[224:227], a[40:43], v[40:43]\n"
      "s_nop 0\n"
      "ds_read_b64_tr_b16 a[240:241], v9 offset:0\n"
      "v_mfma_f32_16x16x32_bf16 v[40:43], a[220:223], a[36:39], v[40:43]\n"
      "ds_read_b64_tr_b16 a[242:243], v9 offset:6144\n"
      "v_fma_f32 v36, v36, s48, v18\n"
      "v_mfma_f32_16x16x32_bf16 v[40:43], a[216:219], a[32:35], v[40:43]\n"
      "ds_read_b64_tr_b16 a[244:245], v9 offset:12288\n"
      "v_fma_f32 v37, v37, s48, v18\n"
      "v_mfma_f32_16x16x32_bf16 v[40:43], a[212:215], a[28:31], v[40:43]\n"
      "ds_read_b64_tr_b16 a[246:247], v9 offset:18432\n"
      "v_fma_f32 v38, v38, s48, v18\n"
      "v_mfma_f32_16x16x32_bf16 v[40:43], a[208:211], a[24:27], v[40:43]\n"
      "ds_read_b64_tr_b16 a[248:249], v10 offset:0\n"
      "v_fma_f32 v39, v39, s48, v18\n"
      "v_mfma_f32_16x16x32_bf16 v[44:47], a[208:211], a[48:51], 0\n"
      "v_exp_f32 v28, v36\n"
      "v_mfma_f32_16x16x32_bf16 v[44:47], a[212:215], a[52:55], v[44:47]\n"
      "v_exp_f32 v29, v37\n"
      "v_mfma_f32_16x16x32_bf16 v[44:47], a[216:219], a[56:59], v[44:47]\n"
      "ds_read_b64_tr_b16 a[250:251], v10 offset:6144\n"
      "v_cvt_pk_bf16_f32 v160, v28, v29\n"
      "v_mfma_f32_16x16x32_bf16 v[44:47], a[220:223], a[60:63], v[44:47]\n"
      "ds_read_b64_tr_b16 a[252:253], v10 offset:12288\n"
      "v_fma_f32 v40, v40, s48, v19\n"
      "v_mfma_f32_16x16x32_bf16 v[44:47], a[224:227], a[64:67], v[44:47]\n"
      "ds_read_b64_tr_b16 a[254:255], v10 offset:18432\n"
      "v_fma_f32 v41, v41, s48, v19\n"
      "v_mfma_f32_16x16x32_bf16 v[44:47], a[228:231], a[68:71], v[44:47]\n"
      "v_exp_f32 v30, v38\n"
      "v_mfma_f32_16x16x32_bf16 v[48:51], a[228:231], a[92:95], 0\n"
      "v_exp_f32 v31, v39\n"
      "v_mfma_f32_16x16x32_bf16 v[48:51], a[224:227], a[88:91], v[48:51]\n"
      "v_exp_f32 v32, v40\n"
      "v_mfma_f32_16x16x32_bf16 v[48:51], a[220:223], a[84:87], v[48:51]\n"
      "v_exp_f32 v33, v41\n"
      "v_mfma_f32_16x16x32_bf16 v[48:51], a[216:219], a[80:83], v[48:51]\n"
      "v_cvt_pk_bf16_f32 v161, v30, v31\n"
      "v_fma_f32 v42, v42, s48, v19\n"
      "v_mfma_f32_16x16x32_bf16 v[48:51], a[212:215], a[76:79], v[48:51]\n"
      "v_exp_f32 v34, v42\n"
      "v_mfma_f32_16x16x32_bf16 v[48:51], a[208:211], a[72:75], v[48:51]\n"
      "s_waitcnt lgkmcnt(0)\n"
      "v_fma_f32 v43, v43, s48, v19\n"
      "v_cvt_pk_bf16_f32 v168, v32, v33\n"
      "v_smfmac_f32_16x16x64_bf16 a[192:195], v[24:27], v[188:195], v16\n"
      "ds_read_b128 a[208:211], v4 offset:6144\n"
      "v_or3_b32 v22, v160, v161, v168\n"
      "v_mfma_f32_16x16x32_bf16 v[64:67], v[192:195], a[236:239], v[64:67]\n"
      "v_exp_f32 v35, v43\n"
      "v_mfma_f32_16x16x32_bf16 v[64:67], v[188:191], a[232:235], v[64:67]\n"
      "ds_read_b128 a[212:215], v5 offset:6144\n"
      "v_cvt_pk_bf16_f32 v169, v34, v35\n"
      "v_mfma_f32_16x16x32_bf16 v[112:115], v[196:199], a[232:235], v[112:115]\n"
      "v_fma_f32 v44, v44, s48, v20\n"
      "v_fma_f32 v45, v45, s48, v20\n"
      "v_mfma_f32_16x16x32_bf16 v[112:115], v[200:203], a[236:239], v[112:115]\n"
      "ds_read_b128 a[216:219], v4 offset:6272\n"
      "v_fma_f32 v46, v46, s48, v20\n"
      "v_mfma_f32_16x16x32_bf16 a[96:99], v[208:211], a[236:239], a[96:99]\n"
      "v_exp_f32 v28, v44\n"
      "v_mfma_f32_16x16x32_bf16 a[96:99], v[204:207], a[232:235], a[96:99]\n"
      "ds_read_b128 a[220:223], v5 offset:6272\n"
      "v_fma_f32 v47, v47, s48, v20\n"
      "v_mfma_f32_16x16x32_bf16 a[144:147], v[212:215], a[232:235], a[144:147]\n"
      "v_exp_f32 v29, v45\n"
      "v_mfma_f32_16x16x32_bf16 v[68:71], v[192:195], a[244:247], v[68:71]\n"
      "ds_read_b128 a[224:227], v4 offset:6400\n"
      "v_cvt_pk_bf16_f32 v176, v28, v29\n"
      "v_mfma_f32_16x16x32_bf16 v[68:71], v[188:191], a[240:243], v[68:71]\n"
      "ds_read_b64_tr_b16 a[232:233], v11 offset:0\n"
      "v_or3_b32 v22, v22, v169, v176\n"
      "v_mfma_f32_16x16x32_bf16 v[116:119], v[196:199], a[240:243], v[116:119]\n"
      "ds_read_b128 a[228:231], v5 offset:6400\n"
      "v_fma_f32 v48, v48, s48, v21\n"
      "v_mfma_f32_16x16x32_bf16 v[116:119], v[200:203], a[244:247], v[116:119]\n"
      "ds_read_b64_tr_b16 a[234:235], v11 offset:6144\n"
      "v_fma_f32 v49, v49, s48, v21\n"
      "v_mfma_f32_16x16x32_bf16 a[100:103], v[208:211], a[244:247], a[100:103]\n"
      "ds_read_b64_tr_b16 a[236:237], v11 offset:12288\n"
      "v_fma_f32 v50, v50, s48, v21\n"
      "v_mfma_f32_16x16x32_bf16 a[100:103], v[204:207], a[240:243], a[100:103]\n"
      "ds_read_b64_tr_b16 a[238:239], v11 offset:18432\n"
      "v_fma_f32 v51, v51, s48, v21\n"
      "v_mfma_f32_16x16x32_bf16 a[148:151], v[212:215], a[240:243], a[148:151]\n"
      "v_exp_f32 v30, v46\n"
      "v_mfma_f32_16x16x32_bf16 v[72:75], v[192:195], a[252:255], v[72:75]\n"
      "v_exp_f32 v31, v47\n"
      "v_mfma_f32_16x16x32_bf16 v[72:75], v[188:191], a[248:251], v[72:75]\n"
      "ds_read_b64_tr_b16 a[240:241], v8 offset:128\n"
      "v_cvt_pk_bf16_f32 v177, v30, v31\n"
      "v_mfma_f32_16x16x32_bf16 v[120:123], v[196:199], a[248:251], v[120:123]\n"
      "v_exp_f32 v32, v48\n"
      "v_mfma_f32_16x16x32_bf16 v[120:123], v[200:203], a[252:255], v[120:123]\n"
      "v_exp_f32 v33, v49\n"
      "v_mfma_f32_16x16x32_bf16 a[104:107], v[208:211], a[252:255], a[104:107]\n"
      "ds_read_b64_tr_b16 a[242:243], v8 offset:6272\n"
      "v_cvt_pk_bf16_f32 v184, v32, v33\n"
      "v_mfma_f32_16x16x32_bf16 a[104:107], v[204:207], a[248:251], a[104:107]\n"
      "ds_read_b64_tr_b16 a[244:245], v8 offset:12416\n"
      "v_or3_b32 v22, v22, v177, v184\n"
      "v_mfma_f32_16x16x32_bf16 a[152:155], v[212:215], a[248:251], a[152:155]\n"
      "s_waitcnt lgkmcnt(6)\n"
      "v_exp_f32 v34, v50\n"
      "v_mfma_f32_16x16x32_bf16 v[216:219], a[208:211], a[0:3], 0\n"
      "v_exp_f32 v35, v51\n"
      "v_mfma_f32_16x16x32_bf16 v[216:219], a[212:215], a[4:7], v[216:219]\n"
      "ds_read_b64_tr_b16 a[246:247], v8 offset:18560\n"
      "v_cvt_pk_bf16_f32 v185, v34, v35\n"
      "v_mfma_f32_16x16x32_bf16 v[216:219], a[216:219], a[8:11], v[216:219]\n"
      "ds_read_b64_tr_b16 a[248:249], v9 offset:128\n"
      "v_mfma_f32_16x16x32_bf16 v[216:219], a[220:223], a[12:15], v[216:219]\n"
      "s_add_u32 m0, s46, 3072\n"
      "ds_read_b64_tr_b16 a[250:251], v9 offset:6272\n"
      "v_mfma_f32_16x16x32_bf16 v[216:219], a[224:227], a[16:19], v[216:219]\n"
      "buffer_load_dwordx4 v1, s[20:23], s37 offen offset:0 lds\n"
      "v_mfma_f32_16x16x32_bf16 v[216:219], a[228:231], a[20:23], v[216:219]\n"
      "buffer_load_dwordx4 v2, s[20:23], s37 offen offset:1024 lds\n"
      "v_mfma_f32_16x16x32_bf16 v[220:223], a[228:231], a[44:47], 0\n"
      "buffer_load_dwordx4 v3, s[20:23], s37 offen offset:2048 lds\n"
      "v_mfma_f32_16x16x32_bf16 v[220:223], a[224:227], a[40:43], v[220:223]\n"
      "s_nop 0\n"
      "ds_read_b64_tr_b16 a[252:253], v9 offset:12416\n"
      "v_mfma_f32_16x16x32_bf16 v[220:223], a[220:223], a[36:39], v[220:223]\n"
      "ds_read_b64_tr_b16 a[254:255], v9 offset:18560\n"
      "v_fma_f32 v216, v216, s48, v18\n"
      "v_mfma_f32_16x16x32_bf16 v[220:223], a[216:219], a[32:35], v[220:223]\n"
      "v_exp_f32 v28, v216\n"
      "v_mfma_f32_16x16x32_bf16 v[220:223], a[212:215], a[28:31], v[220:223]\n"
      "v_fma_f32 v217, v217, s48, v18\n"
      "v_fma_f32 v218, v218, s48, v18\n"
      "v_mfma_f32_16x16x32_bf16 v[220:223], a[208:211], a[24:27], v[220:223]\n"
      "v_exp_f32 v29, v217\n"
      "v_mfma_f32_16x16x32_bf16 v[224:227], a[208:211], a[48:51], 0\n"
      "v_exp_f32 v30, v218\n"
      "v_mfma_f32_16x16x32_bf16 v[224:227], a[212:215], a[52:55], v[224:227]\n"
      "v_fma_f32 v219, v219, s48, v18\n"
      "v_cvt_pk_bf16_f32 v162, v28, v29\n"
      "v_mfma_f32_16x16x32_bf16 v[224:227], a[216:219], a[56:59], v[224:227]\n"
      "v_exp_f32 v31, v219\n"
      "v_mfma_f32_16x16x32_bf16 v[224:227], a[220:223], a[60:63], v[224:227]\n"
      "v_or3_b32 v22, v22, v185, v162\n"
      "v_cvt_pk_bf16_f32 v163, v30, v31\n"
      "v_mfma_f32_16x16x32_bf16 v[224:227], a[224:227], a[64:67], v[224:227]\n"
      "v_fma_f32 v220, v220, s48, v19\n"
      "v_fma_f32 v221, v221, s48, v19\n"
      "v_mfma_f32_16x16x32_bf16 v[224:227], a[228:231], a[68:71], v[224:227]\n"
      "v_exp_f32 v32, v220\n"
      "v_mfma_f32_16x16x32_bf16 v[228:231], a[228:231], a[92:95], 0\n"
      "v_exp_f32 v33, v221\n"
      "v_mfma_f32_16x16x32_bf16 v[228:231], a[224:227], a[88:91], v[228:231]\n"
      "v_fma_f32 v222, v222, s48, v19\n"
      "v_fma_f32 v223, v223, s48, v19\n"
      "v_mfma_f32_16x16x32_bf16 v[228:231], a[220:223], a[84:87], v[228:231]\n"
      "v_exp_f32 v34, v222\n"
      "v_mfma_f32_16x16x32_bf16 v[228:231], a[216:219], a[80:83], v[228:231]\n"
      "v_exp_f32 v35, v223\n"
      "v_mfma_f32_16x16x32_bf16 v[228:231], a[212:215], a[76:79], v[228:231]\n"
      "v_cvt_pk_bf16_f32 v170, v32, v33\n"
      "v_or3_b32 v22, v22, v163, v170\n"
      "v_mfma_f32_16x16x32_bf16 v[228:231], a[208:211], a[72:75], v[228:231]\n"
      "s_waitcnt lgkmcnt(0)\n"
      "v_cvt_pk_bf16_f32 v171, v34, v35\n"
      "v_fma_f32 v224, v224, s48, v20\n"
      "v_smfmac_f32_16x16x64_bf16 a[196:199], v[24:27], v[196:203], v16\n"
      "ds_read_b128 a[208:211], v4 offset:12288\n"
      "v_fma_f32 v225, v225, s48, v20\n"
      "v_mfma_f32_16x16x32_bf16 v[76:79], v[192:195], a[236:239], v[76:79]\n"
      "v_exp_f32 v28, v224\n"
      "v_mfma_f32_16x16x32_bf16 v[76:79], v[188:191], a[232:235], v[76:79]\n"
      "ds_read_b128 a[212:215], v5 offset:12288\n"
      "v_fma_f32 v226, v226, s48, v20\n"
      "v_mfma_f32_16x16x32_bf16 v[124:127], v[196:199], a[232:235], v[124:127]\n"
      "v_exp_f32 v29, v225\n"
      "v_mfma_f32_16x16x32_bf16 v[124:127], v[200:203], a[236:239], v[124:127]\n"
      "ds_read_b128 a[216:219], v4 offset:12416\n"
      "v_fma_f32 v227, v227, s48, v20\n"
      "v_mfma_f32_16x16x32_bf16 a[108:111], v[208:211], a[236:239], a[108:111]\n"
      "v_exp_f32 v30, v226\n"
      "v_mfma_f32_16x16x32_bf16 a[108:111], v[204:207], a[232:235], a[108:111]\n"
      "ds_read_b128 a[220:223], v5 offset:12416\n"
      "v_cvt_pk_bf16_f32 v178, v28, v29\n"
      "v_mfma_f32_16x16x32_bf16 a[156:159], v[212:215], a[232:235], a[156:159]\n"
      "v_exp_f32 v31, v227\n"
      "v_mfma_f32_16x16x32_bf16 v[80:83], v[192:195], a[244:247], v[80:83]\n"
      "ds_read_b128 a[224:227], v4 offset:12544\n"
      "v_or3_b32 v22, v22, v171, v178\n"
      "v_mfma_f32_16x16x32_bf16 v[80:83], v[188:191], a[240:243], v[80:83]\n"
      "v_cvt_pk_bf16_f32 v179, v30, v31\n"
      "v_fma_f32 v228, v228, s48, v21\n"
      "v_mfma_f32_16x16x32_bf16 v[128:131], v[196:199], a[240:243], v[128:131]\n"
      "ds_read_b128 a[228:231], v5 offset:12544\n"
      "v_fma_f32 v229, v229, s48, v21\n"
      "v_mfma_f32_16x16x32_bf16 v[128:131], v[200:203], a[244:247], v[128:131]\n"
      "v_exp_f32 v32, v228\n"
      "v_mfma_f32_16x16x32_bf16 a[112:115], v[208:211], a[244:247], a[112:115]\n"
      "v_exp_f32 v33, v229\n"
      "v_mfma_f32_16x16x32_bf16 a[112:115], v[204:207], a[240:243], a[112:115]\n"
      "v_fma_f32 v230, v230, s48, v21\n"
      "v_fma_f32 v231, v231, s48, v21\n"
      "v_mfma_f32_16x16x32_bf16 a[160:163], v[212:215], a[240:243], a[160:163]\n"
      "v_exp_f32 v34, v230\n"
      "v_mfma_f32_16x16x32_bf16 v[84:87], v[192:195], a[252:255], v[84:87]\n"
      "v_exp_f32 v35, v231\n"
      "v_mfma_f32_16x16x32_bf16 v[84:87], v[188:191], a[248:251], v[84:87]\n"
      "v_cvt_pk_bf16_f32 v186, v32, v33\n"
      "v_or3_b32 v22, v22, v179, v186\n"
      "v_mfma_f32_16x16x32_bf16 v[132:135], v[196:199], a[248:251], v[132:135]\n"
      "v_cvt_pk_bf16_f32 v187, v34, v35\n"
      "v_bitop3_b32 v22, v22, v187, s49 bitop3:0xa8\n"
      "v_mfma_f32_16x16x32_bf16 v[132:135], v[200:203], a[252:255], v[132:135]\n"
      "v_cmp_ne_u32 vcc, 0, v22\n"
      "v_mfma_f32_16x16x32_bf16 a[116:119], v[208:211], a[252:255], a[116:119]\n"
      "v_mfma_f32_16x16x32_bf16 a[116:119], v[204:207], a[248:251], a[116:119]\n"
      "v_mfma_f32_16x16x32_bf16 a[164:167], v[212:215], a[248:251], a[164:167]\n"
      "s_waitcnt lgkmcnt(0)\n"
      "s_cbranch_vccnz L_rare_Ef0_1\n"
      "L_rare_Ef0_1_ret:\n"
      "v_mfma_f32_16x16x32_bf16 v[36:39], a[208:211], a[0:3], 0\n"
      "ds_read_b64_tr_b16 a[232:233], v10 offset:128\n"
      "v_add_u32 v232, s41, v6\n"
      "v_mfma_f32_16x16x32_bf16 v[36:39], a[212:215], a[4:7], v[36:39]\n"
      "ds_read_b64_tr_b16 a[234:235], v10 offset:6272\n"
      "v_add_u32 v233, s41, v7\n"
      "v_mfma_f32_16x16x32_bf16 v[36:39], a[216:219], a[8:11], v[36:39]\n"
      "ds_read_b64_tr_b16 a[236:237], v10 offset:12416\n"
      "v_add_u32 v234, s40, v12\n"
      "v_mfma_f32_16x16x32_bf16 v[36:39], a[220:223], a[12:15], v[36:39]\n"
      "s_mov_b32 m0, s47\n"
      "ds_read_b64_tr_b16 a[238:239], v10 offset:18560\n"
      "v_add_u32 v235, s40, v13\n"
      "v_mfma_f32_16x16x32_bf16 v[36:39], a[224:227], a[16:19], v[36:39]\n"
      "buffer_load_dwordx4 v1, s[24:27], s38 offen offset:0 lds\n"
      "v_mfma_f32_16x16x32_bf16 v[36:39], a[228:231], a[20:23], v[36:39]\n"
      "ds_read_b64_tr_b16 a[240:241], v11 offset:128\n"
      "v_add_u32 v236, s40, v14\n"
      "v_mfma_f32_16x16x32_bf16 v[40:43], a[228:231], a[44:47], 0\n"
      "ds_read_b64_tr_b16 a[242:243], v11 offset:6272\n"
      "v_add_u32 v237, s40, v15\n"
      "v_mfma_f32_16x16x32_bf16 v[40:43], a[224:227], a[40:43], v[40:43]\n"
      "s_mov_b32 s43, s40\n"
      "ds_read_b64_tr_b16 a[244:245], v11 offset:12416\n"
      "v_mfma_f32_16x16x32_bf16 v[40:43], a[220:223], a[36:39], v[40:43]\n"
      "s_mov_b32 s40, s41\n"
      "ds_read_b64_tr_b16 a[246:247], v11 offset:18560\n"
      "v_fma_f32 v36, v36, s48, v18\n"
      "v_mfma_f32_16x16x32_bf16 v[40:43], a[216:219], a[32:35], v[40:43]\n"
      "s_mov_b32 s41, s42\n"
      "ds_read_b64_tr_b16 a[248:249], v8 offset:256\n"
      "v_fma_f32 v37, v37, s48, v18\n"
      "v_mfma_f32_16x16x32_bf16 v[40:43], a[212:215], a[28:31], v[40:43]\n"
      "s_mov_b32 s42, s43\n"
      "ds_read_b64_tr_b16 a[250:251], v8 offset:6400\n"
      "v_fma_f32 v38, v38, s48, v18\n"
      "v_mfma_f32_16x16x32_bf16 v[40:43], a[208:211], a[24:27], v[40:43]\n"
      "ds_read_b64_tr_b16 a[252:253], v8 offset:12544\n"
      "v_fma_f32 v39, v39, s48, v18\n"
      "v_mfma_f32_16x16x32_bf16 v[44:47], a[208:211], a[48:51], 0\n"
      "ds_read_b64_tr_b16 a[254:255], v8 offset:18688\n"
      "v_mfma_f32_16x16x32_bf16 v[44:47], a[212:215], a[52:55], v[44:47]\n"
      "buffer_load_dwordx4 v2, s[24:27], s38 offen offset:1024 lds\n"
      "v_mfma_f32_16x16x32_bf16 v[44:47], a[216:219], a[56:59], v[44:47]\n"
      "buffer_load_dwordx4 v3, s[24:27], s38 offen offset:2048 lds\n"
      "v_mfma_f32_16x16x32_bf16 v[44:47], a[220:223], a[60:63], v[44:47]\n"
      "v_exp_f32 v28, v36\n"
      "v_mfma_f32_16x16x32_bf16 v[44:47], a[224:227], a[64:67], v[44:47]\n"
      "v_exp_f32 v29, v37\n"
      "v_mfma_f32_16x16x32_bf16 v[44:47], a[228:231], a[68:71], v[44:47]\n"
      "s_waitcnt lgkmcnt(0)\n"
      "v_exp_f32 v30, v38\n"
      "v_smfmac_f32_16x16x64_bf16 a[200:203], v[24:27], v[204:211], v16\n"
      "ds_read_b128 a[208:211], v4 offset:18432\n"
      "v_cvt_pk_bf16_f32 v164, v28, v29\n"
      "v_mfma_f32_16x16x32_bf16 v[88:91], v[192:195], a[236:239], v[88:91]\n"
      "v_exp_f32 v31, v39\n"
      "v_mfma_f32_16x16x32_bf16 v[88:91], v[188:191], a[232:235], v[88:91]\n"
      "ds_read_b128 a[212:215], v5 offset:18432\n"
      "v_cvt_pk_bf16_f32 v165, v30, v31\n"
      "v_mfma_f32_16x16x32_bf16 v[136:139], v[196:199], a[232:235], v[136:139]\n"
      "v_fma_f32 v40, v40, s48, v19\n"
      "v_fma_f32 v41, v41, s48, v19\n"
      "v_mfma_f32_16x16x32_bf16 v[136:139], v[200:203], a[236:239], v[136:139]\n"
      "ds_read_b128 a[216:219], v4 offset:18560\n"
      "v_fma_f32 v42, v42, s48, v19\n"
      "v_mfma_f32_16x16x32_bf16 a[120:123], v[208:211], a[236:239], a[120:123]\n"
      "v_exp_f32 v32, v40\n"
      "v_mfma_f32_16x16x32_bf16 a[120:123], v[204:207], a[232:235], a[120:123]\n"
      "ds_read_b128 a[220:223], v5 offset:18560\n"
      "v_fma_f32 v43, v43, s48, v19\n"
      "v_mfma_f32_16x16x32_bf16 a[168:171], v[212:215], a[232:235], a[168:171]\n"
      "v_exp_f32 v33, v41\n"
      "v_mfma_f32_16x16x32_bf16 v[92:95], v[192:195], a[244:247], v[92:95]\n"
      "ds_read_b128 a[224:227], v4 offset:18688\n"
      "v_cvt_pk_bf16_f32 v172, v32, v33\n"
      "v_mfma_f32_16x16x32_bf16 v[92:95], v[188:191], a[240:243], v[92:95]\n"
      "ds_read_b64_tr_b16 a[232:233], v9 offset:256\n"
      "v_or3_b32 v22, v164, v165, v172\n"
      "v_mfma_f32_16x16x32_bf16 v[140:143], v[196:199], a[240:243], v[140:143]\n"
      "ds_read_b128 a[228:231], v5 offset:18688\n"
      "v_fma_f32 v44, v44, s48, v20\n"
      "v_mfma_f32_16x16x32_bf16 v[140:143], v[200:203], a[244:247], v[140:143]\n"
      "ds_read_b64_tr_b16 a[234:235], v9 offset:6400\n"
      "v_fma_f32 v45, v45, s48, v20\n"
      "v_mfma_f32_16x16x32_bf16 a[124:127], v[208:211], a[244:247], a[124:127]\n"
      "ds_read_b64_tr_b16 a[236:237], v9 offset:12544\n"
      "v_fma_f32 v46, v46, s48, v20\n"
      "v_mfma_f32_16x16x32_bf16 a[124:127], v[204:207], a[240:243], a[124:127]\n"
      "ds_read_b64_tr_b16 a[238:239], v9 offset:18688\n"
      "v_fma_f32 v47, v47, s48, v20\n"
      "v_mfma_f32_16x16x32_bf16 a[172:175], v[212:215], a[240:243], a[172:175]\n"
      "v_exp_f32 v34, v42\n"
      "v_mfma_f32_16x16x32_bf16 v[96:99], v[192:195], a[252:255], v[96:99]\n"
      "v_exp_f32 v35, v43\n"
      "v_mfma_f32_16x16x32_bf16 v[96:99], v[188:191], a[248:251], v[96:99]\n"
      "ds_read_b64_tr_b16 a[240:241], v10 offset:256\n"
      "v_cvt_pk_bf16_f32 v173, v34, v35\n"
      "v_mfma_f32_16x16x32_bf16 v[144:147], v[196:199], a[248:251], v[144:147]\n"
      "v_exp_f32 v28, v44\n"
      "v_mfma_f32_16x16x32_bf16 v[144:147], v[200:203], a[252:255], v[144:147]\n"
      "v_exp_f32 v29, v45\n"
      "v_mfma_f32_16x16x32_bf16 a[128:131], v[208:211], a[252:255], a[128:131]\n"
      "ds_read_b64_tr_b16 a[242:243], v10 offset:6400\n"
      "v_cvt_pk_bf16_f32 v180, v28, v29\n"
      "v_mfma_f32_16x16x32_bf16 a[128:131], v[204:207], a[248:251], a[128:131]\n"
      "ds_read_b64_tr_b16 a[244:245], v10 offset:12544\n"
      "v_or3_b32 v22, v22, v173, v180\n"
      "v_mfma_f32_16x16x32_bf16 a[176:179], v[212:215], a[248:251], a[176:179]\n"
      "s_waitcnt lgkmcnt(6)\n"
      "v_exp_f32 v30, v46\n"
      "v_mfma_f32_16x16x32_bf16 v[216:219], a[208:211], a[0:3], 0\n"
      "v_exp_f32 v31, v47\n"
      "v_mfma_f32_16x16x32_bf16 v[216:219], a[212:215], a[4:7], v[216:219]\n"
      "ds_read_b64_tr_b16 a[246:247], v10 offset:18688\n"
      "v_cvt_pk_bf16_f32 v181, v30, v31\n"
      "v_mfma_f32_16x16x32_bf16 v[216:219], a[216:219], a[8:11], v[216:219]\n"
      "ds_read_b64_tr_b16 a[248:249], v11 offset:256\n"
      "v_mfma_f32_16x16x32_bf16 v[216:219], a[220:223], a[12:15], v[216:219]\n"
      "s_add_u32 m0, s47, 3072\n"
      "ds_read_b64_tr_b16 a[250:251], v11 offset:6400\n"
      "v_mfma_f32_16x16x32_bf16 v[216:219], a[224:227], a[16:19], v[216:219]\n"
      "buffer_load_dwordx4 v1, s[24:27], s39 offen offset:0 lds\n"
      "v_mfma_f32_16x16x32_bf16 v[216:219], a[228:231], a[20:23], v[216:219]\n"
      "buffer_load_dwordx4 v2, s[24:27], s39 offen offset:1024 lds\n"
      "v_mfma_f32_16x16x32_bf16 v[220:223], a[228:231], a[44:47], 0\n"
      "buffer_load_dwordx4 v3, s[24:27], s39 offen offset:2048 lds\n"
      "v_mfma_f32_16x16x32_bf16 v[220:223], a[224:227], a[40:43], v[220:223]\n"
      "s_nop 0\n"
      "ds_read_b64_tr_b16 a[252:253], v11 offset:12544\n"
      "v_mfma_f32_16x16x32_bf16 v[220:223], a[220:223], a[36:39], v[220:223]\n"
      "ds_read_b64_tr_b16 a[254:255], v11 offset:18688\n"
      "v_fma_f32 v216, v216, s48, v18\n"
      "v_mfma_f32_16x16x32_bf16 v[220:223], a[216:219], a[32:35], v[220:223]\n"
      "v_exp_f32 v32, v216\n"
      "v_mfma_f32_16x16x32_bf16 v[220:223], a[212:215], a[28:31], v[220:223]\n"
      "v_fma_f32 v217, v217, s48, v18\n"
      "v_fma_f32 v218, v218, s48, v18\n"
      "v_mfma_f32_16x16x32_bf16 v[220:223], a[208:211], a[24:27], v[220:223]\n"
      "s_mov_b32 s38, s36\n"
      "v_exp_f32 v33, v217\n"
      "v_mfma_f32_16x16x32_bf16 v[224:227], a[208:211], a[48:51], 0\n"
      "s_mov_b32 s39, s37\n"
      "v_exp_f32 v34, v218\n"
      "v_mfma_f32_16x16x32_bf16 v[224:227], a[212:215], a[52:55], v[224:227]\n"
      "s_add_u32 s12, s12, 1\n"
      "v_fma_f32 v219, v219, s48, v18\n"
      "v_cvt_pk_bf16_f32 v166, v32, v33\n"
      "v_mfma_f32_16x16x32_bf16 v[224:227], a[216:219], a[56:59], v[224:227]\n"
      "s_add_u32 s15, s13, s12\n"
      "v_exp_f32 v35, v219\n"
      "v_mfma_f32_16x16x32_bf16 v[224:227], a[220:223], a[60:63], v[224:227]\n"
      "s_and_b32 s15, s15, 15\n"
      "v_or3_b32 v22, v22, v181, v166\n"
      "v_cvt_pk_bf16_f32 v167, v34, v35\n"
      "v_mfma_f32_16x16x32_bf16 v[224:227], a[224:227], a[64:67], v[224:227]\n"
      "s_waitcnt vmcnt(12) lgkmcnt(0)\n"
      "v_fma_f32 v220, v220, s48, v19\n"
      "v_fma_f32 v221, v221, s48, v19\n"
      "v_mfma_f32_16x16x32_bf16 v[224:227], a[228:231], a[68:71], v[224:227]\n"
      "s_barrier\n"
      "v_exp_f32 v28, v220\n"
      "v_mfma_f32_16x16x32_bf16 a[204:207], v[24:27], v[212:215], a[204:207]\n"
      "s_mul_i32 s15, s15, 24576\n"
      "ds_read_b128 a[208:211], v232 offset:0\n"
      "v_fma_f32 v222, v222, s48, v19\n"
      "v_mfma_f32_16x16x32_bf16 v[100:103], v[192:195], a[236:239], v[100:103]\n"
      "s_lshr_b32 s16, s12, 4\n"
      "v_exp_f32 v29, v221\n"
      "v_mfma_f32_16x16x32_bf16 v[100:103], v[188:191], a[232:235], v[100:103]\n"
      "s_mul_i32 s16, s16, 393216\n"
      "ds_read_b128 a[212:215], v233 offset:0\n"
      "v_fma_f32 v223, v223, s48, v19\n"
      "v_mfma_f32_16x16x32_bf16 v[148:151], v[196:199], a[232:235], v[148:151]\n"
      "s_add_u32 s15, s15, s16\n"
      "v_exp_f32 v30, v222\n"
      "v_mfma_f32_16x16x32_bf16 v[148:151], v[200:203], a[236:239], v[148:151]\n"
      "s_add_u32 s36, s15, s14\n"
      "ds_read_b128 a[216:219], v232 offset:128\n"
      "v_cvt_pk_bf16_f32 v174, v28, v29\n"
      "v_mfma_f32_16x16x32_bf16 a[132:135], v[208:211], a[236:239], a[132:135]\n"
      "s_add_u32 s37, s36, 3072\n"
      "v_exp_f32 v31, v223\n"
      "v_mfma_f32_16x16x32_bf16 a[132:135], v[204:207], a[232:235], a[132:135]\n"
      "ds_read_b128 a[220:223], v233 offset:128\n"
      "v_or3_b32 v22, v22, v167, v174\n"
      "v_mfma_f32_16x16x32_bf16 a[180:183], v[212:215], a[232:235], a[180:183]\n"
      "v_cvt_pk_bf16_f32 v175, v30, v31\n"
      "v_fma_f32 v224, v224, s48, v20\n"
      "v_mfma_f32_16x16x32_bf16 v[104:107], v[192:195], a[244:247], v[104:107]\n"
      "ds_read_b128 a[224:227], v232 offset:256\n"
      "v_fma_f32 v225, v225, s48, v20\n"
      "v_mfma_f32_16x16x32_bf16 v[104:107], v[188:191], a[240:243], v[104:107]\n"
      "v_exp_f32 v32, v224\n"
      "v_mfma_f32_16x16x32_bf16 v[152:155], v[196:199], a[240:243], v[152:155]\n"
      "ds_read_b128 a[228:231], v233 offset:256\n"
      "v_fma_f32 v226, v226, s48, v20\n"
      "v_mfma_f32_16x16x32_bf16 v[152:155], v[200:203], a[244:247], v[152:155]\n"
      "v_exp_f32 v33, v225\n"
      "v_mfma_f32_16x16x32_bf16 a[136:139], v[208:211], a[244:247], a[136:139]\n"
      "v_exp_f32 v34, v226\n"
      "v_mfma_f32_16x16x32_bf16 a[136:139], v[204:207], a[240:243], a[136:139]\n"
      "v_fma_f32 v227, v227, s48, v20\n"
      "v_cvt_pk_bf16_f32 v182, v32, v33\n"
      "v_mfma_f32_16x16x32_bf16 a[184:187], v[212:215], a[240:243], a[184:187]\n"
      "v_exp_f32 v35, v227\n"
      "v_mfma_f32_16x16x32_bf16 v[108:111], v[192:195], a[252:255], v[108:111]\n"
      "s_cmp_eq_u32 s50, s86\n"
      "s_cbranch_scc1 L_hook_E\n"
      "L_hook_E_ret:\n"
      "v_or3_b32 v22, v22, v175, v182\n"
      "v_mfma_f32_16x16x32_bf16 v[108:111], v[188:191], a[248:251], v[108:111]\n"
      "v_cvt_pk_bf16_f32 v183, v34, v35\n"
      "v_bitop3_b32 v22, v22, v183, s49 bitop3:0xa8\n"
      "v_mfma_f32_16x16x32_bf16 v[156:159], v[196:199], a[248:251], v[156:159]\n"
      "v_cmp_ne_u32 vcc, 0, v22\n"
      "v_mfma_f32_16x16x32_bf16 v[156:159], v[200:203], a[252:255], v[156:159]\n"
      "s_sub_u32 s50, s50, 1\n"
      "v_mfma_f32_16x16x32_bf16 a[140:143], v[208:211], a[252:255], a[140:143]\n"
      "v_mfma_f32_16x16x32_bf16 a[140:143], v[204:207], a[248:251], a[140:143]\n"
      "s_cbranch_vccnz L_rare_Ef0_3\n"
      "L_rare_Ef0_3_ret:\n"
      "v_mfma_f32_16x16x32_bf16 a[188:191], v[212:215], a[248:251], a[188:191]\n"
      "s_waitcnt lgkmcnt(0)\n"
      "s_branch L_loop_E\n"
      "L_last_E:\n"
      "s_cmp_lg_u32 s51, 0\n"
      "s_cbranch_scc1 L_fin_E\n"
      "ds_read_b64_tr_b16 a[232:233], v8 offset:0\n"
      "ds_read_b64_tr_b16 a[234:235], v8 offset:6144\n"
      "ds_read_b64_tr_b16 a[236:237], v8 offset:12288\n"
      "ds_read_b64_tr_b16 a[238:239], v8 offset:18432\n"
      "ds_read_b64_tr_b16 a[240:241], v9 offset:0\n"
      "ds_read_b64_tr_b16 a[242:243], v9 offset:6144\n"
      "ds_read_b64_tr_b16 a[244:245], v9 offset:12288\n"
      "ds_read_b64_tr_b16 a[246:247], v9 offset:18432\n"
      "ds_read_b64_tr_b16 a[248:249], v10 offset:0\n"
      "ds_read_b64_tr_b16 a[250:251], v10 offset:6144\n"
      "ds_read_b64_tr_b16 a[252:253], v10 offset:12288\n"
      "ds_read_b64_tr_b16 a[254:255], v10 offset:18432\n"
      "s_mov_b32 s20, s90\n"
      "s_mov_b32 s24, s93\n"
      "s_mov_b32 s21, s91\n"
      "s_mov_b32 s25, s94\n"
      "s_mov_b32 s22, s92\n"
      "s_mov_b32 s26, s95\n"
      "s_mov_b32 s36, s84\n"
      "s_add_u32 s37, s84, 3072\n"
      "s_mov_b32 s38, s85\n"
      "s_add_u32 s39, s85, 3072\n"
      "s_add_u32 s46, s44, s41\n"
      "s_add_u32 s47, s45, s40\n"
      "s_sub_u32 s63, s46, s65\n"
      "s_sub_u32 s64, s47, s65\n"
      "s_add_u32 s82, s45, s41\n"
      "s_sub_u32 s83, s82, s65\n"
      "v_smfmac_f32_16x16x64_bf16 a[192:195], v[24:27], v[188:195], v16\n"
      "s_add_u32 m0, s82, 0\n"
      "v_add_u32 v216, s63, v6\n"
      "v_add_u32 v217, s63, v7\n"
      "v_smfmac_f32_16x16x64_bf16 a[196:199], v[24:27], v[196:203], v16\n"
      "s_add_u32 s43, s66, 0\n"
      "v_add_u32 v218, s64, v6\n"
      "v_add_u32 v219, s64, v7\n"
      "v_smfmac_f32_16x16x64_bf16 a[200:203], v[24:27], v[204:211], v16\n"
      "buffer_load_dwordx4 v1, s[28:31], s43 offen offset:0 nt lds\n"
      "v_mfma_f32_16x16x32_bf16 a[204:207], v[24:27], v[212:215], a[204:207]\n"
      "s_waitcnt lgkmcnt(8)\n"
      "buffer_load_dwordx4 v2, s[28:31], s43 offen offset:1024 nt lds\n"
      "v_mfma_f32_16x16x32_bf16 v[64:67], v[192:195], a[236:239], v[64:67]\n"
      "buffer_load_dwordx4 v3, s[28:31], s43 offen offset:2048 nt lds\n"
      "v_mfma_f32_16x16x32_bf16 v[64:67], v[188:191], a[232:235], v[64:67]\n"
      "s_add_u32 m0, s82, 3072\n"
      "v_add_u32 v220, s89, v6\n"
      "v_add_u32 v221, s89, v7\n"
      "v_mfma_f32_16x16x32_bf16 v[112:115], v[196:199], a[232:235], v[112:115]\n"
      "s_add_u32 s43, s66, 3072\n"
      "v_add_u32 v222, s83, v6\n"
      "v_add_u32 v223, s83, v7\n"
      "v_mfma_f32_16x16x32_bf16 v[112:115], v[200:203], a[236:239], v[112:115]\n"
      "buffer_load_dwordx4 v1, s[28:31], s43 offen offset:0 nt lds\n"
      "v_mfma_f32_16x16x32_bf16 a[96:99], v[208:211], a[236:239], a[96:99]\n"
      "buffer_load_dwordx4 v2, s[28:31], s43 offen offset:1024 nt lds\n"
      "v_mfma_f32_16x16x32_bf16 a[96:99], v[204:207], a[232:235], a[96:99]\n"
      "buffer_load_dwordx4 v3, s[28:31], s43 offen offset:2048 nt lds\n"
      "v_mfma_f32_16x16x32_bf16 a[144:147], v[212:215], a[232:235], a[144:147]\n"
      "s_waitcnt lgkmcnt(4)\n"
      "ds_read_b64_tr_b16 a[232:233], v11 offset:0\n"
      "v_accvgpr_read_b32 v238, a192\n"
      "v_mfma_f32_16x16x32_bf16 v[68:71], v[192:195], a[244:247], v[68:71]\n"
      "ds_read_b64_tr_b16 a[234:235], v11 offset:6144\n"
      "ds_read_b64_tr_b16 a[236:237], v11 offset:12288\n"
      "v_mfma_f32_16x16x32_bf16 v[68:71], v[188:191], a[240:243], v[68:71]\n"
      "ds_read_b64_tr_b16 a[238:239], v11 offset:18432\n"
      "v_accvgpr_read_b32 v242, a196\n"
      "v_mfma_f32_16x16x32_bf16 v[116:119], v[196:199], a[240:243], v[116:119]\n"
      "v_accvgpr_read_b32 v246, a200\n"
      "v_accvgpr_read_b32 v239, a193\n"
      "v_mfma_f32_16x16x32_bf16 v[116:119], v[200:203], a[244:247], v[116:119]\n"
      "v_accvgpr_read_b32 v243, a197\n"
      "v_accvgpr_read_b32 v247, a201\n"
      "v_mfma_f32_16x16x32_bf16 a[100:103], v[208:211], a[244:247], a[100:103]\n"
      "s_lshl_b32 s63, s61, 9\n"
      "v_mfma_f32_16x16x32_bf16 a[100:103], v[204:207], a[240:243], a[100:103]\n"
      "s_add_u32 s63, s63, s65\n"
      "v_mfma_f32_16x16x32_bf16 a[148:151], v[212:215], a[240:243], a[148:151]\n"
      "s_waitcnt lgkmcnt(4)\n"
      "ds_read_b64_tr_b16 a[240:241], v8 offset:128\n"
      "ds_read_b64_tr_b16 a[242:243], v8 offset:6272\n"
      "v_mfma_f32_16x16x32_bf16 v[72:75], v[192:195], a[252:255], v[72:75]\n"
      "s_add_u32 s63, s63, 161792\n"
      "ds_read_b64_tr_b16 a[244:245], v8 offset:12416\n"
      "ds_read_b64_tr_b16 a[246:247], v8 offset:18560\n"
      "v_mfma_f32_16x16x32_bf16 v[72:75], v[188:191], a[248:251], v[72:75]\n"
      "v_and_b32 v236, 15, v0\n"
      "v_lshl_add_u32 v236, v236, 2, s63\n"
      "v_mfma_f32_16x16x32_bf16 v[120:123], v[196:199], a[248:251], v[120:123]\n"
      "s_waitcnt vmcnt(6)\n"
      "v_and_b32 v237, 48, v0\n"
      "v_add_u32 v237, s63, v237\n"
      "v_mfma_f32_16x16x32_bf16 v[120:123], v[200:203], a[252:255], v[120:123]\n"
      "ds_read_b128 a[24:27], v216 offset:0\n"
      "v_add_f32 v238, v238, v239\n"
      "v_mfma_f32_16x16x32_bf16 a[104:107], v[208:211], a[252:255], a[104:107]\n"
      "ds_read_b128 a[28:31], v217 offset:0\n"
      "v_add_f32 v242, v242, v243\n"
      "v_mfma_f32_16x16x32_bf16 a[104:107], v[204:207], a[248:251], a[104:107]\n"
      "ds_read_b128 a[32:35], v216 offset:128\n"
      "v_add_f32 v246, v246, v247\n"
      "v_mfma_f32_16x16x32_bf16 a[152:155], v[212:215], a[248:251], a[152:155]\n"
      "s_waitcnt lgkmcnt(7)\n"
      "ds_read_b64_tr_b16 a[248:249], v9 offset:128\n"
      "ds_read_b64_tr_b16 a[250:251], v9 offset:6272\n"
      "v_mfma_f32_16x16x32_bf16 v[76:79], v[192:195], a[236:239], v[76:79]\n"
      "s_add_u32 s82, s45, s41\n"
      "ds_read_b64_tr_b16 a[252:253], v9 offset:12416\n"
      "ds_read_b64_tr_b16 a[254:255], v9 offset:18560\n"
      "v_mfma_f32_16x16x32_bf16 v[76:79], v[188:191], a[232:235], v[76:79]\n"
      "v_rcp_f32 v238, v238\n"
      "v_mfma_f32_16x16x32_bf16 v[124:127], v[196:199], a[232:235], v[124:127]\n"
      "ds_read_b128 a[36:39], v217 offset:128\n"
      "v_mfma_f32_16x16x32_bf16 v[124:127], v[200:203], a[236:239], v[124:127]\n"
      "ds_read_b128 a[40:43], v216 offset:256\n"
      "v_mfma_f32_16x16x32_bf16 a[108:111], v[208:211], a[236:239], a[108:111]\n"
      "ds_read_b128 a[44:47], v217 offset:256\n"
      "v_mfma_f32_16x16x32_bf16 a[108:111], v[204:207], a[232:235], a[108:111]\n"
      "ds_read_b128 a[48:51], v218 offset:0\n"
      "v_mfma_f32_16x16x32_bf16 a[156:159], v[212:215], a[232:235], a[156:159]\n"
      "s_waitcnt lgkmcnt(11)\n"
      "ds_read_b128 a[52:55], v219 offset:0\n"
      "ds_read_b64_tr_b16 a[232:233], v10 offset:128\n"
      "v_mfma_f32_16x16x32_bf16 v[80:83], v[192:195], a[244:247], v[80:83]\n"
      "ds_read_b64_tr_b16 a[234:235], v10 offset:6272\n"
      "ds_read_b64_tr_b16 a[236:237], v10 offset:12416\n"
      "v_mfma_f32_16x16x32_bf16 v[80:83], v[188:191], a[240:243], v[80:83]\n"
      "ds_read_b64_tr_b16 a[238:239], v10 offset:18560\n"
      "ds_read_b128 a[56:59], v218 offset:128\n"
      "v_mfma_f32_16x16x32_bf16 v[128:131], v[196:199], a[240:243], v[128:131]\n"
      "ds_read_b128 a[60:63], v219 offset:128\n"
      "v_mfma_f32_16x16x32_bf16 v[128:131], v[200:203], a[244:247], v[128:131]\n"
      "ds_read_b128 a[64:67], v218 offset:256\n"
      "v_mfma_f32_16x16x32_bf16 a[112:115], v[208:211], a[244:247], a[112:115]\n"
      "ds_read_b128 a[68:71], v219 offset:256\n"
      "v_mfma_f32_16x16x32_bf16 a[112:115], v[204:207], a[240:243], a[112:115]\n"
      "ds_read_b128 a[72:75], v220 offset:0\n"
      "v_mfma_f32_16x16x32_bf16 a[160:163], v[212:215], a[240:243], a[160:163]\n"
      "s_waitcnt lgkmcnt(14)\n"
      "ds_read_b128 a[76:79], v221 offset:0\n"
      "ds_read_b64_tr_b16 a[240:241], v11 offset:128\n"
      "v_mfma_f32_16x16x32_bf16 v[84:87], v[192:195], a[252:255], v[84:87]\n"
      "ds_read_b128 a[80:83], v220 offset:128\n"
      "ds_read_b64_tr_b16 a[242:243], v11 offset:6272\n"
      "v_mfma_f32_16x16x32_bf16 v[84:87], v[188:191], a[248:251], v[84:87]\n"
      "ds_read_b64_tr_b16 a[244:245], v11 offset:12416\n"
      "ds_read_b64_tr_b16 a[246:247], v11 offset:18560\n"
      "ds_read_b128 a[84:87], v221 offset:128\n"
      "v_mfma_f32_16x16x32_bf16 v[132:135], v[196:199], a[248:251], v[132:135]\n"
      "ds_read_b128 a[88:91], v220 offset:256\n"
      "v_mfma_f32_16x16x32_bf16 v[132:135], v[200:203], a[252:255], v[132:135]\n"
      "ds_read_b128 a[92:95], v221 offset:256\n"
      "v_mfma_f32_16x16x32_bf16 a[116:119], v[208:211], a[252:255], a[116:119]\n"
      "v_rcp_f32 v242, v242\n"
      "v_mfma_f32_16x16x32_bf16 a[116:119], v[204:207], a[248:251], a[116:119]\n"
      "v_rcp_f32 v246, v246\n"
      "v_mfma_f32_16x16x32_bf16 a[164:167], v[212:215], a[248:251], a[164:167]\n"
      "s_waitcnt lgkmcnt(14)\n"
      "ds_read_b64_tr_b16 a[248:249], v8 offset:256\n"
      "v_add_u32 v232, s82, v251\n"
      "v_mfma_f32_16x16x32_bf16 v[88:91], v[192:195], a[236:239], v[88:91]\n"
      "s_waitcnt lgkmcnt(1)\n"
      "ds_read_b64_tr_b16 a[250:251], v8 offset:6400\n"
      "ds_read_b64_tr_b16 a[252:253], v8 offset:12544\n"
      "v_mfma_f32_16x16x32_bf16 v[88:91], v[188:191], a[232:235], v[88:91]\n"
      "s_mov_b32 m0, s46\n"
      "ds_read_b64_tr_b16 a[254:255], v8 offset:18688\n"
      "ds_write_b32 v236, v238 offset:0\n"
      "v_mfma_f32_16x16x32_bf16 v[136:139], v[196:199], a[232:235], v[136:139]\n"
      "buffer_load_dwordx4 v1, s[20:23], s36 offen offset:0 lds\n"
      "v_mfma_f32_16x16x32_bf16 v[136:139], v[200:203], a[236:239], v[136:139]\n"
      "ds_write_b32 v236, v242 offset:64\n"
      "ds_write_b32 v236, v246 offset:128\n"
      "v_mfma_f32_16x16x32_bf16 a[120:123], v[208:211], a[236:239], a[120:123]\n"
      "buffer_load_dwordx4 v2, s[20:23], s36 offen offset:1024 lds\n"
      "v_mfma_f32_16x16x32_bf16 a[120:123], v[204:207], a[232:235], a[120:123]\n"
      "ds_read_b128 v[238:241], v237 offset:0\n"
      "ds_read_b128 v[242:245], v237 offset:64\n"
      "v_mfma_f32_16x16x32_bf16 a[168:171], v[212:215], a[232:235], a[168:171]\n"
      "s_waitcnt lgkmcnt(9)\n"
      "buffer_load_dwordx4 v3, s[20:23], s36 offen offset:2048 lds\n"
      "v_mfma_f32_16x16x32_bf16 v[92:95], v[192:195], a[244:247], v[92:95]\n"
      "s_add_u32 m0, s46, 3072\n"
      "ds_read_b64_tr_b16 a[232:233], v9 offset:256\n"
      "ds_read_b64_tr_b16 a[234:235], v9 offset:6400\n"
      "v_mfma_f32_16x16x32_bf16 v[92:95], v[188:191], a[240:243], v[92:95]\n"
      "ds_read_b64_tr_b16 a[236:237], v9 offset:12544\n"
      "ds_read_b64_tr_b16 a[238:239], v9 offset:18688\n"
      "v_mfma_f32_16x16x32_bf16 v[140:143], v[196:199], a[240:243], v[140:143]\n"
      "buffer_load_dwordx4 v1, s[20:23], s37 offen offset:0 lds\n"
      "v_mfma_f32_16x16x32_bf16 v[140:143], v[200:203], a[244:247], v[140:143]\n"
      "ds_read_b128 v[246:249], v237 offset:128\n"
      "v_add_u32 v234, s82, v17\n"
      "v_mfma_f32_16x16x32_bf16 a[124:127], v[208:211], a[244:247], a[124:127]\n"
      "buffer_load_dwordx4 v2, s[20:23], s37 offen offset:1024 lds\n"
      "v_mfma_f32_16x16x32_bf16 a[124:127], v[204:207], a[240:243], a[124:127]\n"
      "v_xor_b32 v233, 32, v232\n"
      "v_xor_b32 v235, 32, v234\n"
      "v_mfma_f32_16x16x32_bf16 a[172:175], v[212:215], a[240:243], a[172:175]\n"
      "s_waitcnt lgkmcnt(10)\n"
      "buffer_load_dwordx4 v3, s[20:23], s37 offen offset:2048 lds\n"
      "v_mfma_f32_16x16x32_bf16 v[96:99], v[192:195], a[252:255], v[96:99]\n"
      "s_mov_b32 m0, s47\n"
      "ds_read_b64_tr_b16 a[240:241], v10 offset:256\n"
      "ds_read_b64_tr_b16 a[242:243], v10 offset:6400\n"
      "v_mfma_f32_16x16x32_bf16 v[96:99], v[188:191], a[248:251], v[96:99]\n"
      "ds_read_b64_tr_b16 a[244:245], v10 offset:12544\n"
      "ds_read_b64_tr_b16 a[246:247], v10 offset:18688\n"
      "v_mfma_f32_16x16x32_bf16 v[144:147], v[196:199], a[248:251], v[144:147]\n"
      "buffer_load_dwordx4 v1, s[24:27], s38 offen offset:0 lds\n"
      "v_mfma_f32_16x16x32_bf16 v[144:147], v[200:203], a[252:255], v[144:147]\n"
      "v_lshlrev_b32 v23, 4, v0\n"
      "v_mov_b32 v28, 0\n"
      "v_mfma_f32_16x16x32_bf16 a[128:131], v[208:211], a[252:255], a[128:131]\n"
      "buffer_load_dwordx4 v2, s[24:27], s38 offen offset:1024 lds\n"
      "v_mfma_f32_16x16x32_bf16 a[128:131], v[204:207], a[248:251], a[128:131]\n"
      "v_mov_b32 v29, 0\n"
      "v_mov_b32 v30, 0\n"
      "v_mfma_f32_16x16x32_bf16 a[176:179], v[212:215], a[248:251], a[176:179]\n"
      "s_waitcnt lgkmcnt(5)\n"
      "buffer_load_dwordx4 v3, s[24:27], s38 offen offset:2048 lds\n"
      "v_mfma_f32_16x16x32_bf16 v[100:103], v[192:195], a[236:239], v[100:103]\n"
      "s_add_u32 m0, s47, 3072\n"
      "ds_read_b64_tr_b16 a[248:249], v11 offset:256\n"
      "ds_read_b64_tr_b16 a[250:251], v11 offset:6400\n"
      "v_mfma_f32_16x16x32_bf16 v[100:103], v[188:191], a[232:235], v[100:103]\n"
      "ds_read_b64_tr_b16 a[252:253], v11 offset:12544\n"
      "ds_read_b64_tr_b16 a[254:255], v11 offset:18688\n"
      "v_mfma_f32_16x16x32_bf16 v[148:151], v[196:199], a[232:235], v[148:151]\n"
      "buffer_load_dwordx4 v1, s[24:27], s39 offen offset:0 lds\n"
      "v_mfma_f32_16x16x32_bf16 v[148:151], v[200:203], a[236:239], v[148:151]\n"
      "v_mov_b32 v31, 0\n"
      "v_mfma_f32_16x16x32_bf16 a[132:135], v[208:211], a[236:239], a[132:135]\n"
      "s_add_u32 s63, s44, s42\n"
      "buffer_load_dwordx4 v2, s[24:27], s39 offen offset:1024 lds\n"
      "v_mfma_f32_16x16x32_bf16 a[132:135], v[204:207], a[232:235], a[132:135]\n"
      "s_add_u32 s64, s63, 6144\n"
      "v_add_u32 v52, s64, v23\n"
      "v_add_u32 v23, s63, v23\n"
      "v_mfma_f32_16x16x32_bf16 a[180:183], v[212:215], a[232:235], a[180:183]\n"
      "s_waitcnt lgkmcnt(4)\n"
      "buffer_load_dwordx4 v3, s[24:27], s39 offen offset:2048 lds\n"
      "v_mfma_f32_16x16x32_bf16 v[104:107], v[192:195], a[244:247], v[104:107]\n"
      "s_mov_b32 s38, s36\n"
      "v_mfma_f32_16x16x32_bf16 v[104:107], v[188:191], a[240:243], v[104:107]\n"
      "s_mov_b32 s39, s37\n"
      "v_mfma_f32_16x16x32_bf16 v[152:155], v[196:199], a[240:243], v[152:155]\n"
      "s_add_u32 s15, s13, s12\n"
      "v_mfma_f32_16x16x32_bf16 v[152:155], v[200:203], a[244:247], v[152:155]\n"
      "s_and_b32 s15, s15, 15\n"
      "v_mfma_f32_16x16x32_bf16 a[136:139], v[208:211], a[244:247], a[136:139]\n"
      "s_mul_i32 s15, s15, 24576\n"
      "v_mfma_f32_16x16x32_bf16 a[136:139], v[204:207], a[240:243], a[136:139]\n"
      "s_lshr_b32 s16, s12, 4\n"
      "v_mfma_f32_16x16x32_bf16 a[184:187], v[212:215], a[240:243], a[184:187]\n"
      "s_waitcnt lgkmcnt(0)\n"
      "s_waitcnt vmcnt(12)\n"
      "v_mfma_f32_16x16x32_bf16 v[108:111], v[192:195], a[252:255], v[108:111]\n"
      "s_mul_i32 s16, s16, 393216\n"
      "ds_read_b128 a[0:3], v222 offset:0\n"
      "v_mfma_f32_16x16x32_bf16 v[108:111], v[188:191], a[248:251], v[108:111]\n"
      "s_add_u32 s15, s15, s16\n"
      "ds_read_b128 a[4:7], v223 offset:0\n"
      "v_mfma_f32_16x16x32_bf16 v[156:159], v[196:199], a[248:251], v[156:159]\n"
      "s_add_u32 s36, s15, s14\n"
      "ds_read_b128 a[8:11], v222 offset:128\n"
      "v_mfma_f32_16x16x32_bf16 v[156:159], v[200:203], a[252:255], v[156:159]\n"
      "s_add_u32 s37, s36, 3072\n"
      "ds_read_b128 a[12:15], v223 offset:128\n"
      "v_mfma_f32_16x16x32_bf16 a[140:143], v[208:211], a[252:255], a[140:143]\n"
      "s_lshl_b32 s63, s61, 9\n"
      "s_add_u32 s63, s63, s65\n"
      "ds_read_b128 a[16:19], v222 offset:256\n"
      "v_mfma_f32_16x16x32_bf16 a[140:143], v[204:207], a[248:251], a[140:143]\n"
      "s_add_u32 s63, s63, 147456\n"
      "ds_read_b128 a[20:23], v223 offset:256\n"
      "v_lshlrev_b32 v53, 3, v0\n"
      "v_mfma_f32_16x16x32_bf16 a[188:191], v[212:215], a[248:251], a[188:191]\n"
      "v_add_u32 v53, s63, v53\n"
      "v_mov_b32 v54, v21\n"
      "v_accvgpr_read_b32 v55, a204\n"
      "v_xor_b32 v58, 512, v53\n"
      "s_waitcnt lgkmcnt(6)\n"
      "v_pk_mul_f32 v[64:65], v[64:65], v[238:239]\n"
      "v_pk_mul_f32 v[66:67], v[66:67], v[240:241]\n"
      "v_pk_mul_f32 v[68:69], v[68:69], v[238:239]\n"
      "v_pk_mul_f32 v[70:71], v[70:71], v[240:241]\n"
      "v_pk_mul_f32 v[72:73], v[72:73], v[238:239]\n"
      "v_pk_mul_f32 v[74:75], v[74:75], v[240:241]\n"
      "v_pk_mul_f32 v[76:77], v[76:77], v[238:239]\n"
      "v_pk_mul_f32 v[78:79], v[78:79], v[240:241]\n"
      "ds_write_b64 v53, v[54:55] offset:0\n"
      "ds_write_b128 v52, a[168:171] offset:0\n"
      "v_cvt_pk_bf16_f32 v176, v64, v68\n"
      "v_cvt_pk_bf16_f32 v177, v72, v76\n"
      "v_cvt_pk_bf16_f32 v178, v65, v69\n"
      "v_cvt_pk_bf16_f32 v179, v73, v77\n"
      "v_cvt_pk_bf16_f32 v180, v66, v70\n"
      "v_cvt_pk_bf16_f32 v181, v74, v78\n"
      "v_cvt_pk_bf16_f32 v182, v67, v71\n"
      "v_cvt_pk_bf16_f32 v183, v75, v79\n"
      "ds_write_b128 v52, a[172:175] offset:1024\n"
      "v_pk_mul_f32 v[80:81], v[80:81], v[238:239]\n"
      "v_pk_mul_f32 v[82:83], v[82:83], v[240:241]\n"
      "v_pk_mul_f32 v[84:85], v[84:85], v[238:239]\n"
      "ds_write_b128 v52, a[176:179] offset:2048\n"
      "ds_write_b128 v52, a[180:183] offset:3072\n"
      "v_pk_mul_f32 v[86:87], v[86:87], v[240:241]\n"
      "v_pk_mul_f32 v[88:89], v[88:89], v[238:239]\n"
      "v_pk_mul_f32 v[90:91], v[90:91], v[240:241]\n"
      "ds_write_b64 v232, v[176:177] offset:0\n"
      "ds_write_b64 v232, v[178:179] offset:128\n"
      "v_pk_mul_f32 v[92:93], v[92:93], v[238:239]\n"
      "v_pk_mul_f32 v[94:95], v[94:95], v[240:241]\n"
      "v_mfma_f32_32x32x16_bf16 v[64:79], v[28:31], v[28:31], 0\n"
      "v_cvt_pk_bf16_f32 v184, v80, v84\n"
      "ds_write_b64 v233, v[180:181] offset:256\n"
      "ds_write_b64 v233, v[182:183] offset:384\n"
      "v_cvt_pk_bf16_f32 v185, v88, v92\n"
      "v_cvt_pk_bf16_f32 v186, v81, v85\n"
      "v_cvt_pk_bf16_f32 v187, v89, v93\n"
      "ds_read_b64_tr_b16 v[192:193], v234 offset:0\n"
      "ds_read_b64_tr_b16 v[194:195], v235 offset:0\n"
      "v_cvt_pk_bf16_f32 v188, v82, v86\n"
      "v_cvt_pk_bf16_f32 v189, v90, v94\n"
      "v_cvt_pk_bf16_f32 v190, v83, v87\n"
      "v_cvt_pk_bf16_f32 v191, v91, v95\n"
      "ds_read_b64_tr_b16 v[196:197], v234 offset:1024\n"
      "ds_read_b64_tr_b16 v[198:199], v235 offset:1024\n"
      "v_pk_mul_f32 v[96:97], v[96:97], v[238:239]\n"
      "v_pk_mul_f32 v[98:99], v[98:99], v[240:241]\n"
      "v_pk_mul_f32 v[100:101], v[100:101], v[238:239]\n"
      "v_pk_mul_f32 v[102:103], v[102:103], v[240:241]\n"
      "ds_write_b128 v52, a[184:187] offset:4096\n"
      "ds_write_b128 v52, a[188:191] offset:5120\n"
      "v_pk_mul_f32 v[104:105], v[104:105], v[238:239]\n"
      "v_pk_mul_f32 v[106:107], v[106:107], v[240:241]\n"
      "v_pk_mul_f32 v[108:109], v[108:109], v[238:239]\n"
      "v_pk_mul_f32 v[110:111], v[110:111], v[240:241]\n"
      "ds_write_b64 v232, v[184:185] offset:2048\n"
      "ds_write_b64 v232, v[186:187] offset:2176\n"
      "v_mfma_f32_32x32x16_bf16 v[80:95], v[28:31], v[28:31], 0\n"
      "v_cvt_pk_bf16_f32 v176, v96, v100\n"
      "s_waitcnt lgkmcnt(4)\n"
      "v_cvt_pk_bf16_f32 v177, v104, v108\n"
      "ds_write_b64 v233, v[188:189] offset:2304\n"
      "ds_write_b64 v233, v[190:191] offset:2432\n"
      "buffer_store_dwordx4 v[192:195], v250, s[72:75], s52 offen offset:0\n"
      "v_cvt_pk_bf16_f32 v178, v97, v101\n"
      "v_cvt_pk_bf16_f32 v179, v105, v109\n"
      "v_cvt_pk_bf16_f32 v180, v98, v102\n"
      "ds_read_b64_tr_b16 v[200:201], v234 offset:2048\n"
      "ds_read_b64_tr_b16 v[202:203], v235 offset:2048\n"
      "buffer_store_dwordx4 v[196:199], v250, s[72:75], s52 offen offset:3072\n"
      "v_cvt_pk_bf16_f32 v181, v106, v110\n"
      "v_cvt_pk_bf16_f32 v182, v99, v103\n"
      "v_cvt_pk_bf16_f32 v183, v107, v111\n"
      "ds_read_b64_tr_b16 v[204:205], v234 offset:3072\n"
      "ds_read_b64_tr_b16 v[206:207], v235 offset:3072\n"
      "v_pk_mul_f32 v[112:113], v[112:113], v[242:243]\n"
      "v_pk_mul_f32 v[114:115], v[114:115], v[244:245]\n"
      "v_pk_mul_f32 v[116:117], v[116:117], v[242:243]\n"
      "v_pk_mul_f32 v[118:119], v[118:119], v[244:245]\n"
      "v_pk_mul_f32 v[120:121], v[120:121], v[242:243]\n"
      "ds_write_b64 v232, v[176:177] offset:4096\n"
      "ds_write_b64 v232, v[178:179] offset:4224\n"
      "v_pk_mul_f32 v[122:123], v[122:123], v[244:245]\n"
      "v_pk_mul_f32 v[124:125], v[124:125], v[242:243]\n"
      "v_pk_mul_f32 v[126:127], v[126:127], v[244:245]\n"
      "v_mfma_f32_32x32x16_bf16 v[96:111], v[28:31], v[28:31], 0\n"
      "v_cvt_pk_bf16_f32 v184, v112, v116\n"
      "ds_write_b64 v233, v[180:181] offset:4352\n"
      "ds_write_b64 v233, v[182:183] offset:4480\n"
      "s_waitcnt lgkmcnt(4)\n"
      "v_cvt_pk_bf16_f32 v185, v120, v124\n"
      "buffer_store_dwordx4 v[200:203], v250, s[72:75], s52 offen offset:128\n"
      "v_cvt_pk_bf16_f32 v186, v113, v117\n"
      "v_cvt_pk_bf16_f32 v187, v121, v125\n"
      "ds_read_b64_tr_b16 v[208:209], v234 offset:4096\n"
      "ds_read_b64_tr_b16 v[210:211], v235 offset:4096\n"
      "v_cvt_pk_bf16_f32 v188, v114, v118\n"
      "buffer_store_dwordx4 v[204:207], v250, s[72:75], s52 offen offset:3200\n"
      "v_cvt_pk_bf16_f32 v189, v122, v126\n"
      "v_cvt_pk_bf16_f32 v190, v115, v119\n"
      "v_cvt_pk_bf16_f32 v191, v123, v127\n"
      "ds_read_b64_tr_b16 v[212:213], v234 offset:5120\n"
      "ds_read_b64_tr_b16 v[214:215], v235 offset:5120\n"
      "v_pk_mul_f32 v[128:129], v[128:129], v[242:243]\n"
      "v_pk_mul_f32 v[130:131], v[130:131], v[244:245]\n"
      "v_pk_mul_f32 v[132:133], v[132:133], v[242:243]\n"
      "v_pk_mul_f32 v[134:135], v[134:135], v[244:245]\n"
      "v_pk_mul_f32 v[136:137], v[136:137], v[242:243]\n"
      "ds_write_b64 v232, v[184:185] offset:0\n"
      "ds_write_b64 v232, v[186:187] offset:128\n"
      "v_pk_mul_f32 v[138:139], v[138:139], v[244:245]\n"
      "v_pk_mul_f32 v[140:141], v[140:141], v[242:243]\n"
      "v_pk_mul_f32 v[142:143], v[142:143], v[244:245]\n"
      "v_mfma_f32_32x32x16_bf16 v[112:127], v[28:31], v[28:31], 0\n"
      "v_cvt_pk_bf16_f32 v176, v128, v132\n"
      "ds_write_b64 v233, v[188:189] offset:256\n"
      "ds_write_b64 v233, v[190:191] offset:384\n"
      "s_waitcnt lgkmcnt(4)\n"
      "v_cvt_pk_bf16_f32 v177, v136, v140\n"
      "buffer_store_dwordx4 v[208:211], v250, s[72:75], s52 offen offset:256\n"
      "v_cvt_pk_bf16_f32 v178, v129, v133\n"
      "v_cvt_pk_bf16_f32 v179, v137, v141\n"
      "ds_read_b64_tr_b16 v[192:193], v234 offset:0\n"
      "ds_read_b64_tr_b16 v[194:195], v235 offset:0\n"
      "v_cvt_pk_bf16_f32 v180, v130, v134\n"
      "buffer_store_dwordx4 v[212:215], v250, s[72:75], s52 offen offset:3328\n"
      "v_cvt_pk_bf16_f32 v181, v138, v142\n"
      "v_cvt_pk_bf16_f32 v182, v131, v135\n"
      "v_cvt_pk_bf16_f32 v183, v139, v143\n"
      "ds_read_b64_tr_b16 v[196:197], v234 offset:1024\n"
      "ds_read_b64_tr_b16 v[198:199], v235 offset:1024\n"
      "s_waitcnt lgkmcnt(8)\n"
      "s_barrier\n"
      "v_pk_mul_f32 v[144:145], v[144:145], v[242:243]\n"
      "v_pk_mul_f32 v[146:147], v[146:147], v[244:245]\n"
      "v_pk_mul_f32 v[148:149], v[148:149], v[242:243]\n"
      "ds_write_b64 v232, v[176:177] offset:2048\n"
      "ds_write_b64 v232, v[178:179] offset:2176\n"
      "v_pk_mul_f32 v[150:151], v[150:151], v[244:245]\n"
      "v_pk_mul_f32 v[152:153], v[152:153], v[242:243]\n"
      "v_pk_mul_f32 v[154:155], v[154:155], v[244:245]\n"
      "ds_write_b64 v233, v[180:181] offset:2304\n"
      "ds_write_b64 v233, v[182:183] offset:2432\n"
      "v_pk_mul_f32 v[156:157], v[156:157], v[242:243]\n"
      "v_pk_mul_f32 v[158:159], v[158:159], v[244:245]\n"
      "v_mfma_f32_32x32x16_bf16 v[128:143], v[28:31], v[28:31], 0\n"
      "v_cvt_pk_bf16_f32 v184, v144, v148\n"
      "ds_read_b64_tr_b16 v[200:201], v234 offset:2048\n"
      "ds_read_b64_tr_b16 v[202:203], v235 offset:2048\n"
      "s_waitcnt lgkmcnt(6)\n"
      "v_cvt_pk_bf16_f32 v185, v152, v156\n"
      "buffer_store_dwordx4 v[192:195], v250, s[72:75], s53 offen offset:0\n"
      "ds_read_b64_tr_b16 v[204:205], v234 offset:3072\n"
      "ds_read_b64_tr_b16 v[206:207], v235 offset:3072\n"
      "v_cvt_pk_bf16_f32 v186, v145, v149\n"
      "v_cvt_pk_bf16_f32 v187, v153, v157\n"
      "v_cvt_pk_bf16_f32 v188, v146, v150\n"
      "ds_read_b64 v[56:57], v58 offset:0\n"
      "ds_read_b128 v[36:39], v23 offset:0\n"
      "buffer_store_dwordx4 v[196:199], v250, s[72:75], s53 offen offset:3072\n"
      "v_cvt_pk_bf16_f32 v189, v154, v158\n"
      "v_cvt_pk_bf16_f32 v190, v147, v151\n"
      "v_cvt_pk_bf16_f32 v191, v155, v159\n"
      "ds_read_b128 v[40:43], v23 offset:1024\n"
      "ds_read_b128 v[44:47], v23 offset:2048\n"
      "v_mfma_f32_32x32x16_bf16 v[144:159], v[28:31], v[28:31], 0\n"
      "v_accvgpr_read_b32 v160, a96\n"
      "v_accvgpr_read_b32 v161, a97\n"
      "v_accvgpr_read_b32 v162, a98\n"
      "v_accvgpr_read_b32 v163, a99\n"
      "v_accvgpr_read_b32 v164, a100\n"
      "ds_write_b64 v232, v[184:185] offset:4096\n"
      "ds_write_b64 v232, v[186:187] offset:4224\n"
      "v_accvgpr_read_b32 v165, a101\n"
      "v_accvgpr_read_b32 v166, a102\n"
      "v_accvgpr_read_b32 v167, a103\n"
      "v_accvgpr_read_b32 v168, a104\n"
      "v_accvgpr_read_b32 v169, a105\n"
      "v_accvgpr_read_b32 v170, a106\n"
      "ds_write_b64 v233, v[188:189] offset:4352\n"
      "ds_write_b64 v233, v[190:191] offset:4480\n"
      "v_accvgpr_read_b32 v171, a107\n"
      "v_accvgpr_read_b32 v172, a108\n"
      "v_accvgpr_read_b32 v173, a109\n"
      "v_accvgpr_read_b32 v174, a110\n"
      "v_accvgpr_read_b32 v175, a111\n"
      "v_pk_mul_f32 v[160:161], v[160:161], v[246:247]\n"
      "ds_read_b64_tr_b16 v[208:209], v234 offset:4096\n"
      "ds_read_b64_tr_b16 v[210:211], v235 offset:4096\n"
      "v_pk_mul_f32 v[162:163], v[162:163], v[248:249]\n"
      "v_pk_mul_f32 v[164:165], v[164:165], v[246:247]\n"
      "v_pk_mul_f32 v[166:167], v[166:167], v[248:249]\n"
      "v_pk_mul_f32 v[168:169], v[168:169], v[246:247]\n"
      "v_pk_mul_f32 v[170:171], v[170:171], v[248:249]\n"
      "v_pk_mul_f32 v[172:173], v[172:173], v[246:247]\n"
      "ds_read_b64_tr_b16 v[212:213], v234 offset:5120\n"
      "ds_read_b64_tr_b16 v[214:215], v235 offset:5120\n"
      "v_pk_mul_f32 v[174:175], v[174:175], v[248:249]\n"
      "v_cvt_pk_bf16_f32 v176, v160, v164\n"
      "s_waitcnt lgkmcnt(12)\n"
      "v_cvt_pk_bf16_f32 v177, v168, v172\n"
      "buffer_store_dwordx4 v[200:203], v250, s[72:75], s53 offen offset:128\n"
      "v_cvt_pk_bf16_f32 v178, v161, v165\n"
      "ds_read_b128 v[48:51], v23 offset:3072\n"
      "ds_read_b128 v[216:219], v23 offset:4096\n"
      "v_cvt_pk_bf16_f32 v179, v169, v173\n"
      "v_cvt_pk_bf16_f32 v180, v162, v166\n"
      "buffer_store_dwordx4 v[204:207], v250, s[72:75], s53 offen offset:3200\n"
      "v_cvt_pk_bf16_f32 v181, v170, v174\n"
      "v_cvt_pk_bf16_f32 v182, v163, v167\n"
      "v_cvt_pk_bf16_f32 v183, v171, v175\n"
      "ds_read_b128 v[220:223], v23 offset:5120\n"
      "v_mfma_f32_32x32x16_bf16 a[96:111], v[28:31], v[28:31], 0\n"
      "v_accvgpr_read_b32 v160, a112\n"
      "v_accvgpr_read_b32 v161, a113\n"
      "v_accvgpr_read_b32 v162, a114\n"
      "v_accvgpr_read_b32 v163, a115\n"
      "v_accvgpr_read_b32 v164, a116\n"
      "v_accvgpr_read_b32 v165, a117\n"
      "v_accvgpr_read_b32 v166, a118\n"
      "v_accvgpr_read_b32 v167, a119\n"
      "ds_write_b64 v232, v[176:177] offset:0\n"
      "ds_write_b64 v232, v[178:179] offset:128\n"
      "v_accvgpr_read_b32 v168, a120\n"
      "v_accvgpr_read_b32 v169, a121\n"
      "v_accvgpr_read_b32 v170, a122\n"
      "v_accvgpr_read_b32 v171, a123\n"
      "v_accvgpr_read_b32 v172, a124\n"
      "v_accvgpr_read_b32 v173, a125\n"
      "v_accvgpr_read_b32 v174, a126\n"
      "v_accvgpr_read_b32 v175, a127\n"
      "v_pk_mul_f32 v[160:161], v[160:161], v[246:247]\n"
      "ds_write_b64 v233, v[180:181] offset:256\n"
      "ds_write_b64 v233, v[182:183] offset:384\n"
      "v_pk_mul_f32 v[162:163], v[162:163], v[248:249]\n"
      "v_pk_mul_f32 v[164:165], v[164:165], v[246:247]\n"
      "v_pk_mul_f32 v[166:167], v[166:167], v[248:249]\n"
      "v_pk_mul_f32 v[168:169], v[168:169], v[246:247]\n"
      "v_pk_mul_f32 v[170:171], v[170:171], v[248:249]\n"
      "v_pk_mul_f32 v[172:173], v[172:173], v[246:247]\n"
      "v_pk_mul_f32 v[174:175], v[174:175], v[248:249]\n"
      "v_cvt_pk_bf16_f32 v184, v160, v164\n"
      "s_waitcnt lgkmcnt(7)\n"
      "ds_read_b64_tr_b16 v[192:193], v234 offset:0\n"
      "ds_read_b64_tr_b16 v[194:195], v235 offset:0\n"
      "v_cvt_pk_bf16_f32 v185, v168, v172\n"
      "buffer_store_dwordx4 v[208:211], v250, s[72:75], s53 offen offset:256\n"
      "v_cvt_pk_bf16_f32 v186, v161, v165\n"
      "v_cvt_pk_bf16_f32 v187, v169, v173\n"
      "v_cvt_pk_bf16_f32 v188, v162, v166\n"
      "buffer_store_dwordx4 v[212:215], v250, s[72:75], s53 offen offset:3328\n"
      "v_cvt_pk_bf16_f32 v189, v170, v174\n"
      "v_cvt_pk_bf16_f32 v190, v163, v167\n"
      "v_cvt_pk_bf16_f32 v191, v171, v175\n"
      "ds_read_b64_tr_b16 v[196:197], v234 offset:1024\n"
      "ds_read_b64_tr_b16 v[198:199], v235 offset:1024\n"
      "s_waitcnt lgkmcnt(11)\n"
      "v_min_f32 v59, v54, v56\n"
      "v_sub_f32 v52, v59, v54\n"
      "v_sub_f32 v53, v59, v56\n"
      "v_exp_f32 v52, v52\n"
      "v_exp_f32 v53, v53\n"
      "v_mul_f32 v55, v55, v52\n"
      "v_fma_f32 v55, v57, v53, v55\n"
      "v_rcp_f32 v55, v55\n"
      "s_nop 0\n"
      "v_mul_f32 v52, v52, v55\n"
      "v_mul_f32 v53, v53, v55\n"
      "v_mfma_f32_32x32x16_bf16 a[112:127], v[28:31], v[28:31], 0\n"
      "v_accvgpr_read_b32 v160, a128\n"
      "v_accvgpr_read_b32 v161, a129\n"
      "v_accvgpr_read_b32 v162, a130\n"
      "v_accvgpr_read_b32 v163, a131\n"
      "v_accvgpr_read_b32 v164, a132\n"
      "ds_write_b32 v236, v52 offset:192\n"
      "ds_write_b32 v236, v53 offset:256\n"
      "v_accvgpr_read_b32 v165, a133\n"
      "v_accvgpr_read_b32 v166, a134\n"
      "v_accvgpr_read_b32 v167, a135\n"
      "v_accvgpr_read_b32 v168, a136\n"
      "v_accvgpr_read_b32 v169, a137\n"
      "v_accvgpr_read_b32 v170, a138\n"
      "ds_read_b128 v[56:59], v237 offset:192\n"
      "ds_read_b128 v[60:63], v237 offset:256\n"
      "v_accvgpr_read_b32 v171, a139\n"
      "v_accvgpr_read_b32 v172, a140\n"
      "v_accvgpr_read_b32 v173, a141\n"
      "v_accvgpr_read_b32 v174, a142\n"
      "v_accvgpr_read_b32 v175, a143\n"
      "v_pk_mul_f32 v[160:161], v[160:161], v[246:247]\n"
      "ds_write_b64 v232, v[184:185] offset:2048\n"
      "ds_write_b64 v232, v[186:187] offset:2176\n"
      "v_pk_mul_f32 v[162:163], v[162:163], v[248:249]\n"
      "v_pk_mul_f32 v[164:165], v[164:165], v[246:247]\n"
      "v_pk_mul_f32 v[166:167], v[166:167], v[248:249]\n"
      "v_pk_mul_f32 v[168:169], v[168:169], v[246:247]\n"
      "v_pk_mul_f32 v[170:171], v[170:171], v[248:249]\n"
      "v_pk_mul_f32 v[172:173], v[172:173], v[246:247]\n"
      "ds_write_b64 v233, v[188:189] offset:2304\n"
      "ds_write_b64 v233, v[190:191] offset:2432\n"
      "v_pk_mul_f32 v[174:175], v[174:175], v[248:249]\n"
      "v_cvt_pk_bf16_f32 v176, v160, v164\n"
      "s_waitcnt lgkmcnt(8)\n"
      "v_cvt_pk_bf16_f32 v177, v168, v172\n"
      "buffer_store_dwordx4 v[192:195], v250, s[72:75], s54 offen offset:0\n"
      "v_cvt_pk_bf16_f32 v178, v161, v165\n"
      "ds_read_b64_tr_b16 v[200:201], v234 offset:2048\n"
      "ds_read_b64_tr_b16 v[202:203], v235 offset:2048\n"
      "v_cvt_pk_bf16_f32 v179, v169, v173\n"
      "v_cvt_pk_bf16_f32 v180, v162, v166\n"
      "buffer_store_dwordx4 v[196:199], v250, s[72:75], s54 offen offset:3072\n"
      "v_cvt_pk_bf16_f32 v181, v170, v174\n"
      "v_cvt_pk_bf16_f32 v182, v163, v167\n"
      "v_cvt_pk_bf16_f32 v183, v171, v175\n"
      "ds_read_b64_tr_b16 v[204:205], v234 offset:3072\n"
      "ds_read_b64_tr_b16 v[206:207], v235 offset:3072\n"
      "v_mfma_f32_32x32x16_bf16 a[128:143], v[28:31], v[28:31], 0\n"
      "v_mfma_f32_32x32x16_bf16 a[192:207], v[28:31], v[28:31], 0\n"
      "s_waitcnt lgkmcnt(8)\n"
      "v_accvgpr_read_b32 v160, a144\n"
      "v_accvgpr_read_b32 v161, a145\n"
      "v_accvgpr_read_b32 v162, a146\n"
      "v_accvgpr_read_b32 v163, a147\n"
      "v_accvgpr_read_b32 v164, a148\n"
      "v_accvgpr_read_b32 v165, a149\n"
      "v_accvgpr_read_b32 v166, a150\n"
      "v_accvgpr_read_b32 v167, a151\n"
      "ds_write_b64 v232, v[176:177] offset:4096\n"
      "ds_write_b64 v232, v[178:179] offset:4224\n"
      "v_accvgpr_read_b32 v168, a152\n"
      "v_accvgpr_read_b32 v169, a153\n"
      "v_accvgpr_read_b32 v170, a154\n"
      "v_accvgpr_read_b32 v171, a155\n"
      "v_accvgpr_read_b32 v172, a156\n"
      "v_accvgpr_read_b32 v173, a157\n"
      "v_accvgpr_read_b32 v174, a158\n"
      "v_accvgpr_read_b32 v175, a159\n"
      "v_pk_mul_f32 v[160:161], v[160:161], v[56:57]\n"
      "v_pk_mul_f32 v[162:163], v[162:163], v[58:59]\n"
      "v_pk_mul_f32 v[164:165], v[164:165], v[56:57]\n"
      "v_pk_mul_f32 v[166:167], v[166:167], v[58:59]\n"
      "ds_write_b64 v233, v[180:181] offset:4352\n"
      "ds_write_b64 v233, v[182:183] offset:4480\n"
      "v_pk_mul_f32 v[168:169], v[168:169], v[56:57]\n"
      "v_pk_mul_f32 v[170:171], v[170:171], v[58:59]\n"
      "v_pk_mul_f32 v[172:173], v[172:173], v[56:57]\n"
      "v_pk_mul_f32 v[174:175], v[174:175], v[58:59]\n"
      "v_pk_fma_f32 v[160:161], v[36:37], v[60:61], v[160:161]\n"
      "v_pk_fma_f32 v[162:163], v[38:39], v[62:63], v[162:163]\n"
      "v_pk_fma_f32 v[164:165], v[40:41], v[60:61], v[164:165]\n"
      "v_pk_fma_f32 v[166:167], v[42:43], v[62:63], v[166:167]\n"
      "v_pk_fma_f32 v[168:169], v[44:45], v[60:61], v[168:169]\n"
      "v_pk_fma_f32 v[170:171], v[46:47], v[62:63], v[170:171]\n"
      "v_pk_fma_f32 v[172:173], v[48:49], v[60:61], v[172:173]\n"
      "ds_read_b64_tr_b16 v[208:209], v234 offset:4096\n"
      "ds_read_b64_tr_b16 v[210:211], v235 offset:4096\n"
      "v_pk_fma_f32 v[174:175], v[50:51], v[62:63], v[174:175]\n"
      "v_cvt_pk_bf16_f32 v184, v160, v164\n"
      "s_waitcnt lgkmcnt(6)\n"
      "v_cvt_pk_bf16_f32 v185, v168, v172\n"
      "buffer_store_dwordx4 v[200:203], v250, s[72:75], s54 offen offset:128\n"
      "v_cvt_pk_bf16_f32 v186, v161, v165\n"
      "v_cvt_pk_bf16_f32 v187, v169, v173\n"
      "v_cvt_pk_bf16_f32 v188, v162, v166\n"
      "buffer_store_dwordx4 v[204:207], v250, s[72:75], s54 offen offset:3200\n"
      "v_cvt_pk_bf16_f32 v189, v170, v174\n"
      "v_cvt_pk_bf16_f32 v190, v163, v167\n"
      "v_cvt_pk_bf16_f32 v191, v171, v175\n"
      "ds_read_b64_tr_b16 v[212:213], v234 offset:5120\n"
      "ds_read_b64_tr_b16 v[214:215], v235 offset:5120\n"
      "v_accvgpr_read_b32 v160, a160\n"
      "v_accvgpr_read_b32 v161, a161\n"
      "v_accvgpr_read_b32 v162, a162\n"
      "v_accvgpr_read_b32 v163, a163\n"
      "v_accvgpr_read_b32 v164, a164\n"
      "v_accvgpr_read_b32 v165, a165\n"
      "v_accvgpr_read_b32 v166, a166\n"
      "ds_write_b64 v232, v[184:185] offset:0\n"
      "ds_write_b64 v232, v[186:187] offset:128\n"
      "v_accvgpr_read_b32 v167, a167\n"
      "v_and_b32 v54, 2, v0\n"
      "v_cmp_eq_u32 vcc, 0, v54\n"
      "v_mov_b32 v54, 0x7fff0000\n"
      "s_nop 0\n"
      "v_cndmask_b32 v54, v54, v250, vcc\n"
      "v_pk_mul_f32 v[160:161], v[160:161], v[56:57]\n"
      "v_pk_mul_f32 v[162:163], v[162:163], v[58:59]\n"
      "v_pk_mul_f32 v[164:165], v[164:165], v[56:57]\n"
      "ds_write_b64 v233, v[188:189] offset:256\n"
      "ds_write_b64 v233, v[190:191] offset:384\n"
      "v_pk_mul_f32 v[166:167], v[166:167], v[58:59]\n"
      "v_pk_fma_f32 v[160:161], v[216:217], v[60:61], v[160:161]\n"
      "v_pk_fma_f32 v[162:163], v[218:219], v[62:63], v[162:163]\n"
      "v_pk_fma_f32 v[164:165], v[220:221], v[60:61], v[164:165]\n"
      "v_pk_fma_f32 v[166:167], v[222:223], v[62:63], v[166:167]\n"
      "v_cvt_pk_bf16_f32 v176, v160, v164\n"
      "s_waitcnt lgkmcnt(4)\n"
      "v_cvt_pk_bf16_f32 v177, v160, v164\n"
      "ds_read_b64_tr_b16 v[192:193], v234 offset:0\n"
      "ds_read_b64_tr_b16 v[194:195], v235 offset:0\n"
      "buffer_store_dwordx4 v[208:211], v250, s[72:75], s54 offen offset:256\n"
      "v_cvt_pk_bf16_f32 v178, v161, v165\n"
      "v_cvt_pk_bf16_f32 v179, v161, v165\n"
      "v_cvt_pk_bf16_f32 v180, v162, v166\n"
      "buffer_store_dwordx4 v[212:215], v250, s[72:75], s54 offen offset:3328\n"
      "v_cvt_pk_bf16_f32 v181, v162, v166\n"
      "v_cvt_pk_bf16_f32 v182, v163, v167\n"
      "v_cvt_pk_bf16_f32 v183, v163, v167\n"
      "ds_read_b64_tr_b16 v[196:197], v234 offset:1024\n"
      "ds_read_b64_tr_b16 v[198:199], v235 offset:1024\n"
      "ds_write_b64 v232, v[176:177] offset:2048\n"
      "ds_write_b64 v232, v[178:179] offset:2176\n"
      "ds_write_b64 v233, v[180:181] offset:2304\n"
      "ds_write_b64 v233, v[182:183] offset:2432\n"
      "s_waitcnt lgkmcnt(4)\n"
      "buffer_store_dwordx4 v[192:195], v250, s[72:75], s55 offen offset:0\n"
      "buffer_store_dwordx4 v[196:199], v250, s[72:75], s55 offen offset:3072\n"
      "v_mfma_f32_32x32x16_bf16 a[144:159], v[28:31], v[28:31], 0\n"
      "v_mfma_f32_32x32x16_bf16 a[160:175], v[28:31], v[28:31], 0\n"
      "v_mfma_f32_32x32x16_bf16 a[176:191], v[28:31], v[28:31], 0\n"
      "ds_read_b64_tr_b16 v[200:201], v234 offset:2048\n"
      "ds_read_b64_tr_b16 v[202:203], v235 offset:2048\n"
      "ds_read_b64_tr_b16 v[204:205], v234 offset:3072\n"
      "ds_read_b64_tr_b16 v[206:207], v235 offset:3072\n"
      "s_waitcnt lgkmcnt(0)\n"
      "buffer_store_dwordx4 v[200:203], v54, s[72:75], s55 offen offset:128\n"
      "buffer_store_dwordx4 v[204:207], v54, s[72:75], s55 offen offset:3200\n"
      "s_mov_b32 s51, 1\n"
      "s_mov_b32 s43, s52\n"
      "s_mov_b32 s52, s66\n"
      "s_mov_b32 s66, s43\n"
      "s_mov_b32 s43, s53\n"
      "s_mov_b32 s53, s67\n"
      "s_mov_b32 s67, s43\n"
      "s_mov_b32 s43, s54\n"
      "s_mov_b32 s54, s68\n"
      "s_mov_b32 s68, s43\n"
      "s_mov_b32 s43, s55\n"
      "s_mov_b32 s55, s69\n"
      "s_mov_b32 s69, s43\n"
      "s_mov_b32 s86, -1\n"
      "s_mov_b32 s50, 7\n"
      "s_mov_b32 s35, 0\n"
      "v_mfma_f32_16x16x32_bf16 v[36:39], a[208:211], a[0:3], 0\n"
      "s_add_u32 s46, s44, s42\n"
      "ds_read_b128 a[232:235], v4 offset:6144\n"
      "v_add_u32 v232, s41, v6\n"
      "v_mfma_f32_16x16x32_bf16 v[36:39], a[212:215], a[4:7], v[36:39]\n"
      "s_add_u32 s47, s45, s41\n"
      "ds_read_b128 a[236:239], v5 offset:6144\n"
      "v_add_u32 v233, s41, v7\n"
      "v_mfma_f32_16x16x32_bf16 v[36:39], a[216:219], a[8:11], v[36:39]\n"
      "ds_read_b128 a[240:243], v4 offset:6272\n"
      "ds_read_b128 a[244:247], v5 offset:6272\n"
      "v_mfma_f32_16x16x32_bf16 v[36:39], a[220:223], a[12:15], v[36:39]\n"
      "s_mov_b32 m0, s46\n"
      "ds_read_b128 a[248:251], v4 offset:6400\n"
      "ds_read_b128 a[252:255], v5 offset:6400\n"
      "v_mfma_f32_16x16x32_bf16 v[36:39], a[224:227], a[16:19], v[36:39]\n"
      "buffer_load_dwordx4 v1, s[20:23], s36 offen offset:0 lds\n"
      "v_mfma_f32_16x16x32_bf16 v[36:39], a[228:231], a[20:23], v[36:39]\n"
      "buffer_load_dwordx4 v2, s[20:23], s36 offen offset:1024 lds\n"
      "v_mfma_f32_16x16x32_bf16 v[40:43], a[228:231], a[44:47], 0\n"
      "buffer_load_dwordx4 v3, s[20:23], s36 offen offset:2048 lds\n"
      "v_mfma_f32_16x16x32_bf16 v[40:43], a[224:227], a[40:43], v[40:43]\n"
      "s_nop 0\n"
      "v_add_u32 v234, s40, v12\n"
      "v_add_u32 v235, s40, v13\n"
      "v_mfma_f32_16x16x32_bf16 v[40:43], a[220:223], a[36:39], v[40:43]\n"
      "v_max3_f32 v52, v36, v37, v38\n"
      "v_max_f32 v52, v52, v39\n"
      "v_mfma_f32_16x16x32_bf16 v[40:43], a[216:219], a[32:35], v[40:43]\n"
      "v_mov_b32 v53, v52\n"
      "s_nop 1\n"
      "v_permlane32_swap_b32 v52, v53\n"
      "v_mfma_f32_16x16x32_bf16 v[40:43], a[212:215], a[28:31], v[40:43]\n"
      "v_max_f32 v52, v52, v53\n"
      "v_mov_b32 v53, v52\n"
      "v_mfma_f32_16x16x32_bf16 v[40:43], a[208:211], a[24:27], v[40:43]\n"
      "s_nop 0\n"
      "v_permlane16_swap_b32 v52, v53\n"
      "v_max_f32 v52, v52, v53\n"
      "v_mfma_f32_16x16x32_bf16 v[44:47], a[208:211], a[48:51], 0\n"
      "v_mul_f32 v52, s62, v52\n"
      "v_add_f32 v18, 0xc1800000, v52\n"
      "v_mfma_f32_16x16x32_bf16 v[44:47], a[212:215], a[52:55], v[44:47]\n"
      "v_fma_f32 v36, v36, s48, v18\n"
      "v_fma_f32 v37, v37, s48, v18\n"
      "v_mfma_f32_16x16x32_bf16 v[44:47], a[216:219], a[56:59], v[44:47]\n"
      "v_exp_f32 v28, v36\n"
      "v_mfma_f32_16x16x32_bf16 v[44:47], a[220:223], a[60:63], v[44:47]\n"
      "v_exp_f32 v29, v37\n"
      "v_mfma_f32_16x16x32_bf16 v[44:47], a[224:227], a[64:67], v[44:47]\n"
      "v_fma_f32 v38, v38, s48, v18\n"
      "v_fma_f32 v39, v39, s48, v18\n"
      "v_mfma_f32_16x16x32_bf16 v[44:47], a[228:231], a[68:71], v[44:47]\n"
      "v_exp_f32 v30, v38\n"
      "v_mfma_f32_16x16x32_bf16 v[48:51], a[228:231], a[92:95], 0\n"
      "v_exp_f32 v31, v39\n"
      "v_mfma_f32_16x16x32_bf16 v[48:51], a[224:227], a[88:91], v[48:51]\n"
      "v_cvt_pk_bf16_f32 v160, v28, v29\n"
      "v_cvt_pk_bf16_f32 v161, v30, v31\n"
      "v_mfma_f32_16x16x32_bf16 v[48:51], a[220:223], a[84:87], v[48:51]\n"
      "v_max3_f32 v54, v40, v41, v42\n"
      "v_max_f32 v54, v54, v43\n"
      "v_mfma_f32_16x16x32_bf16 v[48:51], a[216:219], a[80:83], v[48:51]\n"
      "v_mov_b32 v55, v54\n"
      "s_nop 1\n"
      "v_permlane32_swap_b32 v54, v55\n"
      "v_mfma_f32_16x16x32_bf16 v[48:51], a[212:215], a[76:79], v[48:51]\n"
      "v_max_f32 v54, v54, v55\n"
      "v_mov_b32 v55, v54\n"
      "v_mfma_f32_16x16x32_bf16 v[48:51], a[208:211], a[72:75], v[48:51]\n"
      "s_waitcnt lgkmcnt(0)\n"
      "v_permlane16_swap_b32 v54, v55\n"
      "v_max_f32 v54, v54, v55\n"
      "v_mfma_f32_16x16x32_bf16 v[216:219], a[232:235], a[0:3], 0\n"
      "ds_read_b128 a[208:211], v4 offset:12288\n"
      "v_mul_f32 v54, s62, v54\n"
      "v_mfma_f32_16x16x32_bf16 v[216:219], a[236:239], a[4:7], v[216:219]\n"
      "ds_read_b128 a[212:215], v5 offset:12288\n"
      "v_add_f32 v19, 0xc1800000, v54\n"
      "v_mfma_f32_16x16x32_bf16 v[216:219], a[240:243], a[8:11], v[216:219]\n"
      "ds_read_b128 a[216:219], v4 offset:12416\n"
      "ds_read_b128 a[220:223], v5 offset:12416\n"
      "v_mfma_f32_16x16x32_bf16 v[216:219], a[244:247], a[12:15], v[216:219]\n"
      "s_add_u32 m0, s46, 3072\n"
      "ds_read_b128 a[224:227], v4 offset:12544\n"
      "ds_read_b128 a[228:231], v5 offset:12544\n"
      "v_mfma_f32_16x16x32_bf16 v[216:219], a[248:251], a[16:19], v[216:219]\n"
      "buffer_load_dwordx4 v1, s[20:23], s37 offen offset:0 lds\n"
      "v_mfma_f32_16x16x32_bf16 v[216:219], a[252:255], a[20:23], v[216:219]\n"
      "buffer_load_dwordx4 v2, s[20:23], s37 offen offset:1024 lds\n"
      "v_mfma_f32_16x16x32_bf16 v[220:223], a[252:255], a[44:47], 0\n"
      "buffer_load_dwordx4 v3, s[20:23], s37 offen offset:2048 lds\n"
      "v_mfma_f32_16x16x32_bf16 v[220:223], a[248:251], a[40:43], v[220:223]\n"
      "v_fma_f32 v40, v40, s48, v19\n"
      "v_fma_f32 v41, v41, s48, v19\n"
      "v_mfma_f32_16x16x32_bf16 v[220:223], a[244:247], a[36:39], v[220:223]\n"
      "v_exp_f32 v32, v40\n"
      "v_mfma_f32_16x16x32_bf16 v[220:223], a[240:243], a[32:35], v[220:223]\n"
      "v_exp_f32 v33, v41\n"
      "v_mfma_f32_16x16x32_bf16 v[220:223], a[236:239], a[28:31], v[220:223]\n"
      "v_fma_f32 v42, v42, s48, v19\n"
      "v_fma_f32 v43, v43, s48, v19\n"
      "v_mfma_f32_16x16x32_bf16 v[220:223], a[232:235], a[24:27], v[220:223]\n"
      "v_exp_f32 v34, v42\n"
      "v_mfma_f32_16x16x32_bf16 v[224:227], a[232:235], a[48:51], 0\n"
      "v_exp_f32 v35, v43\n"
      "v_mfma_f32_16x16x32_bf16 v[224:227], a[236:239], a[52:55], v[224:227]\n"
      "v_cvt_pk_bf16_f32 v168, v32, v33\n"
      "v_or3_b32 v22, v160, v161, v168\n"
      "v_mfma_f32_16x16x32_bf16 v[224:227], a[240:243], a[56:59], v[224:227]\n"
      "v_cvt_pk_bf16_f32 v169, v34, v35\n"
      "v_max3_f32 v56, v44, v45, v46\n"
      "v_mfma_f32_16x16x32_bf16 v[224:227], a[244:247], a[60:63], v[224:227]\n"
      "v_max_f32 v56, v56, v47\n"
      "v_mov_b32 v57, v56\n"
      "v_mfma_f32_16x16x32_bf16 v[224:227], a[248:251], a[64:67], v[224:227]\n"
      "s_nop 0\n"
      "v_permlane32_swap_b32 v56, v57\n"
      "v_max_f32 v56, v56, v57\n"
      "v_mfma_f32_16x16x32_bf16 v[224:227], a[252:255], a[68:71], v[224:227]\n"
      "v_mov_b32 v57, v56\n"
      "s_nop 1\n"
      "v_permlane16_swap_b32 v56, v57\n"
      "v_mfma_f32_16x16x32_bf16 v[228:231], a[252:255], a[92:95], 0\n"
      "v_max_f32 v56, v56, v57\n"
      "v_mul_f32 v56, s62, v56\n"
      "v_mfma_f32_16x16x32_bf16 v[228:231], a[248:251], a[88:91], v[228:231]\n"
      "v_add_f32 v20, 0xc1800000, v56\n"
      "v_fma_f32 v44, v44, s48, v20\n"
      "v_mfma_f32_16x16x32_bf16 v[228:231], a[244:247], a[84:87], v[228:231]\n"
      "v_fma_f32 v45, v45, s48, v20\n"
      "v_fma_f32 v46, v46, s48, v20\n"
      "v_fma_f32 v47, v47, s48, v20\n"
      "v_exp_f32 v28, v44\n"
      "v_exp_f32 v29, v45\n"
      "v_exp_f32 v30, v46\n"
      "v_exp_f32 v31, v47\n"
      "v_cvt_pk_bf16_f32 v176, v28, v29\n"
      "v_or3_b32 v22, v22, v169, v176\n"
      "v_cvt_pk_bf16_f32 v177, v30, v31\n"
      "v_max3_f32 v58, v48, v49, v50\n"
      "v_max_f32 v58, v58, v51\n"
      "v_mov_b32 v59, v58\n"
      "s_nop 1\n"
      "v_permlane32_swap_b32 v58, v59\n"
      "v_max_f32 v58, v58, v59\n"
      "v_mov_b32 v59, v58\n"
      "s_nop 1\n"
      "v_permlane16_swap_b32 v58, v59\n"
      "v_max_f32 v58, v58, v59\n"
      "v_mul_f32 v58, s62, v58\n"
      "v_add_f32 v21, 0xc1800000, v58\n"
      "v_fma_f32 v48, v48, s48, v21\n"
      "v_fma_f32 v49, v49, s48, v21\n"
      "v_fma_f32 v50, v50, s48, v21\n"
      "v_fma_f32 v51, v51, s48, v21\n"
      "v_exp_f32 v32, v48\n"
      "v_exp_f32 v33, v49\n"
      "v_exp_f32 v34, v50\n"
      "v_exp_f32 v35, v51\n"
      "v_cvt_pk_bf16_f32 v184, v32, v33\n"
      "v_or3_b32 v22, v22, v177, v184\n"
      "v_cvt_pk_bf16_f32 v185, v34, v35\n"
      "v_bitop3_b32 v22, v22, v185, s49 bitop3:0xa8\n"
      "v_cmp_ne_u32 vcc, 0, v22\n"
      "v_mfma_f32_16x16x32_bf16 v[228:231], a[240:243], a[80:83], v[228:231]\n"
      "v_add_u32 v236, s40, v14\n"
      "v_add_u32 v237, s40, v15\n"
      "v_mfma_f32_16x16x32_bf16 v[228:231], a[236:239], a[76:79], v[228:231]\n"
      "s_mov_b32 s43, s40\n"
      "v_mfma_f32_16x16x32_bf16 v[228:231], a[232:235], a[72:75], v[228:231]\n"
      "s_waitcnt lgkmcnt(0)\n"
      "s_cbranch_vccnz L_rare_Ea_0\n"
      "L_rare_Ea_0_ret:\n"
      "v_fma_f32 v216, v216, s48, v18\n"
      "v_mfma_f32_16x16x32_bf16 v[36:39], a[208:211], a[0:3], 0\n"
      "s_mov_b32 s40, s41\n"
      "ds_read_b128 a[232:235], v4 offset:18432\n"
      "v_fma_f32 v217, v217, s48, v18\n"
      "v_mfma_f32_16x16x32_bf16 v[36:39], a[212:215], a[4:7], v[36:39]\n"
      "s_mov_b32 s41, s42\n"
      "ds_read_b128 a[236:239], v5 offset:18432\n"
      "v_fma_f32 v218, v218, s48, v18\n"
      "v_mfma_f32_16x16x32_bf16 v[36:39], a[216:219], a[8:11], v[36:39]\n"
      "s_mov_b32 s42, s43\n"
      "ds_read_b128 a[240:243], v4 offset:18560\n"
      "ds_read_b128 a[244:247], v5 offset:18560\n"
      "v_mfma_f32_16x16x32_bf16 v[36:39], a[220:223], a[12:15], v[36:39]\n"
      "s_mov_b32 m0, s47\n"
      "ds_read_b128 a[248:251], v4 offset:18688\n"
      "ds_read_b128 a[252:255], v5 offset:18688\n"
      "v_mfma_f32_16x16x32_bf16 v[36:39], a[224:227], a[16:19], v[36:39]\n"
      "buffer_load_dwordx4 v1, s[24:27], s38 offen offset:0 lds\n"
      "v_mfma_f32_16x16x32_bf16 v[36:39], a[228:231], a[20:23], v[36:39]\n"
      "buffer_load_dwordx4 v2, s[24:27], s38 offen offset:1024 lds\n"
      "v_mfma_f32_16x16x32_bf16 v[40:43], a[228:231], a[44:47], 0\n"
      "buffer_load_dwordx4 v3, s[24:27], s38 offen offset:2048 lds\n"
      "v_mfma_f32_16x16x32_bf16 v[40:43], a[224:227], a[40:43], v[40:43]\n"
      "v_exp_f32 v28, v216\n"
      "v_mfma_f32_16x16x32_bf16 v[40:43], a[220:223], a[36:39], v[40:43]\n"
      "v_exp_f32 v29, v217\n"
      "v_mfma_f32_16x16x32_bf16 v[40:43], a[216:219], a[32:35], v[40:43]\n"
      "v_exp_f32 v30, v218\n"
      "v_mfma_f32_16x16x32_bf16 v[40:43], a[212:215], a[28:31], v[40:43]\n"
      "v_fma_f32 v219, v219, s48, v18\n"
      "v_cvt_pk_bf16_f32 v162, v28, v29\n"
      "v_mfma_f32_16x16x32_bf16 v[40:43], a[208:211], a[24:27], v[40:43]\n"
      "v_exp_f32 v31, v219\n"
      "v_mfma_f32_16x16x32_bf16 v[44:47], a[208:211], a[48:51], 0\n"
      "v_cvt_pk_bf16_f32 v163, v30, v31\n"
      "v_fma_f32 v220, v220, s48, v19\n"
      "v_mfma_f32_16x16x32_bf16 v[44:47], a[212:215], a[52:55], v[44:47]\n"
      "v_fma_f32 v221, v221, s48, v19\n"
      "v_fma_f32 v222, v222, s48, v19\n"
      "v_mfma_f32_16x16x32_bf16 v[44:47], a[216:219], a[56:59], v[44:47]\n"
      "v_fma_f32 v223, v223, s48, v19\n"
      "v_exp_f32 v32, v220\n"
      "v_exp_f32 v33, v221\n"
      "v_exp_f32 v34, v222\n"
      "v_exp_f32 v35, v223\n"
      "v_cvt_pk_bf16_f32 v170, v32, v33\n"
      "v_or3_b32 v22, v162, v163, v170\n"
      "v_cvt_pk_bf16_f32 v171, v34, v35\n"
      "v_fma_f32 v224, v224, s48, v20\n"
      "v_fma_f32 v225, v225, s48, v20\n"
      "v_fma_f32 v226, v226, s48, v20\n"
      "v_fma_f32 v227, v227, s48, v20\n"
      "v_exp_f32 v28, v224\n"
      "v_exp_f32 v29, v225\n"
      "v_exp_f32 v30, v226\n"
      "v_exp_f32 v31, v227\n"
      "v_cvt_pk_bf16_f32 v178, v28, v29\n"
      "v_or3_b32 v22, v22, v171, v178\n"
      "v_cvt_pk_bf16_f32 v179, v30, v31\n"
      "v_fma_f32 v228, v228, s48, v21\n"
      "v_fma_f32 v229, v229, s48, v21\n"
      "v_fma_f32 v230, v230, s48, v21\n"
      "v_fma_f32 v231, v231, s48, v21\n"
      "v_exp_f32 v32, v228\n"
      "v_exp_f32 v33, v229\n"
      "v_exp_f32 v34, v230\n"
      "v_exp_f32 v35, v231\n"
      "v_cvt_pk_bf16_f32 v186, v32, v33\n"
      "v_or3_b32 v22, v22, v179, v186\n"
      "v_cvt_pk_bf16_f32 v187, v34, v35\n"
      "v_bitop3_b32 v22, v22, v187, s49 bitop3:0xa8\n"
      "v_cmp_ne_u32 vcc, 0, v22\n"
      "v_mfma_f32_16x16x32_bf16 v[44:47], a[220:223], a[60:63], v[44:47]\n"
      "v_mfma_f32_16x16x32_bf16 v[44:47], a[224:227], a[64:67], v[44:47]\n"
      "v_mfma_f32_16x16x32_bf16 v[44:47], a[228:231], a[68:71], v[44:47]\n"
      "s_waitcnt lgkmcnt(0)\n"
      "s_cbranch_vccnz L_rare_Ea_1\n"
      "L_rare_Ea_1_ret:\n"
      "v_fma_f32 v36, v36, s48, v18\n"
      "v_mfma_f32_16x16x32_bf16 v[216:219], a[232:235], a[0:3], 0\n"
      "v_exp_f32 v28, v36\n"
      "v_mfma_f32_16x16x32_bf16 v[216:219], a[236:239], a[4:7], v[216:219]\n"
      "v_fma_f32 v37, v37, s48, v18\n"
      "v_fma_f32 v38, v38, s48, v18\n"
      "v_mfma_f32_16x16x32_bf16 v[216:219], a[240:243], a[8:11], v[216:219]\n"
      "v_exp_f32 v29, v37\n"
      "v_mfma_f32_16x16x32_bf16 v[216:219], a[244:247], a[12:15], v[216:219]\n"
      "s_add_u32 m0, s47, 3072\n"
      "v_exp_f32 v30, v38\n"
      "v_mfma_f32_16x16x32_bf16 v[216:219], a[248:251], a[16:19], v[216:219]\n"
      "buffer_load_dwordx4 v1, s[24:27], s39 offen offset:0 lds\n"
      "v_mfma_f32_16x16x32_bf16 v[216:219], a[252:255], a[20:23], v[216:219]\n"
      "buffer_load_dwordx4 v2, s[24:27], s39 offen offset:1024 lds\n"
      "v_mfma_f32_16x16x32_bf16 v[220:223], a[252:255], a[44:47], 0\n"
      "buffer_load_dwordx4 v3, s[24:27], s39 offen offset:2048 lds\n"
      "v_mfma_f32_16x16x32_bf16 v[220:223], a[248:251], a[40:43], v[220:223]\n"
      "s_mov_b32 s38, s36\n"
      "v_fma_f32 v39, v39, s48, v18\n"
      "v_cvt_pk_bf16_f32 v164, v28, v29\n"
      "v_mfma_f32_16x16x32_bf16 v[220:223], a[244:247], a[36:39], v[220:223]\n"
      "s_mov_b32 s39, s37\n"
      "v_exp_f32 v31, v39\n"
      "v_mfma_f32_16x16x32_bf16 v[220:223], a[240:243], a[32:35], v[220:223]\n"
      "s_add_u32 s12, s12, 1\n"
      "v_cvt_pk_bf16_f32 v165, v30, v31\n"
      "v_fma_f32 v40, v40, s48, v19\n"
      "v_mfma_f32_16x16x32_bf16 v[220:223], a[236:239], a[28:31], v[220:223]\n"
      "s_waitcnt vmcnt(34)\n"
      "s_add_u32 s15, s13, s12\n"
      "s_and_b32 s15, s15, 15\n"
      "s_mul_i32 s15, s15, 24576\n"
      "s_lshr_b32 s16, s12, 4\n"
      "s_mul_i32 s16, s16, 393216\n"
      "s_add_u32 s15, s15, s16\n"
      "s_add_u32 s36, s15, s14\n"
      "s_add_u32 s37, s36, 3072\n"
      "v_mfma_f32_16x16x32_bf16 v[220:223], a[232:235], a[24:27], v[220:223]\n"
      "s_barrier\n"
      "ds_read_b128 a[208:211], v232 offset:0\n"
      "v_fma_f32 v41, v41, s48, v19\n"
      "v_mfma_f32_16x16x32_bf16 v[224:227], a[232:235], a[48:51], 0\n"
      "ds_read_b128 a[212:215], v233 offset:0\n"
      "v_fma_f32 v42, v42, s48, v19\n"
      "v_mfma_f32_16x16x32_bf16 v[224:227], a[236:239], a[52:55], v[224:227]\n"
      "ds_read_b128 a[216:219], v232 offset:128\n"
      "ds_read_b128 a[220:223], v233 offset:128\n"
      "v_mfma_f32_16x16x32_bf16 v[224:227], a[240:243], a[56:59], v[224:227]\n"
      "ds_read_b128 a[224:227], v232 offset:256\n"
      "ds_read_b128 a[228:231], v233 offset:256\n"
      "v_fma_f32 v43, v43, s48, v19\n"
      "v_exp_f32 v32, v40\n"
      "v_exp_f32 v33, v41\n"
      "v_exp_f32 v34, v42\n"
      "v_exp_f32 v35, v43\n"
      "v_cvt_pk_bf16_f32 v172, v32, v33\n"
      "v_or3_b32 v22, v164, v165, v172\n"
      "v_cvt_pk_bf16_f32 v173, v34, v35\n"
      "v_fma_f32 v44, v44, s48, v20\n"
      "v_fma_f32 v45, v45, s48, v20\n"
      "v_fma_f32 v46, v46, s48, v20\n"
      "v_fma_f32 v47, v47, s48, v20\n"
      "v_exp_f32 v28, v44\n"
      "v_exp_f32 v29, v45\n"
      "v_exp_f32 v30, v46\n"
      "v_exp_f32 v31, v47\n"
      "v_cvt_pk_bf16_f32 v180, v28, v29\n"
      "v_or3_b32 v22, v22, v173, v180\n"
      "v_cvt_pk_bf16_f32 v181, v30, v31\n"
      "v_bitop3_b32 v22, v22, v181, s49 bitop3:0xa8\n"
      "v_cmp_ne_u32 vcc, 0, v22\n"
      "v_mfma_f32_16x16x32_bf16 v[224:227], a[244:247], a[60:63], v[224:227]\n"
      "v_mfma_f32_16x16x32_bf16 v[224:227], a[248:251], a[64:67], v[224:227]\n"
      "v_mfma_f32_16x16x32_bf16 v[224:227], a[252:255], a[68:71], v[224:227]\n"
      "s_waitcnt lgkmcnt(0)\n"
      "s_cbranch_vccnz L_rare_Ea_2\n"
      "L_rare_Ea_2_ret:\n"
      "v_fma_f32 v216, v216, s48, v18\n"
      "v_fma_f32 v217, v217, s48, v18\n"
      "v_fma_f32 v218, v218, s48, v18\n"
      "v_fma_f32 v219, v219, s48, v18\n"
      "v_exp_f32 v32, v216\n"
      "v_exp_f32 v33, v217\n"
      "v_exp_f32 v34, v218\n"
      "v_exp_f32 v35, v219\n"
      "v_cvt_pk_bf16_f32 v166, v32, v33\n"
      "v_cvt_pk_bf16_f32 v167, v34, v35\n"
      "v_fma_f32 v220, v220, s48, v19\n"
      "v_fma_f32 v221, v221, s48, v19\n"
      "v_fma_f32 v222, v222, s48, v19\n"
      "v_fma_f32 v223, v223, s48, v19\n"
      "v_exp_f32 v28, v220\n"
      "v_exp_f32 v29, v221\n"
      "v_exp_f32 v30, v222\n"
      "v_exp_f32 v31, v223\n"
      "v_cvt_pk_bf16_f32 v174, v28, v29\n"
      "v_or3_b32 v22, v166, v167, v174\n"
      "v_cvt_pk_bf16_f32 v175, v30, v31\n"
      "v_fma_f32 v224, v224, s48, v20\n"
      "v_fma_f32 v225, v225, s48, v20\n"
      "v_fma_f32 v226, v226, s48, v20\n"
      "v_fma_f32 v227, v227, s48, v20\n"
      "v_exp_f32 v32, v224\n"
      "v_exp_f32 v33, v225\n"
      "v_exp_f32 v34, v226\n"
      "v_exp_f32 v35, v227\n"
      "v_cvt_pk_bf16_f32 v182, v32, v33\n"
      "v_or3_b32 v22, v22, v175, v182\n"
      "v_cvt_pk_bf16_f32 v183, v34, v35\n"
      "v_bitop3_b32 v22, v22, v183, s49 bitop3:0xa8\n"
      "v_cmp_ne_u32 vcc, 0, v22\n"
      "s_nop 1\n"
      "s_cbranch_vccnz L_rare_Ea_3\n"
      "L_rare_Ea_3_ret:\n"
      "s_branch L_loop_E\n"
      "L_fin_E:\n"
      "ds_read_b64_tr_b16 a[232:233], v8 offset:0\n"
      "ds_read_b64_tr_b16 a[234:235], v8 offset:6144\n"
      "ds_read_b64_tr_b16 a[236:237], v8 offset:12288\n"
      "ds_read_b64_tr_b16 a[238:239], v8 offset:18432\n"
      "ds_read_b64_tr_b16 a[240:241], v9 offset:0\n"
      "ds_read_b64_tr_b16 a[242:243], v9 offset:6144\n"
      "ds_read_b64_tr_b16 a[244:245], v9 offset:12288\n"
      "ds_read_b64_tr_b16 a[246:247], v9 offset:18432\n"
      "ds_read_b64_tr_b16 a[248:249], v10 offset:0\n"
      "ds_read_b64_tr_b16 a[250:251], v10 offset:6144\n"
      "ds_read_b64_tr_b16 a[252:253], v10 offset:12288\n"
      "ds_read_b64_tr_b16 a[254:255], v10 offset:18432\n"
      "v_smfmac_f32_16x16x64_bf16 a[192:195], v[24:27], v[188:195], v16\n"
      "v_smfmac_f32_16x16x64_bf16 a[196:199], v[24:27], v[196:203], v16\n"
      "v_smfmac_f32_16x16x64_bf16 a[200:203], v[24:27], v[204:211], v16\n"
      "v_mfma_f32_16x16x32_bf16 a[204:207], v[24:27], v[212:215], a[204:207]\n"
      "s_waitcnt lgkmcnt(8)\n"
      "v_mfma_f32_16x16x32_bf16 v[64:67], v[192:195], a[236:239], v[64:67]\n"
      "v_mfma_f32_16x16x32_bf16 v[64:67], v[188:191], a[232:235], v[64:67]\n"
      "v_mfma_f32_16x16x32_bf16 v[112:115], v[196:199], a[232:235], v[112:115]\n"
      "v_mfma_f32_16x16x32_bf16 v[112:115], v[200:203], a[236:239], v[112:115]\n"
      "v_accvgpr_read_b32 v238, a192\n"
      "v_accvgpr_read_b32 v242, a196\n"
      "v_mfma_f32_16x16x32_bf16 a[96:99], v[208:211], a[236:239], a[96:99]\n"
      "v_accvgpr_read_b32 v246, a200\n"
      "v_accvgpr_read_b32 v239, a193\n"
      "v_mfma_f32_16x16x32_bf16 a[96:99], v[204:207], a[232:235], a[96:99]\n"
      "v_accvgpr_read_b32 v243, a197\n"
      "v_accvgpr_read_b32 v247, a201\n"
      "v_mfma_f32_16x16x32_bf16 a[144:147], v[212:215], a[232:235], a[144:147]\n"
      "s_waitcnt lgkmcnt(4)\n"
      "ds_read_b64_tr_b16 a[232:233], v11 offset:0\n"
      "ds_read_b64_tr_b16 a[234:235], v11 offset:6144\n"
      "v_mfma_f32_16x16x32_bf16 v[68:71], v[192:195], a[244:247], v[68:71]\n"
      "s_lshl_b32 s63, s61, 9\n"
      "ds_read_b64_tr_b16 a[236:237], v11 offset:12288\n"
      "ds_read_b64_tr_b16 a[238:239], v11 offset:18432\n"
      "v_mfma_f32_16x16x32_bf16 v[68:71], v[188:191], a[240:243], v[68:71]\n"
      "s_add_u32 s63, s63, s65\n"
      "v_mfma_f32_16x16x32_bf16 v[116:119], v[196:199], a[240:243], v[116:119]\n"
      "s_add_u32 s63, s63, 161792\n"
      "v_and_b32 v236, 15, v0\n"
      "v_lshl_add_u32 v236, v236, 2, s63\n"
      "v_mfma_f32_16x16x32_bf16 v[116:119], v[200:203], a[244:247], v[116:119]\n"
      "v_and_b32 v237, 48, v0\n"
      "v_add_u32 v237, s63, v237\n"
      "v_mfma_f32_16x16x32_bf16 a[100:103], v[208:211], a[244:247], a[100:103]\n"
      "v_add_f32 v238, v238, v239\n"
      "v_add_f32 v242, v242, v243\n"
      "v_mfma_f32_16x16x32_bf16 a[100:103], v[204:207], a[240:243], a[100:103]\n"
      "v_add_f32 v246, v246, v247\n"
      "v_mfma_f32_16x16x32_bf16 a[148:151], v[212:215], a[240:243], a[148:151]\n"
      "s_waitcnt lgkmcnt(4)\n"
      "ds_read_b64_tr_b16 a[240:241], v8 offset:128\n"
      "ds_read_b64_tr_b16 a[242:243], v8 offset:6272\n"
      "v_mfma_f32_16x16x32_bf16 v[72:75], v[192:195], a[252:255], v[72:75]\n"
      "s_add_u32 s82, s45, s41\n"
      "ds_read_b64_tr_b16 a[244:245], v8 offset:12416\n"
      "ds_read_b64_tr_b16 a[246:247], v8 offset:18560\n"
      "v_mfma_f32_16x16x32_bf16 v[72:75], v[188:191], a[248:251], v[72:75]\n"
      "v_rcp_f32 v238, v238\n"
      "v_mfma_f32_16x16x32_bf16 v[120:123], v[196:199], a[248:251], v[120:123]\n"
      "v_rcp_f32 v242, v242\n"
      "v_mfma_f32_16x16x32_bf16 v[120:123], v[200:203], a[252:255], v[120:123]\n"
      "v_rcp_f32 v246, v246\n"
      "v_mfma_f32_16x16x32_bf16 a[104:107], v[208:211], a[252:255], a[104:107]\n"
      "v_add_u32 v232, s82, v251\n"
      "v_mfma_f32_16x16x32_bf16 a[104:107], v[204:207], a[248:251], a[104:107]\n"
      "ds_write_b32 v236, v238 offset:0\n"
      "ds_write_b32 v236, v242 offset:64\n"
      "v_mfma_f32_16x16x32_bf16 a[152:155], v[212:215], a[248:251], a[152:155]\n"
      "s_waitcnt lgkmcnt(6)\n"
      "ds_read_b64_tr_b16 a[248:249], v9 offset:128\n"
      "ds_read_b64_tr_b16 a[250:251], v9 offset:6272\n"
      "v_mfma_f32_16x16x32_bf16 v[76:79], v[192:195], a[236:239], v[76:79]\n"
      "ds_read_b64_tr_b16 a[252:253], v9 offset:12416\n"
      "ds_read_b64_tr_b16 a[254:255], v9 offset:18560\n"
      "v_mfma_f32_16x16x32_bf16 v[76:79], v[188:191], a[232:235], v[76:79]\n"
      "ds_write_b32 v236, v246 offset:128\n"
      "ds_read_b128 v[238:241], v237 offset:0\n"
      "v_mfma_f32_16x16x32_bf16 v[124:127], v[196:199], a[232:235], v[124:127]\n"
      "ds_read_b128 v[242:245], v237 offset:64\n"
      "ds_read_b128 v[246:249], v237 offset:128\n"
      "v_mfma_f32_16x16x32_bf16 v[124:127], v[200:203], a[236:239], v[124:127]\n"
      "v_add_u32 v234, s82, v17\n"
      "v_xor_b32 v233, 32, v232\n"
      "v_mfma_f32_16x16x32_bf16 a[108:111], v[208:211], a[236:239], a[108:111]\n"
      "v_xor_b32 v235, 32, v234\n"
      "v_lshlrev_b32 v23, 4, v0\n"
      "v_mfma_f32_16x16x32_bf16 a[108:111], v[204:207], a[232:235], a[108:111]\n"
      "s_waitcnt lgkmcnt(0)\n"
      "v_mul_f32 v64, v64, v238\n"
      "v_mul_f32 v65, v65, v239\n"
      "v_mfma_f32_16x16x32_bf16 a[156:159], v[212:215], a[232:235], a[156:159]\n"
      "s_waitcnt lgkmcnt(0)\n"
      "ds_read_b64_tr_b16 a[232:233], v10 offset:128\n"
      "v_mul_f32 v66, v66, v240\n"
      "v_mfma_f32_16x16x32_bf16 v[80:83], v[192:195], a[244:247], v[80:83]\n"
      "ds_read_b64_tr_b16 a[234:235], v10 offset:6272\n"
      "ds_read_b64_tr_b16 a[236:237], v10 offset:12416\n"
      "v_mfma_f32_16x16x32_bf16 v[80:83], v[188:191], a[240:243], v[80:83]\n"
      "ds_read_b64_tr_b16 a[238:239], v10 offset:18560\n"
      "v_mul_f32 v67, v67, v241\n"
      "v_mfma_f32_16x16x32_bf16 v[128:131], v[196:199], a[240:243], v[128:131]\n"
      "v_mul_f32 v68, v68, v238\n"
      "v_mul_f32 v69, v69, v239\n"
      "v_mfma_f32_16x16x32_bf16 v[128:131], v[200:203], a[244:247], v[128:131]\n"
      "v_mul_f32 v70, v70, v240\n"
      "v_mul_f32 v71, v71, v241\n"
      "v_mfma_f32_16x16x32_bf16 a[112:115], v[208:211], a[244:247], a[112:115]\n"
      "v_mul_f32 v72, v72, v238\n"
      "v_mul_f32 v73, v73, v239\n"
      "v_mfma_f32_16x16x32_bf16 a[112:115], v[204:207], a[240:243], a[112:115]\n"
      "v_mul_f32 v74, v74, v240\n"
      "v_mul_f32 v75, v75, v241\n"
      "v_mfma_f32_16x16x32_bf16 a[160:163], v[212:215], a[240:243], a[160:163]\n"
      "s_waitcnt lgkmcnt(4)\n"
      "ds_read_b64_tr_b16 a[240:241], v11 offset:128\n"
      "v_mul_f32 v76, v76, v238\n"
      "v_mfma_f32_16x16x32_bf16 v[84:87], v[192:195], a[252:255], v[84:87]\n"
      "ds_read_b64_tr_b16 a[242:243], v11 offset:6272\n"
      "ds_read_b64_tr_b16 a[244:245], v11 offset:12416\n"
      "v_mfma_f32_16x16x32_bf16 v[84:87], v[188:191], a[248:251], v[84:87]\n"
      "ds_read_b64_tr_b16 a[246:247], v11 offset:18560\n"
      "v_mul_f32 v77, v77, v239\n"
      "v_mfma_f32_16x16x32_bf16 v[132:135], v[196:199], a[248:251], v[132:135]\n"
      "v_mul_f32 v78, v78, v240\n"
      "v_mul_f32 v79, v79, v241\n"
      "v_mfma_f32_16x16x32_bf16 v[132:135], v[200:203], a[252:255], v[132:135]\n"
      "v_cvt_pk_bf16_f32 v176, v64, v68\n"
      "v_cvt_pk_bf16_f32 v177, v72, v76\n"
      "v_mfma_f32_16x16x32_bf16 a[116:119], v[208:211], a[252:255], a[116:119]\n"
      "v_cvt_pk_bf16_f32 v178, v65, v69\n"
      "v_cvt_pk_bf16_f32 v179, v73, v77\n"
      "v_mfma_f32_16x16x32_bf16 a[116:119], v[204:207], a[248:251], a[116:119]\n"
      "v_cvt_pk_bf16_f32 v180, v66, v70\n"
      "v_cvt_pk_bf16_f32 v181, v74, v78\n"
      "v_mfma_f32_16x16x32_bf16 a[164:167], v[212:215], a[248:251], a[164:167]\n"
      "s_waitcnt lgkmcnt(4)\n"
      "ds_read_b64_tr_b16 a[248:249], v8 offset:256\n"
      "v_cvt_pk_bf16_f32 v182, v67, v71\n"
      "v_mfma_f32_16x16x32_bf16 v[88:91], v[192:195], a[236:239], v[88:91]\n"
      "ds_read_b64_tr_b16 a[250:251], v8 offset:6400\n"
      "ds_read_b64_tr_b16 a[252:253], v8 offset:12544\n"
      "v_mfma_f32_16x16x32_bf16 v[88:91], v[188:191], a[232:235], v[88:91]\n"
      "ds_read_b64_tr_b16 a[254:255], v8 offset:18688\n"
      "v_cvt_pk_bf16_f32 v183, v75, v79\n"
      "v_mfma_f32_16x16x32_bf16 v[136:139], v[196:199], a[232:235], v[136:139]\n"
      "ds_write_b64 v232, v[176:177] offset:0\n"
      "ds_write_b64 v232, v[178:179] offset:128\n"
      "v_mfma_f32_16x16x32_bf16 v[136:139], v[200:203], a[236:239], v[136:139]\n"
      "ds_write_b64 v233, v[180:181] offset:256\n"
      "ds_write_b64 v233, v[182:183] offset:384\n"
      "v_mfma_f32_16x16x32_bf16 a[120:123], v[208:211], a[236:239], a[120:123]\n"
      "ds_read_b64_tr_b16 v[216:217], v234 offset:0\n"
      "ds_read_b64_tr_b16 v[218:219], v235 offset:0\n"
      "v_mfma_f32_16x16x32_bf16 a[120:123], v[204:207], a[232:235], a[120:123]\n"
      "ds_read_b64_tr_b16 v[220:221], v234 offset:1024\n"
      "ds_read_b64_tr_b16 v[222:223], v235 offset:1024\n"
      "v_mfma_f32_16x16x32_bf16 a[168:171], v[212:215], a[232:235], a[168:171]\n"
      "s_waitcnt lgkmcnt(12)\n"
      "ds_read_b64_tr_b16 a[232:233], v9 offset:256\n"
      "v_mul_f32 v112, v112, v242\n"
      "v_mfma_f32_16x16x32_bf16 v[92:95], v[192:195], a[244:247], v[92:95]\n"
      "ds_read_b64_tr_b16 a[234:235], v9 offset:6400\n"
      "ds_read_b64_tr_b16 a[236:237], v9 offset:12544\n"
      "v_mfma_f32_16x16x32_bf16 v[92:95], v[188:191], a[240:243], v[92:95]\n"
      "ds_read_b64_tr_b16 a[238:239], v9 offset:18688\n"
      "v_mul_f32 v113, v113, v243\n"
      "v_mfma_f32_16x16x32_bf16 v[140:143], v[196:199], a[240:243], v[140:143]\n"
      "v_mul_f32 v114, v114, v244\n"
      "v_mul_f32 v115, v115, v245\n"
      "v_mfma_f32_16x16x32_bf16 v[140:143], v[200:203], a[244:247], v[140:143]\n"
      "v_mul_f32 v116, v116, v242\n"
      "v_mul_f32 v117, v117, v243\n"
      "v_mfma_f32_16x16x32_bf16 a[124:127], v[208:211], a[244:247], a[124:127]\n"
      "v_mul_f32 v118, v118, v244\n"
      "v_mul_f32 v119, v119, v245\n"
      "v_mfma_f32_16x16x32_bf16 a[124:127], v[204:207], a[240:243], a[124:127]\n"
      "v_mul_f32 v120, v120, v242\n"
      "v_mul_f32 v121, v121, v243\n"
      "v_mfma_f32_16x16x32_bf16 a[172:175], v[212:215], a[240:243], a[172:175]\n"
      "s_waitcnt lgkmcnt(12)\n"
      "ds_read_b64_tr_b16 a[240:241], v10 offset:256\n"
      "v_mul_f32 v122, v122, v244\n"
      "v_mfma_f32_16x16x32_bf16 v[96:99], v[192:195], a[252:255], v[96:99]\n"
      "s_waitcnt lgkmcnt(5)\n"
      "ds_read_b64_tr_b16 a[242:243], v10 offset:6400\n"
      "ds_read_b64_tr_b16 a[244:245], v10 offset:12544\n"
      "v_mfma_f32_16x16x32_bf16 v[96:99], v[188:191], a[248:251], v[96:99]\n"
      "ds_read_b64_tr_b16 a[246:247], v10 offset:18688\n"
      "v_mul_f32 v123, v123, v245\n"
      "v_mfma_f32_16x16x32_bf16 v[144:147], v[196:199], a[248:251], v[144:147]\n"
      "buffer_store_dwordx4 v[216:219], v250, s[72:75], s52 offen offset:0 sc0 sc1 nt\n"
      "v_mfma_f32_16x16x32_bf16 v[144:147], v[200:203], a[252:255], v[144:147]\n"
      "buffer_store_dwordx4 v[220:223], v250, s[72:75], s52 offen offset:3072 sc0 sc1 nt\n"
      "v_mfma_f32_16x16x32_bf16 a[128:131], v[208:211], a[252:255], a[128:131]\n"
      "v_mul_f32 v124, v124, v242\n"
      "v_mul_f32 v125, v125, v243\n"
      "v_mfma_f32_16x16x32_bf16 a[128:131], v[204:207], a[248:251], a[128:131]\n"
      "v_mul_f32 v126, v126, v244\n"
      "v_mul_f32 v127, v127, v245\n"
      "v_mfma_f32_16x16x32_bf16 a[176:179], v[212:215], a[248:251], a[176:179]\n"
      "s_waitcnt lgkmcnt(4)\n"
      "ds_read_b64_tr_b16 a[248:249], v11 offset:256\n"
      "v_cvt_pk_bf16_f32 v36, v112, v116\n"
      "v_mfma_f32_16x16x32_bf16 v[100:103], v[192:195], a[236:239], v[100:103]\n"
      "ds_read_b64_tr_b16 a[250:251], v11 offset:6400\n"
      "ds_read_b64_tr_b16 a[252:253], v11 offset:12544\n"
      "v_mfma_f32_16x16x32_bf16 v[100:103], v[188:191], a[232:235], v[100:103]\n"
      "ds_read_b64_tr_b16 a[254:255], v11 offset:18688\n"
      "v_cvt_pk_bf16_f32 v37, v120, v124\n"
      "v_mfma_f32_16x16x32_bf16 v[148:151], v[196:199], a[232:235], v[148:151]\n"
      "v_cvt_pk_bf16_f32 v38, v113, v117\n"
      "v_cvt_pk_bf16_f32 v39, v121, v125\n"
      "v_mfma_f32_16x16x32_bf16 v[148:151], v[200:203], a[236:239], v[148:151]\n"
      "v_cvt_pk_bf16_f32 v40, v114, v118\n"
      "v_cvt_pk_bf16_f32 v41, v122, v126\n"
      "v_mfma_f32_16x16x32_bf16 a[132:135], v[208:211], a[236:239], a[132:135]\n"
      "v_cvt_pk_bf16_f32 v42, v115, v119\n"
      "v_cvt_pk_bf16_f32 v43, v123, v127\n"
      "v_mfma_f32_16x16x32_bf16 a[132:135], v[204:207], a[232:235], a[132:135]\n"
      "ds_write_b64 v232, v[36:37] offset:2048\n"
      "ds_write_b64 v232, v[38:39] offset:2176\n"
      "v_mfma_f32_16x16x32_bf16 a[180:183], v[212:215], a[232:235], a[180:183]\n"
      "s_waitcnt lgkmcnt(6)\n"
      "ds_write_b64 v233, v[40:41] offset:2304\n"
      "ds_write_b64 v233, v[42:43] offset:2432\n"
      "v_mfma_f32_16x16x32_bf16 v[104:107], v[192:195], a[244:247], v[104:107]\n"
      "ds_read_b64_tr_b16 v[224:225], v234 offset:2048\n"
      "ds_read_b64_tr_b16 v[226:227], v235 offset:2048\n"
      "v_mfma_f32_16x16x32_bf16 v[104:107], v[188:191], a[240:243], v[104:107]\n"
      "ds_read_b64_tr_b16 v[228:229], v234 offset:3072\n"
      "ds_read_b64_tr_b16 v[230:231], v235 offset:3072\n"
      "v_mfma_f32_16x16x32_bf16 v[152:155], v[196:199], a[240:243], v[152:155]\n"
      "v_accvgpr_read_b32 v160, a96\n"
      "v_accvgpr_read_b32 v161, a97\n"
      "v_mfma_f32_16x16x32_bf16 v[152:155], v[200:203], a[244:247], v[152:155]\n"
      "v_accvgpr_read_b32 v162, a98\n"
      "v_accvgpr_read_b32 v163, a99\n"
      "v_mfma_f32_16x16x32_bf16 a[136:139], v[208:211], a[244:247], a[136:139]\n"
      "v_accvgpr_read_b32 v164, a100\n"
      "v_accvgpr_read_b32 v165, a101\n"
      "v_mfma_f32_16x16x32_bf16 a[136:139], v[204:207], a[240:243], a[136:139]\n"
      "v_accvgpr_read_b32 v166, a102\n"
      "v_accvgpr_read_b32 v167, a103\n"
      "v_mfma_f32_16x16x32_bf16 a[184:187], v[212:215], a[240:243], a[184:187]\n"
      "s_waitcnt lgkmcnt(8)\n"
      "v_accvgpr_read_b32 v168, a104\n"
      "v_accvgpr_read_b32 v169, a105\n"
      "v_mfma_f32_16x16x32_bf16 v[108:111], v[192:195], a[252:255], v[108:111]\n"
      "v_accvgpr_read_b32 v170, a106\n"
      "v_accvgpr_read_b32 v171, a107\n"
      "v_mfma_f32_16x16x32_bf16 v[108:111], v[188:191], a[248:251], v[108:111]\n"
      "v_accvgpr_read_b32 v172, a108\n"
      "v_accvgpr_read_b32 v173, a109\n"
      "v_mfma_f32_16x16x32_bf16 v[156:159], v[196:199], a[248:251], v[156:159]\n"
      "s_waitcnt lgkmcnt(0)\n"
      "v_accvgpr_read_b32 v174, a110\n"
      "v_accvgpr_read_b32 v175, a111\n"
      "v_mfma_f32_16x16x32_bf16 v[156:159], v[200:203], a[252:255], v[156:159]\n"
      "buffer_store_dwordx4 v[224:227], v250, s[72:75], s53 offen offset:0 sc0 sc1 nt\n"
      "v_mfma_f32_16x16x32_bf16 a[140:143], v[208:211], a[252:255], a[140:143]\n"
      "buffer_store_dwordx4 v[228:231], v250, s[72:75], s53 offen offset:3072 sc0 sc1 nt\n"
      "v_mfma_f32_16x16x32_bf16 a[140:143], v[204:207], a[248:251], a[140:143]\n"
      "v_mul_f32 v160, v160, v246\n"
      "v_mul_f32 v161, v161, v247\n"
      "v_mfma_f32_16x16x32_bf16 a[188:191], v[212:215], a[248:251], a[188:191]\n"
      "v_mul_f32 v162, v162, v248\n"
      "v_mul_f32 v163, v163, v249\n"
      "v_mul_f32 v164, v164, v246\n"
      "v_mul_f32 v165, v165, v247\n"
      "v_mul_f32 v166, v166, v248\n"
      "v_mul_f32 v167, v167, v249\n"
      "v_mul_f32 v168, v168, v246\n"
      "v_mul_f32 v169, v169, v247\n"
      "v_mul_f32 v170, v170, v248\n"
      "v_mul_f32 v171, v171, v249\n"
      "v_mul_f32 v172, v172, v246\n"
      "v_mul_f32 v173, v173, v247\n"
      "v_mul_f32 v174, v174, v248\n"
      "v_mul_f32 v175, v175, v249\n"
      "v_cvt_pk_bf16_f32 v176, v160, v164\n"
      "v_cvt_pk_bf16_f32 v177, v168, v172\n"
      "v_cvt_pk_bf16_f32 v178, v161, v165\n"
      "v_cvt_pk_bf16_f32 v179, v169, v173\n"
      "v_cvt_pk_bf16_f32 v180, v162, v166\n"
      "v_cvt_pk_bf16_f32 v181, v170, v174\n"
      "v_cvt_pk_bf16_f32 v182, v163, v167\n"
      "v_cvt_pk_bf16_f32 v183, v171, v175\n"
      "ds_write_b64 v232, v[176:177] offset:4096\n"
      "ds_write_b64 v232, v[178:179] offset:4224\n"
      "ds_write_b64 v233, v[180:181] offset:4352\n"
      "ds_write_b64 v233, v[182:183] offset:4480\n"
      "ds_read_b64_tr_b16 v[44:45], v234 offset:4096\n"
      "ds_read_b64_tr_b16 v[46:47], v235 offset:4096\n"
      "ds_read_b64_tr_b16 v[48:49], v234 offset:5120\n"
      "ds_read_b64_tr_b16 v[50:51], v235 offset:5120\n"
      "s_add_u32 s63, s44, s42\n"
      "s_add_u32 s64, s63, 6144\n"
      "v_add_u32 v52, s64, v23\n"
      "v_add_u32 v23, s63, v23\n"
      "s_lshl_b32 s63, s61, 9\n"
      "s_add_u32 s63, s63, s65\n"
      "s_add_u32 s63, s63, 147456\n"
      "v_lshlrev_b32 v53, 3, v0\n"
      "v_add_u32 v53, s63, v53\n"
      "v_mov_b32 v54, v21\n"
      "v_accvgpr_read_b32 v55, a204\n"
      "v_xor_b32 v58, 512, v53\n"
      "v_pk_mul_f32 v[80:81], v[80:81], v[238:239]\n"
      "v_pk_mul_f32 v[82:83], v[82:83], v[240:241]\n"
      "v_pk_mul_f32 v[84:85], v[84:85], v[238:239]\n"
      "v_pk_mul_f32 v[86:87], v[86:87], v[240:241]\n"
      "v_pk_mul_f32 v[88:89], v[88:89], v[238:239]\n"
      "v_pk_mul_f32 v[90:91], v[90:91], v[240:241]\n"
      "v_pk_mul_f32 v[92:93], v[92:93], v[238:239]\n"
      "v_pk_mul_f32 v[94:95], v[94:95], v[240:241]\n"
      "s_waitcnt lgkmcnt(0)\n"
      "buffer_store_dwordx4 v[44:47], v250, s[72:75], s54 offen offset:0 sc0 sc1 nt\n"
      "buffer_store_dwordx4 v[48:51], v250, s[72:75], s54 offen offset:3072 sc0 sc1 nt\n"
      "ds_write_b64 v53, v[54:55] offset:0\n"
      "ds_write_b128 v52, a[168:171] offset:0\n"
      "v_cvt_pk_bf16_f32 v176, v80, v84\n"
      "v_cvt_pk_bf16_f32 v177, v88, v92\n"
      "v_cvt_pk_bf16_f32 v178, v81, v85\n"
      "v_cvt_pk_bf16_f32 v179, v89, v93\n"
      "v_cvt_pk_bf16_f32 v180, v82, v86\n"
      "v_cvt_pk_bf16_f32 v181, v90, v94\n"
      "v_cvt_pk_bf16_f32 v182, v83, v87\n"
      "v_cvt_pk_bf16_f32 v183, v91, v95\n"
      "ds_write_b128 v52, a[172:175] offset:1024\n"
      "v_pk_mul_f32 v[96:97], v[96:97], v[238:239]\n"
      "v_pk_mul_f32 v[98:99], v[98:99], v[240:241]\n"
      "v_pk_mul_f32 v[100:101], v[100:101], v[238:239]\n"
      "ds_write_b128 v52, a[176:179] offset:2048\n"
      "ds_write_b128 v52, a[180:183] offset:3072\n"
      "v_pk_mul_f32 v[102:103], v[102:103], v[240:241]\n"
      "v_pk_mul_f32 v[104:105], v[104:105], v[238:239]\n"
      "v_pk_mul_f32 v[106:107], v[106:107], v[240:241]\n"
      "ds_write_b64 v232, v[176:177] offset:0\n"
      "ds_write_b64 v232, v[178:179] offset:128\n"
      "v_pk_mul_f32 v[108:109], v[108:109], v[238:239]\n"
      "v_pk_mul_f32 v[110:111], v[110:111], v[240:241]\n"
      "v_cvt_pk_bf16_f32 v184, v96, v100\n"
      "ds_write_b64 v233, v[180:181] offset:256\n"
      "ds_write_b64 v233, v[182:183] offset:384\n"
      "v_cvt_pk_bf16_f32 v185, v104, v108\n"
      "v_cvt_pk_bf16_f32 v186, v97, v101\n"
      "v_cvt_pk_bf16_f32 v187, v105, v109\n"
      "ds_read_b64_tr_b16 v[192:193], v234 offset:0\n"
      "ds_read_b64_tr_b16 v[194:195], v235 offset:0\n"
      "v_cvt_pk_bf16_f32 v188, v98, v102\n"
      "v_cvt_pk_bf16_f32 v189, v106, v110\n"
      "v_cvt_pk_bf16_f32 v190, v99, v103\n"
      "v_cvt_pk_bf16_f32 v191, v107, v111\n"
      "ds_read_b64_tr_b16 v[196:197], v234 offset:1024\n"
      "ds_read_b64_tr_b16 v[198:199], v235 offset:1024\n"
      "v_pk_mul_f32 v[128:129], v[128:129], v[242:243]\n"
      "v_pk_mul_f32 v[130:131], v[130:131], v[244:245]\n"
      "v_pk_mul_f32 v[132:133], v[132:133], v[242:243]\n"
      "ds_write_b128 v52, a[184:187] offset:4096\n"
      "ds_write_b128 v52, a[188:191] offset:5120\n"
      "v_pk_mul_f32 v[134:135], v[134:135], v[244:245]\n"
      "v_pk_mul_f32 v[136:137], v[136:137], v[242:243]\n"
      "v_pk_mul_f32 v[138:139], v[138:139], v[244:245]\n"
      "v_pk_mul_f32 v[140:141], v[140:141], v[242:243]\n"
      "ds_write_b64 v232, v[184:185] offset:2048\n"
      "ds_write_b64 v232, v[186:187] offset:2176\n"
      "v_pk_mul_f32 v[142:143], v[142:143], v[244:245]\n"
      "v_cvt_pk_bf16_f32 v176, v128, v132\n"
      "s_waitcnt lgkmcnt(4)\n"
      "v_cvt_pk_bf16_f32 v177, v136, v140\n"
      "ds_write_b64 v233, v[188:189] offset:2304\n"
      "ds_write_b64 v233, v[190:191] offset:2432\n"
      "buffer_store_dwordx4 v[192:195], v250, s[72:75], s52 offen offset:128 sc0 sc1 nt\n"
      "v_cvt_pk_bf16_f32 v178, v129, v133\n"
      "v_cvt_pk_bf16_f32 v179, v137, v141\n"
      "v_cvt_pk_bf16_f32 v180, v130, v134\n"
      "ds_read_b64_tr_b16 v[200:201], v234 offset:2048\n"
      "ds_read_b64_tr_b16 v[202:203], v235 offset:2048\n"
      "buffer_store_dwordx4 v[196:199], v250, s[72:75], s52 offen offset:3200 sc0 sc1 nt\n"
      "v_cvt_pk_bf16_f32 v181, v138, v142\n"
      "v_cvt_pk_bf16_f32 v182, v131, v135\n"
      "v_cvt_pk_bf16_f32 v183, v139, v143\n"
      "ds_read_b64_tr_b16 v[204:205], v234 offset:3072\n"
      "ds_read_b64_tr_b16 v[206:207], v235 offset:3072\n"
      "v_pk_mul_f32 v[144:145], v[144:145], v[242:243]\n"
      "v_pk_mul_f32 v[146:147], v[146:147], v[244:245]\n"
      "v_pk_mul_f32 v[148:149], v[148:149], v[242:243]\n"
      "v_pk_mul_f32 v[150:151], v[150:151], v[244:245]\n"
      "ds_write_b64 v232, v[176:177] offset:4096\n"
      "ds_write_b64 v232, v[178:179] offset:4224\n"
      "v_pk_mul_f32 v[152:153], v[152:153], v[242:243]\n"
      "v_pk_mul_f32 v[154:155], v[154:155], v[244:245]\n"
      "v_pk_mul_f32 v[156:157], v[156:157], v[242:243]\n"
      "v_pk_mul_f32 v[158:159], v[158:159], v[244:245]\n"
      "v_cvt_pk_bf16_f32 v184, v144, v148\n"
      "ds_write_b64 v233, v[180:181] offset:4352\n"
      "ds_write_b64 v233, v[182:183] offset:4480\n"
      "s_waitcnt lgkmcnt(4)\n"
      "v_cvt_pk_bf16_f32 v185, v152, v156\n"
      "buffer_store_dwordx4 v[200:203], v250, s[72:75], s52 offen offset:256 sc0 sc1 nt\n"
      "v_cvt_pk_bf16_f32 v186, v145, v149\n"
      "v_cvt_pk_bf16_f32 v187, v153, v157\n"
      "ds_read_b64_tr_b16 v[208:209], v234 offset:4096\n"
      "ds_read_b64_tr_b16 v[210:211], v235 offset:4096\n"
      "v_cvt_pk_bf16_f32 v188, v146, v150\n"
      "buffer_store_dwordx4 v[204:207], v250, s[72:75], s52 offen offset:3328 sc0 sc1 nt\n"
      "v_cvt_pk_bf16_f32 v189, v154, v158\n"
      "v_cvt_pk_bf16_f32 v190, v147, v151\n"
      "v_cvt_pk_bf16_f32 v191, v155, v159\n"
      "ds_read_b64_tr_b16 v[212:213], v234 offset:5120\n"
      "ds_read_b64_tr_b16 v[214:215], v235 offset:5120\n"
      "s_waitcnt lgkmcnt(8)\n"
      "s_barrier\n"
      "v_accvgpr_read_b32 v160, a112\n"
      "v_accvgpr_read_b32 v161, a113\n"
      "v_accvgpr_read_b32 v162, a114\n"
      "v_accvgpr_read_b32 v163, a115\n"
      "v_accvgpr_read_b32 v164, a116\n"
      "ds_write_b64 v232, v[184:185] offset:0\n"
      "ds_write_b64 v232, v[186:187] offset:128\n"
      "v_accvgpr_read_b32 v165, a117\n"
      "v_accvgpr_read_b32 v166, a118\n"
      "v_accvgpr_read_b32 v167, a119\n"
      "v_accvgpr_read_b32 v168, a120\n"
      "v_accvgpr_read_b32 v169, a121\n"
      "v_accvgpr_read_b32 v170, a122\n"
      "ds_write_b64 v233, v[188:189] offset:256\n"
      "ds_write_b64 v233, v[190:191] offset:384\n"
      "v_accvgpr_read_b32 v171, a123\n"
      "v_accvgpr_read_b32 v172, a124\n"
      "v_accvgpr_read_b32 v173, a125\n"
      "v_accvgpr_read_b32 v174, a126\n"
      "v_accvgpr_read_b32 v175, a127\n"
      "v_pk_mul_f32 v[160:161], v[160:161], v[246:247]\n"
      "ds_read_b64_tr_b16 v[192:193], v234 offset:0\n"
      "ds_read_b64_tr_b16 v[194:195], v235 offset:0\n"
      "v_pk_mul_f32 v[162:163], v[162:163], v[248:249]\n"
      "v_pk_mul_f32 v[164:165], v[164:165], v[246:247]\n"
      "v_pk_mul_f32 v[166:167], v[166:167], v[248:249]\n"
      "v_pk_mul_f32 v[168:169], v[168:169], v[246:247]\n"
      "v_pk_mul_f32 v[170:171], v[170:171], v[248:249]\n"
      "v_pk_mul_f32 v[172:173], v[172:173], v[246:247]\n"
      "ds_read_b64_tr_b16 v[196:197], v234 offset:1024\n"
      "ds_read_b64_tr_b16 v[198:199], v235 offset:1024\n"
      "v_pk_mul_f32 v[174:175], v[174:175], v[248:249]\n"
      "v_cvt_pk_bf16_f32 v176, v160, v164\n"
      "s_waitcnt lgkmcnt(8)\n"
      "v_cvt_pk_bf16_f32 v177, v168, v172\n"
      "buffer_store_dwordx4 v[208:211], v250, s[72:75], s53 offen offset:128 sc0 sc1 nt\n"
      "v_cvt_pk_bf16_f32 v178, v161, v165\n"
      "ds_read_b64 v[56:57], v58 offset:0\n"
      "ds_read_b128 v[36:39], v23 offset:0\n"
      "v_cvt_pk_bf16_f32 v179, v169, v173\n"
      "v_cvt_pk_bf16_f32 v180, v162, v166\n"
      "buffer_store_dwordx4 v[212:215], v250, s[72:75], s53 offen offset:3200 sc0 sc1 nt\n"
      "v_cvt_pk_bf16_f32 v181, v170, v174\n"
      "v_cvt_pk_bf16_f32 v182, v163, v167\n"
      "v_cvt_pk_bf16_f32 v183, v171, v175\n"
      "ds_read_b128 v[40:43], v23 offset:1024\n"
      "ds_read_b128 v[44:47], v23 offset:2048\n"
      "s_waitcnt lgkmcnt(3)\n"
      "v_min_f32 v59, v54, v56\n"
      "v_sub_f32 v52, v59, v54\n"
      "v_sub_f32 v53, v59, v56\n"
      "v_exp_f32 v52, v52\n"
      "v_exp_f32 v53, v53\n"
      "v_mul_f32 v55, v55, v52\n"
      "v_fma_f32 v55, v57, v53, v55\n"
      "v_rcp_f32 v55, v55\n"
      "s_nop 0\n"
      "v_mul_f32 v52, v52, v55\n"
      "v_mul_f32 v53, v53, v55\n"
      "v_accvgpr_read_b32 v160, a128\n"
      "v_accvgpr_read_b32 v161, a129\n"
      "v_accvgpr_read_b32 v162, a130\n"
      "v_accvgpr_read_b32 v163, a131\n"
      "ds_write_b32 v236, v52 offset:192\n"
      "ds_write_b32 v236, v53 offset:256\n"
      "v_accvgpr_read_b32 v164, a132\n"
      "v_accvgpr_read_b32 v165, a133\n"
      "v_accvgpr_read_b32 v166, a134\n"
      "v_accvgpr_read_b32 v167, a135\n"
      "ds_read_b128 v[56:59], v237 offset:192\n"
      "ds_read_b128 v[60:63], v237 offset:256\n"
      "v_accvgpr_read_b32 v168, a136\n"
      "v_accvgpr_read_b32 v169, a137\n"
      "v_accvgpr_read_b32 v170, a138\n"
      "v_accvgpr_read_b32 v171, a139\n"
      "v_accvgpr_read_b32 v172, a140\n"
      "ds_write_b64 v232, v[176:177] offset:2048\n"
      "ds_write_b64 v232, v[178:179] offset:2176\n"
      "v_accvgpr_read_b32 v173, a141\n"
      "v_accvgpr_read_b32 v174, a142\n"
      "v_accvgpr_read_b32 v175, a143\n"
      "v_pk_mul_f32 v[160:161], v[160:161], v[246:247]\n"
      "ds_write_b64 v233, v[180:181] offset:2304\n"
      "ds_write_b64 v233, v[182:183] offset:2432\n"
      "v_pk_mul_f32 v[162:163], v[162:163], v[248:249]\n"
      "v_pk_mul_f32 v[164:165], v[164:165], v[246:247]\n"
      "v_pk_mul_f32 v[166:167], v[166:167], v[248:249]\n"
      "v_pk_mul_f32 v[168:169], v[168:169], v[246:247]\n"
      "ds_read_b64_tr_b16 v[200:201], v234 offset:2048\n"
      "ds_read_b64_tr_b16 v[202:203], v235 offset:2048\n"
      "v_pk_mul_f32 v[170:171], v[170:171], v[248:249]\n"
      "v_pk_mul_f32 v[172:173], v[172:173], v[246:247]\n"
      "v_pk_mul_f32 v[174:175], v[174:175], v[248:249]\n"
      "v_cvt_pk_bf16_f32 v184, v160, v164\n"
      "s_waitcnt lgkmcnt(13)\n"
      "ds_read_b64_tr_b16 v[204:205], v234 offset:3072\n"
      "ds_read_b64_tr_b16 v[206:207], v235 offset:3072\n"
      "v_cvt_pk_bf16_f32 v185, v168, v172\n"
      "buffer_store_dwordx4 v[192:195], v250, s[72:75], s53 offen offset:256 sc0 sc1 nt\n"
      "v_cvt_pk_bf16_f32 v186, v161, v165\n"
      "v_cvt_pk_bf16_f32 v187, v169, v173\n"
      "ds_read_b128 v[48:51], v23 offset:3072\n"
      "ds_read_b128 v[216:219], v23 offset:4096\n"
      "v_cvt_pk_bf16_f32 v188, v162, v166\n"
      "buffer_store_dwordx4 v[196:199], v250, s[72:75], s53 offen offset:3328 sc0 sc1 nt\n"
      "v_cvt_pk_bf16_f32 v189, v170, v174\n"
      "v_cvt_pk_bf16_f32 v190, v163, v167\n"
      "v_cvt_pk_bf16_f32 v191, v171, v175\n"
      "ds_read_b128 v[220:223], v23 offset:5120\n"
      "s_waitcnt lgkmcnt(0)\n"
      "v_accvgpr_read_b32 v160, a144\n"
      "v_accvgpr_read_b32 v161, a145\n"
      "v_accvgpr_read_b32 v162, a146\n"
      "v_accvgpr_read_b32 v163, a147\n"
      "v_accvgpr_read_b32 v164, a148\n"
      "v_accvgpr_read_b32 v165, a149\n"
      "v_accvgpr_read_b32 v166, a150\n"
      "v_accvgpr_read_b32 v167, a151\n"
      "v_accvgpr_read_b32 v168, a152\n"
      "v_accvgpr_read_b32 v169, a153\n"
      "ds_write_b64 v232, v[184:185] offset:4096\n"
      "ds_write_b64 v232, v[186:187] offset:4224\n"
      "v_accvgpr_read_b32 v170, a154\n"
      "v_accvgpr_read_b32 v171, a155\n"
      "v_accvgpr_read_b32 v172, a156\n"
      "v_accvgpr_read_b32 v173, a157\n"
      "v_accvgpr_read_b32 v174, a158\n"
      "v_accvgpr_read_b32 v175, a159\n"
      "v_pk_mul_f32 v[160:161], v[160:161], v[56:57]\n"
      "v_pk_mul_f32 v[162:163], v[162:163], v[58:59]\n"
      "v_pk_mul_f32 v[164:165], v[164:165], v[56:57]\n"
      "v_pk_mul_f32 v[166:167], v[166:167], v[58:59]\n"
      "v_pk_mul_f32 v[168:169], v[168:169], v[56:57]\n"
      "ds_write_b64 v233, v[188:189] offset:4352\n"
      "ds_write_b64 v233, v[190:191] offset:4480\n"
      "v_pk_mul_f32 v[170:171], v[170:171], v[58:59]\n"
      "v_pk_mul_f32 v[172:173], v[172:173], v[56:57]\n"
      "v_pk_mul_f32 v[174:175], v[174:175], v[58:59]\n"
      "v_pk_fma_f32 v[160:161], v[36:37], v[60:61], v[160:161]\n"
      "v_pk_fma_f32 v[162:163], v[38:39], v[62:63], v[162:163]\n"
      "v_pk_fma_f32 v[164:165], v[40:41], v[60:61], v[164:165]\n"
      "v_pk_fma_f32 v[166:167], v[42:43], v[62:63], v[166:167]\n"
      "v_pk_fma_f32 v[168:169], v[44:45], v[60:61], v[168:169]\n"
      "v_pk_fma_f32 v[170:171], v[46:47], v[62:63], v[170:171]\n"
      "v_pk_fma_f32 v[172:173], v[48:49], v[60:61], v[172:173]\n"
      "v_pk_fma_f32 v[174:175], v[50:51], v[62:63], v[174:175]\n"
      "ds_read_b64_tr_b16 v[208:209], v234 offset:4096\n"
      "ds_read_b64_tr_b16 v[210:211], v235 offset:4096\n"
      "v_cvt_pk_bf16_f32 v176, v160, v164\n"
      "s_waitcnt lgkmcnt(6)\n"
      "v_cvt_pk_bf16_f32 v177, v168, v172\n"
      "buffer_store_dwordx4 v[200:203], v250, s[72:75], s54 offen offset:128 sc0 sc1 nt\n"
      "v_cvt_pk_bf16_f32 v178, v161, v165\n"
      "v_cvt_pk_bf16_f32 v179, v169, v173\n"
      "v_cvt_pk_bf16_f32 v180, v162, v166\n"
      "buffer_store_dwordx4 v[204:207], v250, s[72:75], s54 offen offset:3200 sc0 sc1 nt\n"
      "v_cvt_pk_bf16_f32 v181, v170, v174\n"
      "v_cvt_pk_bf16_f32 v182, v163, v167\n"
      "v_cvt_pk_bf16_f32 v183, v171, v175\n"
      "ds_read_b64_tr_b16 v[212:213], v234 offset:5120\n"
      "ds_read_b64_tr_b16 v[214:215], v235 offset:5120\n"
      "v_accvgpr_read_b32 v160, a160\n"
      "v_accvgpr_read_b32 v161, a161\n"
      "v_accvgpr_read_b32 v162, a162\n"
      "v_accvgpr_read_b32 v163, a163\n"
      "v_accvgpr_read_b32 v164, a164\n"
      "v_accvgpr_read_b32 v165, a165\n"
      "v_accvgpr_read_b32 v166, a166\n"
      "ds_write_b64 v232, v[176:177] offset:0\n"
      "ds_write_b64 v232, v[178:179] offset:128\n"
      "v_accvgpr_read_b32 v167, a167\n"
      "v_and_b32 v54, 2, v0\n"
      "v_cmp_eq_u32 vcc, 0, v54\n"
      "v_mov_b32 v54, 0x7fff0000\n"
      "s_nop 0\n"
      "v_cndmask_b32 v54, v54, v250, vcc\n"
      "v_pk_mul_f32 v[160:161], v[160:161], v[56:57]\n"
      "v_pk_mul_f32 v[162:163], v[162:163], v[58:59]\n"
      "v_pk_mul_f32 v[164:165], v[164:165], v[56:57]\n"
      "ds_write_b64 v233, v[180:181] offset:256\n"
      "ds_write_b64 v233, v[182:183] offset:384\n"
      "v_pk_mul_f32 v[166:167], v[166:167], v[58:59]\n"
      "v_pk_fma_f32 v[160:161], v[216:217], v[60:61], v[160:161]\n"
      "v_pk_fma_f32 v[162:163], v[218:219], v[62:63], v[162:163]\n"
      "v_pk_fma_f32 v[164:165], v[220:221], v[60:61], v[164:165]\n"
      "v_pk_fma_f32 v[166:167], v[222:223], v[62:63], v[166:167]\n"
      "v_cvt_pk_bf16_f32 v184, v160, v164\n"
      "s_waitcnt lgkmcnt(4)\n"
      "v_cvt_pk_bf16_f32 v185, v160, v164\n"
      "ds_read_b64_tr_b16 v[192:193], v234 offset:0\n"
      "ds_read_b64_tr_b16 v[194:195], v235 offset:0\n"
      "buffer_store_dwordx4 v[208:211], v250, s[72:75], s54 offen offset:256 sc0 sc1 nt\n"
      "v_cvt_pk_bf16_f32 v186, v161, v165\n"
      "v_cvt_pk_bf16_f32 v187, v161, v165\n"
      "v_cvt_pk_bf16_f32 v188, v162, v166\n"
      "buffer_store_dwordx4 v[212:215], v250, s[72:75], s54 offen offset:3328 sc0 sc1 nt\n"
      "v_cvt_pk_bf16_f32 v189, v162, v166\n"
      "v_cvt_pk_bf16_f32 v190, v163, v167\n"
      "v_cvt_pk_bf16_f32 v191, v163, v167\n"
      "ds_read_b64_tr_b16 v[196:197], v234 offset:1024\n"
      "ds_read_b64_tr_b16 v[198:199], v235 offset:1024\n"
      "ds_write_b64 v232, v[184:185] offset:2048\n"
      "ds_write_b64 v232, v[186:187] offset:2176\n"
      "ds_write_b64 v233, v[188:189] offset:2304\n"
      "ds_write_b64 v233, v[190:191] offset:2432\n"
      "s_waitcnt lgkmcnt(4)\n"
      "buffer_store_dwordx4 v[192:195], v250, s[72:75], s55 offen offset:0 sc0 sc1 nt\n"
      "buffer_store_dwordx4 v[196:199], v250, s[72:75], s55 offen offset:3072 sc0 sc1 nt\n"
      "ds_read_b64_tr_b16 v[200:201], v234 offset:2048\n"
      "ds_read_b64_tr_b16 v[202:203], v235 offset:2048\n"
      "ds_read_b64_tr_b16 v[204:205], v234 offset:3072\n"
      "ds_read_b64_tr_b16 v[206:207], v235 offset:3072\n"
      "s_waitcnt lgkmcnt(0)\n"
      "buffer_store_dwordx4 v[200:203], v54, s[72:75], s55 offen offset:128 sc0 sc1\n"
      "buffer_store_dwordx4 v[204:207], v54, s[72:75], s55 offen offset:3200 sc0 sc1\n"
      "s_branch L_end\n"
      "L_pro_O:\n"
      "s_mov_b32 s50, 7\n"
      "ds_read_b128 a[208:211], v4 offset:0\n"
      "ds_read_b128 a[212:215], v5 offset:0\n"
      "ds_read_b128 a[216:219], v4 offset:128\n"
      "ds_read_b128 a[220:223], v5 offset:128\n"
      "ds_read_b128 a[224:227], v4 offset:256\n"
      "ds_read_b128 a[228:231], v5 offset:256\n"
      "s_add_u32 s43, s44, 49152\n"
      "s_sub_u32 s43, s43, s65\n"
      "v_add_u32 v232, s43, v6\n"
      "v_add_u32 v233, s43, v7\n"
      "s_waitcnt vmcnt(6)\n"
      "ds_read_b128 a[0:3], v232 offset:0\n"
      "ds_read_b128 a[4:7], v233 offset:0\n"
      "ds_read_b128 a[8:11], v232 offset:128\n"
      "ds_read_b128 a[12:15], v233 offset:128\n"
      "ds_read_b128 a[16:19], v232 offset:256\n"
      "ds_read_b128 a[20:23], v233 offset:256\n"
      "s_add_u32 m0, s45, 49152\n"
      "s_add_u32 s63, s54, 0\n"
      "buffer_load_dwordx4 v1, s[28:31], s63 offen offset:0 nt lds\n"
      "buffer_load_dwordx4 v2, s[28:31], s63 offen offset:1024 nt lds\n"
      "buffer_load_dwordx4 v3, s[28:31], s63 offen offset:2048 nt lds\n"
      "s_add_u32 m0, s45, 52224\n"
      "s_add_u32 s63, s54, 3072\n"
      "buffer_load_dwordx4 v1, s[28:31], s63 offen offset:0 nt lds\n"
      "buffer_load_dwordx4 v2, s[28:31], s63 offen offset:1024 nt lds\n"
      "buffer_load_dwordx4 v3, s[28:31], s63 offen offset:2048 nt lds\n"
      "s_waitcnt lgkmcnt(0)\n"
      "v_mfma_f32_16x16x32_bf16 v[36:39], a[208:211], a[0:3], 0\n"
      "ds_read_b128 a[232:235], v4 offset:6144\n"
      "ds_read_b128 a[236:239], v5 offset:6144\n"
      "v_mfma_f32_16x16x32_bf16 v[36:39], a[212:215], a[4:7], v[36:39]\n"
      "ds_read_b128 a[240:243], v4 offset:6272\n"
      "ds_read_b128 a[244:247], v5 offset:6272\n"
      "v_mfma_f32_16x16x32_bf16 v[36:39], a[216:219], a[8:11], v[36:39]\n"
      "ds_read_b128 a[248:251], v4 offset:6400\n"
      "ds_read_b128 a[252:255], v5 offset:6400\n"
      "v_mfma_f32_16x16x32_bf16 v[36:39], a[220:223], a[12:15], v[36:39]\n"
      "v_mfma_f32_16x16x32_bf16 v[36:39], a[224:227], a[16:19], v[36:39]\n"
      "v_mfma_f32_16x16x32_bf16 v[36:39], a[228:231], a[20:23], v[36:39]\n"
      "s_waitcnt lgkmcnt(4)\n"
      "v_mfma_f32_16x16x32_bf16 v[40:43], a[232:235], a[0:3], 0\n"
      "ds_read_b128 a[208:211], v4 offset:12288\n"
      "ds_read_b128 a[212:215], v5 offset:12288\n"
      "v_mfma_f32_16x16x32_bf16 v[40:43], a[236:239], a[4:7], v[40:43]\n"
      "s_waitcnt lgkmcnt(4)\n"
      "ds_read_b128 a[216:219], v4 offset:12416\n"
      "ds_read_b128 a[220:223], v5 offset:12416\n"
      "v_mfma_f32_16x16x32_bf16 v[40:43], a[240:243], a[8:11], v[40:43]\n"
      "ds_read_b128 a[224:227], v4 offset:12544\n"
      "ds_read_b128 a[228:231], v5 offset:12544\n"
      "v_mfma_f32_16x16x32_bf16 v[40:43], a[244:247], a[12:15], v[40:43]\n"
      "s_waitcnt lgkmcnt(6)\n"
      "v_max3_f32 v52, v36, v37, v38\n"
      "v_max_f32 v52, v52, v39\n"
      "v_mfma_f32_16x16x32_bf16 v[40:43], a[248:251], a[16:19], v[40:43]\n"
      "v_mfma_f32_16x16x32_bf16 v[40:43], a[252:255], a[20:23], v[40:43]\n"
      "s_waitcnt lgkmcnt(4)\n"
      "v_mfma_f32_16x16x32_bf16 v[44:47], a[208:211], a[0:3], 0\n"
      "ds_read_b128 a[232:235], v4 offset:18432\n"
      "ds_read_b128 a[236:239], v5 offset:18432\n"
      "v_mfma_f32_16x16x32_bf16 v[44:47], a[212:215], a[4:7], v[44:47]\n"
      "s_waitcnt lgkmcnt(4)\n"
      "ds_read_b128 a[240:243], v4 offset:18560\n"
      "ds_read_b128 a[244:247], v5 offset:18560\n"
      "v_mfma_f32_16x16x32_bf16 v[44:47], a[216:219], a[8:11], v[44:47]\n"
      "ds_read_b128 a[248:251], v4 offset:18688\n"
      "ds_read_b128 a[252:255], v5 offset:18688\n"
      "v_mfma_f32_16x16x32_bf16 v[44:47], a[220:223], a[12:15], v[44:47]\n"
      "s_waitcnt lgkmcnt(6)\n"
      "v_max3_f32 v52, v52, v40, v41\n"
      "v_max3_f32 v52, v52, v42, v43\n"
      "v_mfma_f32_16x16x32_bf16 v[44:47], a[224:227], a[16:19], v[44:47]\n"
      "v_mfma_f32_16x16x32_bf16 v[44:47], a[228:231], a[20:23], v[44:47]\n"
      "s_waitcnt lgkmcnt(4)\n"
      "v_mfma_f32_16x16x32_bf16 v[48:51], a[232:235], a[0:3], 0\n"
      "ds_read_b128 a[208:211], v4 offset:0\n"
      "ds_read_b128 a[212:215], v5 offset:0\n"
      "v_mfma_f32_16x16x32_bf16 v[48:51], a[236:239], a[4:7], v[48:51]\n"
      "s_waitcnt lgkmcnt(4)\n"
      "ds_read_b128 a[216:219], v4 offset:128\n"
      "ds_read_b128 a[220:223], v5 offset:128\n"
      "v_mfma_f32_16x16x32_bf16 v[48:51], a[240:243], a[8:11], v[48:51]\n"
      "ds_read_b128 a[224:227], v4 offset:256\n"
      "ds_read_b128 a[228:231], v5 offset:256\n"
      "v_mfma_f32_16x16x32_bf16 v[48:51], a[244:247], a[12:15], v[48:51]\n"
      "s_waitcnt lgkmcnt(6)\n"
      "v_max3_f32 v52, v52, v44, v45\n"
      "v_max3_f32 v52, v52, v46, v47\n"
      "v_mfma_f32_16x16x32_bf16 v[48:51], a[248:251], a[16:19], v[48:51]\n"
      "v_mfma_f32_16x16x32_bf16 v[48:51], a[252:255], a[20:23], v[48:51]\n"
      "s_nop 7\n"
      "v_max3_f32 v52, v52, v48, v49\n"
      "v_max3_f32 v52, v52, v50, v51\n"
      "v_mov_b32 v53, v52\n"
      "s_nop 1\n"
      "v_permlane32_swap_b32 v52, v53\n"
      "v_max_f32 v52, v52, v53\n"
      "v_mov_b32 v53, v52\n"
      "s_nop 1\n"
      "v_permlane16_swap_b32 v52, v53\n"
      "v_max_f32 v52, v52, v53\n"
      "v_mul_f32 v52, s62, v52\n"
      "v_add_f32 v18, 0xc1800000, v52\n"
      "v_fma_f32 v36, v36, s48, v18\n"
      "v_fma_f32 v37, v37, s48, v18\n"
      "v_fma_f32 v38, v38, s48, v18\n"
      "v_fma_f32 v39, v39, s48, v18\n"
      "v_exp_f32 v28, v36\n"
      "v_exp_f32 v29, v37\n"
      "v_exp_f32 v30, v38\n"
      "v_exp_f32 v31, v39\n"
      "v_cvt_pk_bf16_f32 v160, v28, v29\n"
      "v_cvt_pk_bf16_f32 v161, v30, v31\n"
      "v_fma_f32 v40, v40, s48, v18\n"
      "v_fma_f32 v41, v41, s48, v18\n"
      "v_fma_f32 v42, v42, s48, v18\n"
      "v_fma_f32 v43, v43, s48, v18\n"
      "v_exp_f32 v32, v40\n"
      "v_exp_f32 v33, v41\n"
      "v_exp_f32 v34, v42\n"
      "v_exp_f32 v35, v43\n"
      "v_cvt_pk_bf16_f32 v162, v32, v33\n"
      "v_cvt_pk_bf16_f32 v163, v34, v35\n"
      "v_fma_f32 v44, v44, s48, v18\n"
      "v_fma_f32 v45, v45, s48, v18\n"
      "v_fma_f32 v46, v46, s48, v18\n"
      "v_fma_f32 v47, v47, s48, v18\n"
      "v_exp_f32 v28, v44\n"
      "v_exp_f32 v29, v45\n"
      "v_exp_f32 v30, v46\n"
      "v_exp_f32 v31, v47\n"
      "v_cvt_pk_bf16_f32 v164, v28, v29\n"
      "v_cvt_pk_bf16_f32 v165, v30, v31\n"
      "v_fma_f32 v48, v48, s48, v18\n"
      "v_fma_f32 v49, v49, s48, v18\n"
      "v_fma_f32 v50, v50, s48, v18\n"
      "v_fma_f32 v51, v51, s48, v18\n"
      "v_exp_f32 v32, v48\n"
      "v_exp_f32 v33, v49\n"
      "v_exp_f32 v34, v50\n"
      "v_exp_f32 v35, v51\n"
      "v_cvt_pk_bf16_f32 v166, v32, v33\n"
      "v_cvt_pk_bf16_f32 v167, v34, v35\n"
      "s_add_u32 s43, s45, 24576\n"
      "s_sub_u32 s43, s43, s65\n"
      "v_add_u32 v232, s43, v6\n"
      "v_add_u32 v233, s43, v7\n"
      "s_waitcnt vmcnt(6)\n"
      "ds_read_b128 a[24:27], v232 offset:0\n"
      "ds_read_b128 a[28:31], v233 offset:0\n"
      "ds_read_b128 a[32:35], v232 offset:128\n"
      "ds_read_b128 a[36:39], v233 offset:128\n"
      "ds_read_b128 a[40:43], v232 offset:256\n"
      "ds_read_b128 a[44:47], v233 offset:256\n"
      "s_mov_b32 m0, s87\n"
      "s_add_u32 s63, s55, s88\n"
      "buffer_load_dwordx4 v1, s[28:31], s63 offen offset:0 nt lds\n"
      "buffer_load_dwordx4 v2, s[28:31], s63 offen offset:1024 nt lds\n"
      "buffer_load_dwordx4 v3, s[28:31], s63 offen offset:2048 nt lds\n"
      "s_add_u32 m0, s44, 24576\n"
      "s_add_u32 s63, s37, 0\n"
      "buffer_load_dwordx4 v1, s[20:23], s63 offen offset:0 lds\n"
      "buffer_load_dwordx4 v2, s[20:23], s63 offen offset:1024 lds\n"
      "buffer_load_dwordx4 v3, s[20:23], s63 offen offset:2048 lds\n"
      "s_waitcnt lgkmcnt(0)\n"
      "v_mfma_f32_16x16x32_bf16 v[36:39], a[208:211], a[24:27], 0\n"
      "s_add_u32 m0, s44, 27648\n"
      "ds_read_b128 a[232:235], v4 offset:6144\n"
      "ds_read_b128 a[236:239], v5 offset:6144\n"
      "v_mfma_f32_16x16x32_bf16 v[36:39], a[212:215], a[28:31], v[36:39]\n"
      "s_add_u32 s63, s37, 3072\n"
      "ds_read_b128 a[240:243], v4 offset:6272\n"
      "ds_read_b128 a[244:247], v5 offset:6272\n"
      "v_mfma_f32_16x16x32_bf16 v[36:39], a[216:219], a[32:35], v[36:39]\n"
      "ds_read_b128 a[248:251], v4 offset:6400\n"
      "ds_read_b128 a[252:255], v5 offset:6400\n"
      "v_mfma_f32_16x16x32_bf16 v[36:39], a[220:223], a[36:39], v[36:39]\n"
      "buffer_load_dwordx4 v1, s[20:23], s63 offen offset:0 lds\n"
      "v_mfma_f32_16x16x32_bf16 v[36:39], a[224:227], a[40:43], v[36:39]\n"
      "v_mfma_f32_16x16x32_bf16 v[36:39], a[228:231], a[44:47], v[36:39]\n"
      "s_waitcnt lgkmcnt(4)\n"
      "buffer_load_dwordx4 v2, s[20:23], s63 offen offset:1024 lds\n"
      "v_mfma_f32_16x16x32_bf16 v[40:43], a[232:235], a[24:27], 0\n"
      "ds_read_b128 a[208:211], v4 offset:12288\n"
      "ds_read_b128 a[212:215], v5 offset:12288\n"
      "v_mfma_f32_16x16x32_bf16 v[40:43], a[236:239], a[28:31], v[40:43]\n"
      "s_waitcnt lgkmcnt(4)\n"
      "buffer_load_dwordx4 v3, s[20:23], s63 offen offset:2048 lds\n"
      "v_mfma_f32_16x16x32_bf16 v[40:43], a[240:243], a[32:35], v[40:43]\n"
      "ds_read_b128 a[216:219], v4 offset:12416\n"
      "ds_read_b128 a[220:223], v5 offset:12416\n"
      "v_mfma_f32_16x16x32_bf16 v[40:43], a[244:247], a[36:39], v[40:43]\n"
      "s_waitcnt lgkmcnt(4)\n"
      "ds_read_b128 a[224:227], v4 offset:12544\n"
      "ds_read_b128 a[228:231], v5 offset:12544\n"
      "v_mfma_f32_16x16x32_bf16 v[40:43], a[248:251], a[40:43], v[40:43]\n"
      "v_max3_f32 v52, v36, v37, v38\n"
      "v_max_f32 v52, v52, v39\n"
      "v_mfma_f32_16x16x32_bf16 v[40:43], a[252:255], a[44:47], v[40:43]\n"
      "s_waitcnt lgkmcnt(4)\n"
      "v_mfma_f32_16x16x32_bf16 v[44:47], a[208:211], a[24:27], 0\n"
      "ds_read_b128 a[232:235], v4 offset:18432\n"
      "ds_read_b128 a[236:239], v5 offset:18432\n"
      "v_mfma_f32_16x16x32_bf16 v[44:47], a[212:215], a[28:31], v[44:47]\n"
      "s_waitcnt lgkmcnt(4)\n"
      "ds_read_b128 a[240:243], v4 offset:18560\n"
      "ds_read_b128 a[244:247], v5 offset:18560\n"
      "v_mfma_f32_16x16x32_bf16 v[44:47], a[216:219], a[32:35], v[44:47]\n"
      "ds_read_b128 a[248:251], v4 offset:18688\n"
      "ds_read_b128 a[252:255], v5 offset:18688\n"
      "v_mfma_f32_16x16x32_bf16 v[44:47], a[220:223], a[36:39], v[44:47]\n"
      "s_waitcnt lgkmcnt(6)\n"
      "v_max3_f32 v52, v52, v40, v41\n"
      "v_max3_f32 v52, v52, v42, v43\n"
      "v_mfma_f32_16x16x32_bf16 v[44:47], a[224:227], a[40:43], v[44:47]\n"
      "v_mfma_f32_16x16x32_bf16 v[44:47], a[228:231], a[44:47], v[44:47]\n"
      "s_waitcnt lgkmcnt(4)\n"
      "v_mfma_f32_16x16x32_bf16 v[48:51], a[232:235], a[24:27], 0\n"
      "ds_read_b128 a[208:211], v4 offset:0\n"
      "ds_read_b128 a[212:215], v5 offset:0\n"
      "v_mfma_f32_16x16x32_bf16 v[48:51], a[236:239], a[28:31], v[48:51]\n"
      "s_waitcnt lgkmcnt(4)\n"
      "ds_read_b128 a[216:219], v4 offset:128\n"
      "ds_read_b128 a[220:223], v5 offset:128\n"
      "v_mfma_f32_16x16x32_bf16 v[48:51], a[240:243], a[32:35], v[48:51]\n"
      "ds_read_b128 a[224:227], v4 offset:256\n"
      "ds_read_b128 a[228:231], v5 offset:256\n"
      "v_mfma_f32_16x16x32_bf16 v[48:51], a[244:247], a[36:39], v[48:51]\n"
      "s_waitcnt lgkmcnt(6)\n"
      "v_max3_f32 v52, v52, v44, v45\n"
      "v_max3_f32 v52, v52, v46, v47\n"
      "v_mfma_f32_16x16x32_bf16 v[48:51], a[248:251], a[40:43], v[48:51]\n"
      "v_mfma_f32_16x16x32_bf16 v[48:51], a[252:255], a[44:47], v[48:51]\n"
      "s_nop 7\n"
      "v_max3_f32 v52, v52, v48, v49\n"
      "v_max3_f32 v52, v52, v50, v51\n"
      "v_mov_b32 v53, v52\n"
      "s_nop 1\n"
      "v_permlane32_swap_b32 v52, v53\n"
      "v_max_f32 v52, v52, v53\n"
      "v_mov_b32 v53, v52\n"
      "s_nop 1\n"
      "v_permlane16_swap_b32 v52, v53\n"
      "v_max_f32 v52, v52, v53\n"
      "v_mul_f32 v52, s62, v52\n"
      "v_add_f32 v19, 0xc1800000, v52\n"
      "v_fma_f32 v36, v36, s48, v19\n"
      "v_fma_f32 v37, v37, s48, v19\n"
      "v_fma_f32 v38, v38, s48, v19\n"
      "v_fma_f32 v39, v39, s48, v19\n"
      "v_exp_f32 v28, v36\n"
      "v_exp_f32 v29, v37\n"
      "v_exp_f32 v30, v38\n"
      "v_exp_f32 v31, v39\n"
      "v_cvt_pk_bf16_f32 v168, v28, v29\n"
      "v_cvt_pk_bf16_f32 v169, v30, v31\n"
      "v_fma_f32 v40, v40, s48, v19\n"
      "v_fma_f32 v41, v41, s48, v19\n"
      "v_fma_f32 v42, v42, s48, v19\n"
      "v_fma_f32 v43, v43, s48, v19\n"
      "v_exp_f32 v32, v40\n"
      "v_exp_f32 v33, v41\n"
      "v_exp_f32 v34, v42\n"
      "v_exp_f32 v35, v43\n"
      "v_cvt_pk_bf16_f32 v170, v32, v33\n"
      "v_cvt_pk_bf16_f32 v171, v34, v35\n"
      "v_fma_f32 v44, v44, s48, v19\n"
      "v_fma_f32 v45, v45, s48, v19\n"
      "v_fma_f32 v46, v46, s48, v19\n"
      "v_fma_f32 v47, v47, s48, v19\n"
      "v_exp_f32 v28, v44\n"
      "v_exp_f32 v29, v45\n"
      "v_exp_f32 v30, v46\n"
      "v_exp_f32 v31, v47\n"
      "v_cvt_pk_bf16_f32 v172, v28, v29\n"
      "v_cvt_pk_bf16_f32 v173, v30, v31\n"
      "v_fma_f32 v48, v48, s48, v19\n"
      "v_fma_f32 v49, v49, s48, v19\n"
      "v_fma_f32 v50, v50, s48, v19\n"
      "v_fma_f32 v51, v51, s48, v19\n"
      "v_exp_f32 v32, v48\n"
      "v_exp_f32 v33, v49\n"
      "v_exp_f32 v34, v50\n"
      "v_exp_f32 v35, v51\n"
      "v_cvt_pk_bf16_f32 v174, v32, v33\n"
      "v_cvt_pk_bf16_f32 v175, v34, v35\n"
      "s_add_u32 s43, s45, 49152\n"
      "s_sub_u32 s43, s43, s65\n"
      "v_add_u32 v232, s43, v6\n"
      "v_add_u32 v233, s43, v7\n"
      "s_waitcnt vmcnt(9)\n"
      "ds_read_b128 a[48:51], v232 offset:0\n"
      "ds_read_b128 a[52:55], v233 offset:0\n"
      "ds_read_b128 a[56:59], v232 offset:128\n"
      "ds_read_b128 a[60:63], v233 offset:128\n"
      "ds_read_b128 a[64:67], v232 offset:256\n"
      "ds_read_b128 a[68:71], v233 offset:256\n"
      "s_add_u32 m0, s45, 0\n"
      "s_add_u32 s63, s36, 0\n"
      "buffer_load_dwordx4 v1, s[24:27], s63 offen offset:0 lds\n"
      "buffer_load_dwordx4 v2, s[24:27], s63 offen offset:1024 lds\n"
      "buffer_load_dwordx4 v3, s[24:27], s63 offen offset:2048 lds\n"
      "s_add_u32 m0, s45, 3072\n"
      "s_add_u32 s63, s36, 3072\n"
      "buffer_load_dwordx4 v1, s[24:27], s63 offen offset:0 lds\n"
      "buffer_load_dwordx4 v2, s[24:27], s63 offen offset:1024 lds\n"
      "buffer_load_dwordx4 v3, s[24:27], s63 offen offset:2048 lds\n"
      "s_waitcnt lgkmcnt(0)\n"
      "v_mfma_f32_16x16x32_bf16 v[36:39], a[208:211], a[48:51], 0\n"
      "ds_read_b128 a[232:235], v4 offset:6144\n"
      "ds_read_b128 a[236:239], v5 offset:6144\n"
      "v_mfma_f32_16x16x32_bf16 v[36:39], a[212:215], a[52:55], v[36:39]\n"
      "ds_read_b128 a[240:243], v4 offset:6272\n"
      "ds_read_b128 a[244:247], v5 offset:6272\n"
      "v_mfma_f32_16x16x32_bf16 v[36:39], a[216:219], a[56:59], v[36:39]\n"
      "ds_read_b128 a[248:251], v4 offset:6400\n"
      "ds_read_b128 a[252:255], v5 offset:6400\n"
      "v_mfma_f32_16x16x32_bf16 v[36:39], a[220:223], a[60:63], v[36:39]\n"
      "v_mfma_f32_16x16x32_bf16 v[36:39], a[224:227], a[64:67], v[36:39]\n"
      "v_mfma_f32_16x16x32_bf16 v[36:39], a[228:231], a[68:71], v[36:39]\n"
      "s_waitcnt lgkmcnt(4)\n"
      "v_mfma_f32_16x16x32_bf16 v[40:43], a[232:235], a[48:51], 0\n"
      "ds_read_b128 a[208:211], v4 offset:12288\n"
      "ds_read_b128 a[212:215], v5 offset:12288\n"
      "v_mfma_f32_16x16x32_bf16 v[40:43], a[236:239], a[52:55], v[40:43]\n"
      "s_waitcnt lgkmcnt(4)\n"
      "ds_read_b128 a[216:219], v4 offset:12416\n"
      "ds_read_b128 a[220:223], v5 offset:12416\n"
      "v_mfma_f32_16x16x32_bf16 v[40:43], a[240:243], a[56:59], v[40:43]\n"
      "ds_read_b128 a[224:227], v4 offset:12544\n"
      "ds_read_b128 a[228:231], v5 offset:12544\n"
      "v_mfma_f32_16x16x32_bf16 v[40:43], a[244:247], a[60:63], v[40:43]\n"
      "s_waitcnt lgkmcnt(6)\n"
      "v_max3_f32 v52, v36, v37, v38\n"
      "v_max_f32 v52, v52, v39\n"
      "v_mfma_f32_16x16x32_bf16 v[40:43], a[248:251], a[64:67], v[40:43]\n"
      "v_mfma_f32_16x16x32_bf16 v[40:43], a[252:255], a[68:71], v[40:43]\n"
      "s_waitcnt lgkmcnt(4)\n"
      "v_mfma_f32_16x16x32_bf16 v[44:47], a[208:211], a[48:51], 0\n"
      "ds_read_b128 a[232:235], v4 offset:18432\n"
      "ds_read_b128 a[236:239], v5 offset:18432\n"
      "v_mfma_f32_16x16x32_bf16 v[44:47], a[212:215], a[52:55], v[44:47]\n"
      "s_waitcnt lgkmcnt(4)\n"
      "ds_read_b128 a[240:243], v4 offset:18560\n"
      "ds_read_b128 a[244:247], v5 offset:18560\n"
      "v_mfma_f32_16x16x32_bf16 v[44:47], a[216:219], a[56:59], v[44:47]\n"
      "ds_read_b128 a[248:251], v4 offset:18688\n"
      "ds_read_b128 a[252:255], v5 offset:18688\n"
      "v_mfma_f32_16x16x32_bf16 v[44:47], a[220:223], a[60:63], v[44:47]\n"
      "s_waitcnt lgkmcnt(6)\n"
      "v_max3_f32 v52, v52, v40, v41\n"
      "v_max3_f32 v52, v52, v42, v43\n"
      "v_mfma_f32_16x16x32_bf16 v[44:47], a[224:227], a[64:67], v[44:47]\n"
      "v_mfma_f32_16x16x32_bf16 v[44:47], a[228:231], a[68:71], v[44:47]\n"
      "s_waitcnt lgkmcnt(4)\n"
      "v_mfma_f32_16x16x32_bf16 v[48:51], a[232:235], a[48:51], 0\n"
      "ds_read_b128 a[208:211], v4 offset:12288\n"
      "ds_read_b128 a[212:215], v5 offset:12288\n"
      "v_mfma_f32_16x16x32_bf16 v[48:51], a[236:239], a[52:55], v[48:51]\n"
      "s_waitcnt lgkmcnt(4)\n"
      "ds_read_b128 a[216:219], v4 offset:12416\n"
      "ds_read_b128 a[220:223], v5 offset:12416\n"
      "v_mfma_f32_16x16x32_bf16 v[48:51], a[240:243], a[56:59], v[48:51]\n"
      "ds_read_b128 a[224:227], v4 offset:12544\n"
      "ds_read_b128 a[228:231], v5 offset:12544\n"
      "v_mfma_f32_16x16x32_bf16 v[48:51], a[244:247], a[60:63], v[48:51]\n"
      "s_waitcnt lgkmcnt(6)\n"
      "v_max3_f32 v52, v52, v44, v45\n"
      "v_max3_f32 v52, v52, v46, v47\n"
      "v_mfma_f32_16x16x32_bf16 v[48:51], a[248:251], a[64:67], v[48:51]\n"
      "v_mfma_f32_16x16x32_bf16 v[48:51], a[252:255], a[68:71], v[48:51]\n"
      "s_nop 7\n"
      "v_max3_f32 v52, v52, v48, v49\n"
      "v_max3_f32 v52, v52, v50, v51\n"
      "v_mov_b32 v53, v52\n"
      "s_nop 1\n"
      "v_permlane32_swap_b32 v52, v53\n"
      "v_max_f32 v52, v52, v53\n"
      "v_mov_b32 v53, v52\n"
      "s_nop 1\n"
      "v_permlane16_swap_b32 v52, v53\n"
      "v_max_f32 v52, v52, v53\n"
      "v_mul_f32 v52, s62, v52\n"
      "v_add_f32 v20, 0xc1800000, v52\n"
      "v_fma_f32 v36, v36, s48, v20\n"
      "v_fma_f32 v37, v37, s48, v20\n"
      "v_fma_f32 v38, v38, s48, v20\n"
      "v_fma_f32 v39, v39, s48, v20\n"
      "v_exp_f32 v28, v36\n"
      "v_exp_f32 v29, v37\n"
      "v_exp_f32 v30, v38\n"
      "v_exp_f32 v31, v39\n"
      "v_cvt_pk_bf16_f32 v176, v28, v29\n"
      "v_cvt_pk_bf16_f32 v177, v30, v31\n"
      "v_fma_f32 v40, v40, s48, v20\n"
      "v_fma_f32 v41, v41, s48, v20\n"
      "v_fma_f32 v42, v42, s48, v20\n"
      "v_fma_f32 v43, v43, s48, v20\n"
      "v_exp_f32 v32, v40\n"
      "v_exp_f32 v33, v41\n"
      "v_exp_f32 v34, v42\n"
      "v_exp_f32 v35, v43\n"
      "v_cvt_pk_bf16_f32 v178, v32, v33\n"
      "v_cvt_pk_bf16_f32 v179, v34, v35\n"
      "v_fma_f32 v44, v44, s48, v20\n"
      "v_fma_f32 v45, v45, s48, v20\n"
      "v_fma_f32 v46, v46, s48, v20\n"
      "v_fma_f32 v47, v47, s48, v20\n"
      "v_exp_f32 v28, v44\n"
      "v_exp_f32 v29, v45\n"
      "v_exp_f32 v30, v46\n"
      "v_exp_f32 v31, v47\n"
      "v_cvt_pk_bf16_f32 v180, v28, v29\n"
      "v_cvt_pk_bf16_f32 v181, v30, v31\n"
      "v_fma_f32 v48, v48, s48, v20\n"
      "v_fma_f32 v49, v49, s48, v20\n"
      "v_fma_f32 v50, v50, s48, v20\n"
      "v_fma_f32 v51, v51, s48, v20\n"
      "v_exp_f32 v32, v48\n"
      "v_exp_f32 v33, v49\n"
      "v_exp_f32 v34, v50\n"
      "v_exp_f32 v35, v51\n"
      "v_cvt_pk_bf16_f32 v182, v32, v33\n"
      "v_cvt_pk_bf16_f32 v183, v34, v35\n"
      "v_add_u32 v232, s89, v6\n"
      "v_add_u32 v233, s89, v7\n"
      "s_waitcnt vmcnt(12)\n"
      "s_barrier\n"
      "ds_read_b128 a[72:75], v232 offset:0\n"
      "ds_read_b128 a[76:79], v233 offset:0\n"
      "ds_read_b128 a[80:83], v232 offset:128\n"
      "ds_read_b128 a[84:87], v233 offset:128\n"
      "ds_read_b128 a[88:91], v232 offset:256\n"
      "ds_read_b128 a[92:95], v233 offset:256\n"
      "s_waitcnt lgkmcnt(0)\n"
      "v_mfma_f32_16x16x32_bf16 v[44:47], a[208:211], a[72:75], 0\n"
      "ds_read_b128 a[232:235], v4 offset:18432\n"
      "ds_read_b128 a[236:239], v5 offset:18432\n"
      "v_mfma_f32_16x16x32_bf16 v[44:47], a[212:215], a[76:79], v[44:47]\n"
      "ds_read_b128 a[240:243], v4 offset:18560\n"
      "ds_read_b128 a[244:247], v5 offset:18560\n"
      "v_mfma_f32_16x16x32_bf16 v[44:47], a[216:219], a[80:83], v[44:47]\n"
      "ds_read_b128 a[248:251], v4 offset:18688\n"
      "ds_read_b128 a[252:255], v5 offset:18688\n"
      "v_mfma_f32_16x16x32_bf16 v[44:47], a[220:223], a[84:87], v[44:47]\n"
      "v_mfma_f32_16x16x32_bf16 v[44:47], a[224:227], a[88:91], v[44:47]\n"
      "v_mfma_f32_16x16x32_bf16 v[44:47], a[228:231], a[92:95], v[44:47]\n"
      "s_waitcnt lgkmcnt(4)\n"
      "v_mfma_f32_16x16x32_bf16 v[48:51], a[232:235], a[72:75], 0\n"
      "s_nop 2\n"
      "v_mfma_f32_16x16x32_bf16 v[48:51], a[236:239], a[76:79], v[48:51]\n"
      "s_waitcnt lgkmcnt(2)\n"
      "v_mfma_f32_16x16x32_bf16 v[48:51], a[240:243], a[80:83], v[48:51]\n"
      "v_max3_f32 v52, v44, v45, v46\n"
      "v_max_f32 v52, v52, v47\n"
      "v_mfma_f32_16x16x32_bf16 v[48:51], a[244:247], a[84:87], v[48:51]\n"
      "s_waitcnt lgkmcnt(0)\n"
      "v_mfma_f32_16x16x32_bf16 v[48:51], a[248:251], a[88:91], v[48:51]\n"
      "v_mfma_f32_16x16x32_bf16 v[48:51], a[252:255], a[92:95], v[48:51]\n"
      "s_nop 7\n"
      "v_max3_f32 v52, v52, v48, v49\n"
      "v_max3_f32 v52, v52, v50, v51\n"
      "v_mov_b32 v53, v52\n"
      "s_nop 1\n"
      "v_permlane32_swap_b32 v52, v53\n"
      "v_max_f32 v52, v52, v53\n"
      "v_mov_b32 v53, v52\n"
      "s_nop 1\n"
      "v_permlane16_swap_b32 v52, v53\n"
      "v_max_f32 v52, v52, v53\n"
      "v_mul_f32 v52, s62, v52\n"
      "v_add_f32 v21, 0xc1800000, v52\n"
      "v_fma_f32 v44, v44, s48, v21\n"
      "v_fma_f32 v45, v45, s48, v21\n"
      "v_fma_f32 v46, v46, s48, v21\n"
      "v_fma_f32 v47, v47, s48, v21\n"
      "v_exp_f32 v28, v44\n"
      "v_exp_f32 v29, v45\n"
      "v_exp_f32 v30, v46\n"
      "v_exp_f32 v31, v47\n"
      "v_cvt_pk_bf16_f32 v184, v28, v29\n"
      "v_cvt_pk_bf16_f32 v185, v30, v31\n"
      "v_fma_f32 v48, v48, s48, v21\n"
      "v_fma_f32 v49, v49, s48, v21\n"
      "v_fma_f32 v50, v50, s48, v21\n"
      "v_fma_f32 v51, v51, s48, v21\n"
      "v_exp_f32 v32, v48\n"
      "v_exp_f32 v33, v49\n"
      "v_exp_f32 v34, v50\n"
      "v_exp_f32 v35, v51\n"
      "v_cvt_pk_bf16_f32 v186, v32, v33\n"
      "v_cvt_pk_bf16_f32 v187, v34, v35\n"
      "s_add_u32 m0, s44, 49152\n"
      "s_add_u32 s63, s38, 0\n"
      "buffer_load_dwordx4 v1, s[20:23], s63 offen offset:0 lds\n"
      "buffer_load_dwordx4 v2, s[20:23], s63 offen offset:1024 lds\n"
      "buffer_load_dwordx4 v3, s[20:23], s63 offen offset:2048 lds\n"
      "s_add_u32 m0, s44, 52224\n"
      "s_add_u32 s63, s38, 3072\n"
      "buffer_load_dwordx4 v1, s[20:23], s63 offen offset:0 lds\n"
      "buffer_load_dwordx4 v2, s[20:23], s63 offen offset:1024 lds\n"
      "buffer_load_dwordx4 v3, s[20:23], s63 offen offset:2048 lds\n"
      "s_add_u32 m0, s45, 24576\n"
      "s_add_u32 s63, s37, 0\n"
      "buffer_load_dwordx4 v1, s[24:27], s63 offen offset:0 lds\n"
      "buffer_load_dwordx4 v2, s[24:27], s63 offen offset:1024 lds\n"
      "buffer_load_dwordx4 v3, s[24:27], s63 offen offset:2048 lds\n"
      "s_add_u32 m0, s45, 27648\n"
      "s_add_u32 s63, s37, 3072\n"
      "buffer_load_dwordx4 v1, s[24:27], s63 offen offset:0 lds\n"
      "buffer_load_dwordx4 v2, s[24:27], s63 offen offset:1024 lds\n"
      "buffer_load_dwordx4 v3, s[24:27], s63 offen offset:2048 lds\n"
      "v_add_u32 v232, 24576, v6\n"
      "v_add_u32 v233, 24576, v7\n"
      "v_mov_b32 v234, v12\n"
      "v_mov_b32 v235, v13\n"
      "v_mov_b32 v236, v14\n"
      "v_mov_b32 v237, v15\n"
      "s_add_u32 s39, s38, 3072\n"
      "s_mov_b32 s12, 3\n"
      "s_add_u32 s15, s13, 3\n"
      "s_and_b32 s15, s15, 15\n"
      "s_mul_i32 s15, s15, 24576\n"
      "s_add_u32 s36, s15, s14\n"
      "s_add_u32 s37, s36, 3072\n"
      "s_mov_b32 s40, 24576\n"
      "s_mov_b32 s41, 49152\n"
      "s_mov_b32 s42, 0\n"
      "s_waitcnt vmcnt(12)\n"
      "s_barrier\n"
      "ds_read_b128 a[208:211], v232 offset:0\n"
      "ds_read_b128 a[212:215], v233 offset:0\n"
      "ds_read_b128 a[216:219], v232 offset:128\n"
      "ds_read_b128 a[220:223], v233 offset:128\n"
      "ds_read_b128 a[224:227], v232 offset:256\n"
      "ds_read_b128 a[228:231], v233 offset:256\n"
      "s_waitcnt lgkmcnt(0)\n"
      "s_branch L_loop_O\n"
      ".p2align 6\n"
      "L_loop_O:\n"
      "v_mfma_f32_16x16x32_bf16 v[36:39], a[208:211], a[0:3], 0\n"
      "s_add_u32 s46, s44, s42\n"
      "ds_read_b64_tr_b16 a[232:233], v234 offset:0\n"
      "v_add_u32 v4, s41, v6\n"
      "v_mfma_f32_16x16x32_bf16 v[36:39], a[212:215], a[4:7], v[36:39]\n"
      "s_add_u32 s47, s45, s41\n"
      "ds_read_b64_tr_b16 a[234:235], v234 offset:6144\n"
      "v_add_u32 v5, s41, v7\n"
      "v_mfma_f32_16x16x32_bf16 v[36:39], a[216:219], a[8:11], v[36:39]\n"
      "ds_read_b64_tr_b16 a[236:237], v234 offset:12288\n"
      "v_add_u32 v8, s40, v12\n"
      "v_mfma_f32_16x16x32_bf16 v[36:39], a[220:223], a[12:15], v[36:39]\n"
      "s_mov_b32 m0, s46\n"
      "ds_read_b64_tr_b16 a[238:239], v234 offset:18432\n"
      "v_add_u32 v9, s40, v13\n"
      "v_mfma_f32_16x16x32_bf16 v[36:39], a[224:227], a[16:19], v[36:39]\n"
      "buffer_load_dwordx4 v1, s[20:23], s36 offen offset:0 lds\n"
      "v_mfma_f32_16x16x32_bf16 v[36:39], a[228:231], a[20:23], v[36:39]\n"
      "ds_read_b64_tr_b16 a[240:241], v235 offset:0\n"
      "v_add_u32 v10, s40, v14\n"
      "v_mfma_f32_16x16x32_bf16 v[40:43], a[228:231], a[44:47], 0\n"
      "ds_read_b64_tr_b16 a[242:243], v235 offset:6144\n"
      "v_add_u32 v11, s40, v15\n"
      "v_mfma_f32_16x16x32_bf16 v[40:43], a[224:227], a[40:43], v[40:43]\n"
      "s_mov_b32 s43, s40\n"
      "ds_read_b64_tr_b16 a[244:245], v235 offset:12288\n"
      "v_mfma_f32_16x16x32_bf16 v[40:43], a[220:223], a[36:39], v[40:43]\n"
      "s_mov_b32 s40, s41\n"
      "ds_read_b64_tr_b16 a[246:247], v235 offset:18432\n"
      "v_fma_f32 v36, v36, s48, v18\n"
      "v_mfma_f32_16x16x32_bf16 v[40:43], a[216:219], a[32:35], v[40:43]\n"
      "s_mov_b32 s41, s42\n"
      "ds_read_b64_tr_b16 a[248:249], v236 offset:0\n"
      "v_fma_f32 v37, v37, s48, v18\n"
      "v_mfma_f32_16x16x32_bf16 v[40:43], a[212:215], a[28:31], v[40:43]\n"
      "s_mov_b32 s42, s43\n"
      "ds_read_b64_tr_b16 a[250:251], v236 offset:6144\n"
      "v_fma_f32 v38, v38, s48, v18\n"
      "v_mfma_f32_16x16x32_bf16 v[40:43], a[208:211], a[24:27], v[40:43]\n"
      "ds_read_b64_tr_b16 a[252:253], v236 offset:12288\n"
      "v_fma_f32 v39, v39, s48, v18\n"
      "v_mfma_f32_16x16x32_bf16 v[44:47], a[208:211], a[48:51], 0\n"
      "ds_read_b64_tr_b16 a[254:255], v236 offset:18432\n"
      "v_mfma_f32_16x16x32_bf16 v[44:47], a[212:215], a[52:55], v[44:47]\n"
      "buffer_load_dwordx4 v2, s[20:23], s36 offen offset:1024 lds\n"
      "v_mfma_f32_16x16x32_bf16 v[44:47], a[216:219], a[56:59], v[44:47]\n"
      "buffer_load_dwordx4 v3, s[20:23], s36 offen offset:2048 lds\n"
      "v_mfma_f32_16x16x32_bf16 v[44:47], a[220:223], a[60:63], v[44:47]\n"
      "v_exp_f32 v28, v36\n"
      "v_mfma_f32_16x16x32_bf16 v[44:47], a[224:227], a[64:67], v[44:47]\n"
      "v_exp_f32 v29, v37\n"
      "v_mfma_f32_16x16x32_bf16 v[44:47], a[228:231], a[68:71], v[44:47]\n"
      "s_waitcnt lgkmcnt(0)\n"
      "v_exp_f32 v30, v38\n"
      "v_smfmac_f32_16x16x64_bf16 a[192:195], v[24:27], v[160:167], v16\n"
      "ds_read_b128 a[208:211], v232 offset:6144\n"
      "v_cvt_pk_bf16_f32 v188, v28, v29\n"
      "v_mfma_f32_16x16x32_bf16 v[64:67], v[160:163], a[232:235], v[64:67]\n"
      "v_exp_f32 v31, v39\n"
      "v_mfma_f32_16x16x32_bf16 v[64:67], v[164:167], a[236:239], v[64:67]\n"
      "ds_read_b128 a[212:215], v233 offset:6144\n"
      "v_cvt_pk_bf16_f32 v189, v30, v31\n"
      "v_mfma_f32_16x16x32_bf16 v[112:115], v[172:175], a[236:239], v[112:115]\n"
      "v_fma_f32 v40, v40, s48, v19\n"
      "v_fma_f32 v41, v41, s48, v19\n"
      "v_mfma_f32_16x16x32_bf16 v[112:115], v[168:171], a[232:235], v[112:115]\n"
      "ds_read_b128 a[216:219], v232 offset:6272\n"
      "v_fma_f32 v42, v42, s48, v19\n"
      "v_mfma_f32_16x16x32_bf16 a[96:99], v[176:179], a[232:235], a[96:99]\n"
      "v_exp_f32 v32, v40\n"
      "v_mfma_f32_16x16x32_bf16 a[96:99], v[180:183], a[236:239], a[96:99]\n"
      "ds_read_b128 a[220:223], v233 offset:6272\n"
      "v_fma_f32 v43, v43, s48, v19\n"
      "v_mfma_f32_16x16x32_bf16 a[144:147], v[184:187], a[236:239], a[144:147]\n"
      "v_exp_f32 v33, v41\n"
      "v_mfma_f32_16x16x32_bf16 v[68:71], v[160:163], a[240:243], v[68:71]\n"
      "ds_read_b128 a[224:227], v232 offset:6400\n"
      "v_cvt_pk_bf16_f32 v196, v32, v33\n"
      "v_mfma_f32_16x16x32_bf16 v[68:71], v[164:167], a[244:247], v[68:71]\n"
      "ds_read_b64_tr_b16 a[232:233], v237 offset:0\n"
      "v_or3_b32 v22, v188, v189, v196\n"
      "v_mfma_f32_16x16x32_bf16 v[116:119], v[172:175], a[244:247], v[116:119]\n"
      "ds_read_b128 a[228:231], v233 offset:6400\n"
      "v_fma_f32 v44, v44, s48, v20\n"
      "v_mfma_f32_16x16x32_bf16 v[116:119], v[168:171], a[240:243], v[116:119]\n"
      "ds_read_b64_tr_b16 a[234:235], v237 offset:6144\n"
      "v_fma_f32 v45, v45, s48, v20\n"
      "v_mfma_f32_16x16x32_bf16 a[100:103], v[176:179], a[240:243], a[100:103]\n"
      "ds_read_b64_tr_b16 a[236:237], v237 offset:12288\n"
      "v_fma_f32 v46, v46, s48, v20\n"
      "v_mfma_f32_16x16x32_bf16 a[100:103], v[180:183], a[244:247], a[100:103]\n"
      "ds_read_b64_tr_b16 a[238:239], v237 offset:18432\n"
      "v_fma_f32 v47, v47, s48, v20\n"
      "v_mfma_f32_16x16x32_bf16 a[148:151], v[184:187], a[244:247], a[148:151]\n"
      "v_exp_f32 v34, v42\n"
      "v_mfma_f32_16x16x32_bf16 v[72:75], v[160:163], a[248:251], v[72:75]\n"
      "v_exp_f32 v35, v43\n"
      "v_mfma_f32_16x16x32_bf16 v[72:75], v[164:167], a[252:255], v[72:75]\n"
      "ds_read_b64_tr_b16 a[240:241], v234 offset:128\n"
      "v_cvt_pk_bf16_f32 v197, v34, v35\n"
      "v_mfma_f32_16x16x32_bf16 v[120:123], v[172:175], a[252:255], v[120:123]\n"
      "v_exp_f32 v28, v44\n"
      "v_mfma_f32_16x16x32_bf16 v[120:123], v[168:171], a[248:251], v[120:123]\n"
      "v_exp_f32 v29, v45\n"
      "v_mfma_f32_16x16x32_bf16 a[104:107], v[176:179], a[248:251], a[104:107]\n"
      "ds_read_b64_tr_b16 a[242:243], v234 offset:6272\n"
      "v_cvt_pk_bf16_f32 v204, v28, v29\n"
      "v_mfma_f32_16x16x32_bf16 a[104:107], v[180:183], a[252:255], a[104:107]\n"
      "ds_read_b64_tr_b16 a[244:245], v234 offset:12416\n"
      "v_or3_b32 v22, v22, v197, v204\n"
      "v_mfma_f32_16x16x32_bf16 a[152:155], v[184:187], a[252:255], a[152:155]\n"
      "s_waitcnt lgkmcnt(6)\n"
      "v_exp_f32 v30, v46\n"
      "v_mfma_f32_16x16x32_bf16 v[216:219], a[208:211], a[0:3], 0\n"
      "v_exp_f32 v31, v47\n"
      "v_mfma_f32_16x16x32_bf16 v[216:219], a[212:215], a[4:7], v[216:219]\n"
      "ds_read_b64_tr_b16 a[246:247], v234 offset:18560\n"
      "v_cvt_pk_bf16_f32 v205, v30, v31\n"
      "v_mfma_f32_16x16x32_bf16 v[216:219], a[216:219], a[8:11], v[216:219]\n"
      "ds_read_b64_tr_b16 a[248:249], v235 offset:128\n"
      "v_mfma_f32_16x16x32_bf16 v[216:219], a[220:223], a[12:15], v[216:219]\n"
      "s_add_u32 m0, s46, 3072\n"
      "ds_read_b64_tr_b16 a[250:251], v235 offset:6272\n"
      "v_mfma_f32_16x16x32_bf16 v[216:219], a[224:227], a[16:19], v[216:219]\n"
      "buffer_load_dwordx4 v1, s[20:23], s37 offen offset:0 lds\n"
      "v_mfma_f32_16x16x32_bf16 v[216:219], a[228:231], a[20:23], v[216:219]\n"
      "buffer_load_dwordx4 v2, s[20:23], s37 offen offset:1024 lds\n"
      "v_mfma_f32_16x16x32_bf16 v[220:223], a[228:231], a[44:47], 0\n"
      "buffer_load_dwordx4 v3, s[20:23], s37 offen offset:2048 lds\n"
      "v_mfma_f32_16x16x32_bf16 v[220:223], a[224:227], a[40:43], v[220:223]\n"
      "s_nop 0\n"
      "ds_read_b64_tr_b16 a[252:253], v235 offset:12416\n"
      "v_mfma_f32_16x16x32_bf16 v[220:223], a[220:223], a[36:39], v[220:223]\n"
      "ds_read_b64_tr_b16 a[254:255], v235 offset:18560\n"
      "v_fma_f32 v216, v216, s48, v18\n"
      "v_mfma_f32_16x16x32_bf16 v[220:223], a[216:219], a[32:35], v[220:223]\n"
      "v_exp_f32 v32, v216\n"
      "v_mfma_f32_16x16x32_bf16 v[220:223], a[212:215], a[28:31], v[220:223]\n"
      "v_fma_f32 v217, v217, s48, v18\n"
      "v_fma_f32 v218, v218, s48, v18\n"
      "v_mfma_f32_16x16x32_bf16 v[220:223], a[208:211], a[24:27], v[220:223]\n"
      "v_exp_f32 v33, v217\n"
      "v_mfma_f32_16x16x32_bf16 v[224:227], a[208:211], a[48:51], 0\n"
      "v_exp_f32 v34, v218\n"
      "v_mfma_f32_16x16x32_bf16 v[224:227], a[212:215], a[52:55], v[224:227]\n"
      "v_fma_f32 v219, v219, s48, v18\n"
      "v_cvt_pk_bf16_f32 v190, v32, v33\n"
      "v_mfma_f32_16x16x32_bf16 v[224:227], a[216:219], a[56:59], v[224:227]\n"
      "v_exp_f32 v35, v219\n"
      "v_mfma_f32_16x16x32_bf16 v[224:227], a[220:223], a[60:63], v[224:227]\n"
      "v_or3_b32 v22, v22, v205, v190\n"
      "v_cvt_pk_bf16_f32 v191, v34, v35\n"
      "v_mfma_f32_16x16x32_bf16 v[224:227], a[224:227], a[64:67], v[224:227]\n"
      "v_fma_f32 v220, v220, s48, v19\n"
      "v_fma_f32 v221, v221, s48, v19\n"
      "v_mfma_f32_16x16x32_bf16 v[224:227], a[228:231], a[68:71], v[224:227]\n"
      "s_waitcnt lgkmcnt(0)\n"
      "v_exp_f32 v28, v220\n"
      "v_smfmac_f32_16x16x64_bf16 a[196:199], v[24:27], v[168:175], v16\n"
      "ds_read_b128 a[208:211], v232 offset:12288\n"
      "v_fma_f32 v222, v222, s48, v19\n"
      "v_mfma_f32_16x16x32_bf16 v[76:79], v[160:163], a[232:235], v[76:79]\n"
      "v_exp_f32 v29, v221\n"
      "v_mfma_f32_16x16x32_bf16 v[76:79], v[164:167], a[236:239], v[76:79]\n"
      "ds_read_b128 a[212:215], v233 offset:12288\n"
      "v_fma_f32 v223, v223, s48, v19\n"
      "v_mfma_f32_16x16x32_bf16 v[124:127], v[172:175], a[236:239], v[124:127]\n"
      "v_exp_f32 v30, v222\n"
      "v_mfma_f32_16x16x32_bf16 v[124:127], v[168:171], a[232:235], v[124:127]\n"
      "ds_read_b128 a[216:219], v232 offset:12416\n"
      "v_cvt_pk_bf16_f32 v198, v28, v29\n"
      "v_mfma_f32_16x16x32_bf16 a[108:111], v[176:179], a[232:235], a[108:111]\n"
      "v_exp_f32 v31, v223\n"
      "v_mfma_f32_16x16x32_bf16 a[108:111], v[180:183], a[236:239], a[108:111]\n"
      "ds_read_b128 a[220:223], v233 offset:12416\n"
      "v_or3_b32 v22, v22, v191, v198\n"
      "v_mfma_f32_16x16x32_bf16 a[156:159], v[184:187], a[236:239], a[156:159]\n"
      "v_cvt_pk_bf16_f32 v199, v30, v31\n"
      "v_fma_f32 v224, v224, s48, v20\n"
      "v_mfma_f32_16x16x32_bf16 v[80:83], v[160:163], a[240:243], v[80:83]\n"
      "ds_read_b128 a[224:227], v232 offset:12544\n"
      "v_fma_f32 v225, v225, s48, v20\n"
      "v_mfma_f32_16x16x32_bf16 v[80:83], v[164:167], a[244:247], v[80:83]\n"
      "v_exp_f32 v32, v224\n"
      "v_mfma_f32_16x16x32_bf16 v[128:131], v[172:175], a[244:247], v[128:131]\n"
      "ds_read_b128 a[228:231], v233 offset:12544\n"
      "v_fma_f32 v226, v226, s48, v20\n"
      "v_mfma_f32_16x16x32_bf16 v[128:131], v[168:171], a[240:243], v[128:131]\n"
      "v_exp_f32 v33, v225\n"
      "v_mfma_f32_16x16x32_bf16 a[112:115], v[176:179], a[240:243], a[112:115]\n"
      "v_exp_f32 v34, v226\n"
      "v_mfma_f32_16x16x32_bf16 a[112:115], v[180:183], a[244:247], a[112:115]\n"
      "v_fma_f32 v227, v227, s48, v20\n"
      "v_cvt_pk_bf16_f32 v206, v32, v33\n"
      "v_mfma_f32_16x16x32_bf16 a[160:163], v[184:187], a[244:247], a[160:163]\n"
      "v_exp_f32 v35, v227\n"
      "v_mfma_f32_16x16x32_bf16 v[84:87], v[160:163], a[248:251], v[84:87]\n"
      "v_or3_b32 v22, v22, v199, v206\n"
      "v_cvt_pk_bf16_f32 v207, v34, v35\n"
      "v_mfma_f32_16x16x32_bf16 v[84:87], v[164:167], a[252:255], v[84:87]\n"
      "v_bitop3_b32 v22, v22, v207, s49 bitop3:0xa8\n"
      "v_cmp_ne_u32 vcc, 0, v22\n"
      "v_mfma_f32_16x16x32_bf16 v[132:135], v[172:175], a[252:255], v[132:135]\n"
      "v_mfma_f32_16x16x32_bf16 v[132:135], v[168:171], a[248:251], v[132:135]\n"
      "v_mfma_f32_16x16x32_bf16 a[116:119], v[176:179], a[248:251], a[116:119]\n"
      "s_cbranch_vccnz L_rare_Of1_1\n"
      "L_rare_Of1_1_ret:\n"
      "v_mfma_f32_16x16x32_bf16 a[116:119], v[180:183], a[252:255], a[116:119]\n"
      "v_mfma_f32_16x16x32_bf16 a[164:167], v[184:187], a[252:255], a[164:167]\n"
      "s_waitcnt lgkmcnt(0)\n"
      "v_mfma_f32_16x16x32_bf16 v[36:39], a[208:211], a[0:3], 0\n"
      "ds_read_b64_tr_b16 a[232:233], v236 offset:128\n"
      "v_mfma_f32_16x16x32_bf16 v[36:39], a[212:215], a[4:7], v[36:39]\n"
      "ds_read_b64_tr_b16 a[234:235], v236 offset:6272\n"
      "v_mfma_f32_16x16x32_bf16 v[36:39], a[216:219], a[8:11], v[36:39]\n"
      "ds_read_b64_tr_b16 a[236:237], v236 offset:12416\n"
      "v_mfma_f32_16x16x32_bf16 v[36:39], a[220:223], a[12:15], v[36:39]\n"
      "s_mov_b32 m0, s47\n"
      "ds_read_b64_tr_b16 a[238:239], v236 offset:18560\n"
      "v_mfma_f32_16x16x32_bf16 v[36:39], a[224:227], a[16:19], v[36:39]\n"
      "buffer_load_dwordx4 v1, s[24:27], s38 offen offset:0 lds\n"
      "v_mfma_f32_16x16x32_bf16 v[36:39], a[228:231], a[20:23], v[36:39]\n"
      "buffer_load_dwordx4 v2, s[24:27], s38 offen offset:1024 lds\n"
      "v_mfma_f32_16x16x32_bf16 v[40:43], a[228:231], a[44:47], 0\n"
      "buffer_load_dwordx4 v3, s[24:27], s38 offen offset:2048 lds\n"
      "v_mfma_f32_16x16x32_bf16 v[40:43], a[224:227], a[40:43], v[40:43]\n"
      "s_nop 0\n"
      "ds_read_b64_tr_b16 a[240:241], v237 offset:128\n"
      "v_mfma_f32_16x16x32_bf16 v[40:43], a[220:223], a[36:39], v[40:43]\n"
      "ds_read_b64_tr_b16 a[242:243], v237 offset:6272\n"
      "v_fma_f32 v36, v36, s48, v18\n"
      "v_mfma_f32_16x16x32_bf16 v[40:43], a[216:219], a[32:35], v[40:43]\n"
      "ds_read_b64_tr_b16 a[244:245], v237 offset:12416\n"
      "v_fma_f32 v37, v37, s48, v18\n"
      "v_mfma_f32_16x16x32_bf16 v[40:43], a[212:215], a[28:31], v[40:43]\n"
      "ds_read_b64_tr_b16 a[246:247], v237 offset:18560\n"
      "v_fma_f32 v38, v38, s48, v18\n"
      "v_mfma_f32_16x16x32_bf16 v[40:43], a[208:211], a[24:27], v[40:43]\n"
      "ds_read_b64_tr_b16 a[248:249], v234 offset:256\n"
      "v_fma_f32 v39, v39, s48, v18\n"
      "v_mfma_f32_16x16x32_bf16 v[44:47], a[208:211], a[48:51], 0\n"
      "v_exp_f32 v28, v36\n"
      "v_mfma_f32_16x16x32_bf16 v[44:47], a[212:215], a[52:55], v[44:47]\n"
      "v_exp_f32 v29, v37\n"
      "v_mfma_f32_16x16x32_bf16 v[44:47], a[216:219], a[56:59], v[44:47]\n"
      "ds_read_b64_tr_b16 a[250:251], v234 offset:6400\n"
      "v_cvt_pk_bf16_f32 v192, v28, v29\n"
      "v_mfma_f32_16x16x32_bf16 v[44:47], a[220:223], a[60:63], v[44:47]\n"
      "ds_read_b64_tr_b16 a[252:253], v234 offset:12544\n"
      "v_fma_f32 v40, v40, s48, v19\n"
      "v_mfma_f32_16x16x32_bf16 v[44:47], a[224:227], a[64:67], v[44:47]\n"
      "ds_read_b64_tr_b16 a[254:255], v234 offset:18688\n"
      "v_fma_f32 v41, v41, s48, v19\n"
      "v_mfma_f32_16x16x32_bf16 v[44:47], a[228:231], a[68:71], v[44:47]\n"
      "v_exp_f32 v30, v38\n"
      "v_mfma_f32_16x16x32_bf16 v[48:51], a[228:231], a[92:95], 0\n"
      "v_exp_f32 v31, v39\n"
      "v_mfma_f32_16x16x32_bf16 v[48:51], a[224:227], a[88:91], v[48:51]\n"
      "v_exp_f32 v32, v40\n"
      "v_mfma_f32_16x16x32_bf16 v[48:51], a[220:223], a[84:87], v[48:51]\n"
      "v_exp_f32 v33, v41\n"
      "v_mfma_f32_16x16x32_bf16 v[48:51], a[216:219], a[80:83], v[48:51]\n"
      "v_cvt_pk_bf16_f32 v193, v30, v31\n"
      "v_fma_f32 v42, v42, s48, v19\n"
      "v_mfma_f32_16x16x32_bf16 v[48:51], a[212:215], a[76:79], v[48:51]\n"
      "v_exp_f32 v34, v42\n"
      "v_mfma_f32_16x16x32_bf16 v[48:51], a[208:211], a[72:75], v[48:51]\n"
      "s_waitcnt lgkmcnt(0)\n"
      "v_fma_f32 v43, v43, s48, v19\n"
      "v_cvt_pk_bf16_f32 v200, v32, v33\n"
      "v_smfmac_f32_16x16x64_bf16 a[200:203], v[24:27], v[176:183], v16\n"
      "ds_read_b128 a[208:211], v232 offset:18432\n"
      "v_or3_b32 v22, v192, v193, v200\n"
      "v_mfma_f32_16x16x32_bf16 v[88:91], v[160:163], a[232:235], v[88:91]\n"
      "v_exp_f32 v35, v43\n"
      "v_mfma_f32_16x16x32_bf16 v[88:91], v[164:167], a[236:239], v[88:91]\n"
      "ds_read_b128 a[212:215], v233 offset:18432\n"
      "v_cvt_pk_bf16_f32 v201, v34, v35\n"
      "v_mfma_f32_16x16x32_bf16 v[136:139], v[172:175], a[236:239], v[136:139]\n"
      "v_fma_f32 v44, v44, s48, v20\n"
      "v_fma_f32 v45, v45, s48, v20\n"
      "v_mfma_f32_16x16x32_bf16 v[136:139], v[168:171], a[232:235], v[136:139]\n"
      "ds_read_b128 a[216:219], v232 offset:18560\n"
      "v_fma_f32 v46, v46, s48, v20\n"
      "v_mfma_f32_16x16x32_bf16 a[120:123], v[176:179], a[232:235], a[120:123]\n"
      "v_exp_f32 v28, v44\n"
      "v_mfma_f32_16x16x32_bf16 a[120:123], v[180:183], a[236:239], a[120:123]\n"
      "ds_read_b128 a[220:223], v233 offset:18560\n"
      "v_fma_f32 v47, v47, s48, v20\n"
      "v_mfma_f32_16x16x32_bf16 a[168:171], v[184:187], a[236:239], a[168:171]\n"
      "v_exp_f32 v29, v45\n"
      "v_mfma_f32_16x16x32_bf16 v[92:95], v[160:163], a[240:243], v[92:95]\n"
      "ds_read_b128 a[224:227], v232 offset:18688\n"
      "v_cvt_pk_bf16_f32 v208, v28, v29\n"
      "v_mfma_f32_16x16x32_bf16 v[92:95], v[164:167], a[244:247], v[92:95]\n"
      "ds_read_b64_tr_b16 a[232:233], v235 offset:256\n"
      "v_or3_b32 v22, v22, v201, v208\n"
      "v_mfma_f32_16x16x32_bf16 v[140:143], v[172:175], a[244:247], v[140:143]\n"
      "ds_read_b128 a[228:231], v233 offset:18688\n"
      "v_fma_f32 v48, v48, s48, v21\n"
      "v_mfma_f32_16x16x32_bf16 v[140:143], v[168:171], a[240:243], v[140:143]\n"
      "ds_read_b64_tr_b16 a[234:235], v235 offset:6400\n"
      "v_fma_f32 v49, v49, s48, v21\n"
      "v_mfma_f32_16x16x32_bf16 a[124:127], v[176:179], a[240:243], a[124:127]\n"
      "ds_read_b64_tr_b16 a[236:237], v235 offset:12544\n"
      "v_fma_f32 v50, v50, s48, v21\n"
      "v_mfma_f32_16x16x32_bf16 a[124:127], v[180:183], a[244:247], a[124:127]\n"
      "ds_read_b64_tr_b16 a[238:239], v235 offset:18688\n"
      "v_fma_f32 v51, v51, s48, v21\n"
      "v_mfma_f32_16x16x32_bf16 a[172:175], v[184:187], a[244:247], a[172:175]\n"
      "v_exp_f32 v30, v46\n"
      "v_mfma_f32_16x16x32_bf16 v[96:99], v[160:163], a[248:251], v[96:99]\n"
      "v_exp_f32 v31, v47\n"
      "v_mfma_f32_16x16x32_bf16 v[96:99], v[164:167], a[252:255], v[96:99]\n"
      "ds_read_b64_tr_b16 a[240:241], v236 offset:256\n"
      "v_cvt_pk_bf16_f32 v209, v30, v31\n"
      "v_mfma_f32_16x16x32_bf16 v[144:147], v[172:175], a[252:255], v[144:147]\n"
      "v_exp_f32 v32, v48\n"
      "v_mfma_f32_16x16x32_bf16 v[144:147], v[168:171], a[248:251], v[144:147]\n"
      "v_exp_f32 v33, v49\n"
      "v_mfma_f32_16x16x32_bf16 a[128:131], v[176:179], a[248:251], a[128:131]\n"
      "ds_read_b64_tr_b16 a[242:243], v236 offset:6400\n"
      "v_cvt_pk_bf16_f32 v212, v32, v33\n"
      "v_mfma_f32_16x16x32_bf16 a[128:131], v[180:183], a[252:255], a[128:131]\n"
      "ds_read_b64_tr_b16 a[244:245], v236 offset:12544\n"
      "v_or3_b32 v22, v22, v209, v212\n"
      "v_mfma_f32_16x16x32_bf16 a[176:179], v[184:187], a[252:255], a[176:179]\n"
      "s_waitcnt lgkmcnt(6)\n"
      "v_exp_f32 v34, v50\n"
      "v_mfma_f32_16x16x32_bf16 v[216:219], a[208:211], a[0:3], 0\n"
      "s_add_u32 s34, s34, 6144\n"
      "v_exp_f32 v35, v51\n"
      "v_mfma_f32_16x16x32_bf16 v[216:219], a[212:215], a[4:7], v[216:219]\n"
      "ds_read_b64_tr_b16 a[246:247], v236 offset:18688\n"
      "v_cvt_pk_bf16_f32 v213, v34, v35\n"
      "v_mfma_f32_16x16x32_bf16 v[216:219], a[216:219], a[8:11], v[216:219]\n"
      "ds_read_b64_tr_b16 a[248:249], v237 offset:256\n"
      "v_mfma_f32_16x16x32_bf16 v[216:219], a[220:223], a[12:15], v[216:219]\n"
      "s_add_u32 m0, s47, 3072\n"
      "ds_read_b64_tr_b16 a[250:251], v237 offset:6400\n"
      "v_mfma_f32_16x16x32_bf16 v[216:219], a[224:227], a[16:19], v[216:219]\n"
      "buffer_load_dwordx4 v1, s[24:27], s39 offen offset:0 lds\n"
      "v_mfma_f32_16x16x32_bf16 v[216:219], a[228:231], a[20:23], v[216:219]\n"
      "buffer_load_dwordx4 v2, s[24:27], s39 offen offset:1024 lds\n"
      "v_mfma_f32_16x16x32_bf16 v[220:223], a[228:231], a[44:47], 0\n"
      "buffer_load_dwordx4 v3, s[24:27], s39 offen offset:2048 lds\n"
      "v_mfma_f32_16x16x32_bf16 v[220:223], a[224:227], a[40:43], v[220:223]\n"
      "s_nop 0\n"
      "ds_read_b64_tr_b16 a[252:253], v237 offset:12544\n"
      "v_mfma_f32_16x16x32_bf16 v[220:223], a[220:223], a[36:39], v[220:223]\n"
      "ds_read_b64_tr_b16 a[254:255], v237 offset:18688\n"
      "v_fma_f32 v216, v216, s48, v18\n"
      "v_mfma_f32_16x16x32_bf16 v[220:223], a[216:219], a[32:35], v[220:223]\n"
      "v_exp_f32 v28, v216\n"
      "v_mfma_f32_16x16x32_bf16 v[220:223], a[212:215], a[28:31], v[220:223]\n"
      "v_fma_f32 v217, v217, s48, v18\n"
      "v_fma_f32 v218, v218, s48, v18\n"
      "v_mfma_f32_16x16x32_bf16 v[220:223], a[208:211], a[24:27], v[220:223]\n"
      "v_exp_f32 v29, v217\n"
      "v_mfma_f32_16x16x32_bf16 v[224:227], a[208:211], a[48:51], 0\n"
      "v_exp_f32 v30, v218\n"
      "v_mfma_f32_16x16x32_bf16 v[224:227], a[212:215], a[52:55], v[224:227]\n"
      "v_fma_f32 v219, v219, s48, v18\n"
      "v_cvt_pk_bf16_f32 v194, v28, v29\n"
      "v_mfma_f32_16x16x32_bf16 v[224:227], a[216:219], a[56:59], v[224:227]\n"
      "v_exp_f32 v31, v219\n"
      "v_mfma_f32_16x16x32_bf16 v[224:227], a[220:223], a[60:63], v[224:227]\n"
      "v_or3_b32 v22, v22, v213, v194\n"
      "v_cvt_pk_bf16_f32 v195, v30, v31\n"
      "v_mfma_f32_16x16x32_bf16 v[224:227], a[224:227], a[64:67], v[224:227]\n"
      "v_fma_f32 v220, v220, s48, v19\n"
      "v_fma_f32 v221, v221, s48, v19\n"
      "v_mfma_f32_16x16x32_bf16 v[224:227], a[228:231], a[68:71], v[224:227]\n"
      "s_mov_b32 s38, s36\n"
      "v_exp_f32 v32, v220\n"
      "v_mfma_f32_16x16x32_bf16 v[228:231], a[228:231], a[92:95], 0\n"
      "s_mov_b32 s39, s37\n"
      "v_exp_f32 v33, v221\n"
      "v_mfma_f32_16x16x32_bf16 v[228:231], a[224:227], a[88:91], v[228:231]\n"
      "s_add_u32 s12, s12, 1\n"
      "v_fma_f32 v222, v222, s48, v19\n"
      "v_fma_f32 v223, v223, s48, v19\n"
      "v_mfma_f32_16x16x32_bf16 v[228:231], a[220:223], a[84:87], v[228:231]\n"
      "s_add_u32 s15, s13, s12\n"
      "v_exp_f32 v34, v222\n"
      "v_mfma_f32_16x16x32_bf16 v[228:231], a[216:219], a[80:83], v[228:231]\n"
      "s_and_b32 s15, s15, 15\n"
      "v_exp_f32 v35, v223\n"
      "v_mfma_f32_16x16x32_bf16 v[228:231], a[212:215], a[76:79], v[228:231]\n"
      "s_waitcnt vmcnt(12) lgkmcnt(0)\n"
      "v_cvt_pk_bf16_f32 v202, v32, v33\n"
      "v_or3_b32 v22, v22, v195, v202\n"
      "v_mfma_f32_16x16x32_bf16 v[228:231], a[208:211], a[72:75], v[228:231]\n"
      "s_barrier\n"
      "v_cvt_pk_bf16_f32 v203, v34, v35\n"
      "v_fma_f32 v224, v224, s48, v20\n"
      "v_mfma_f32_16x16x32_bf16 a[204:207], v[24:27], v[184:187], a[204:207]\n"
      "s_mul_i32 s15, s15, 24576\n"
      "ds_read_b128 a[208:211], v4 offset:0\n"
      "v_fma_f32 v225, v225, s48, v20\n"
      "v_mfma_f32_16x16x32_bf16 v[100:103], v[160:163], a[232:235], v[100:103]\n"
      "s_lshr_b32 s16, s12, 4\n"
      "s_cmp_lt_u32 s34, 21504\n"
      "s_cbranch_scc0 L_not_O\n"
      "buffer_load_dword v255, v254, s[96:99], s34 offen\n"
      "L_not_O:\n"
      "v_mfma_f32_16x16x32_bf16 v[100:103], v[164:167], a[236:239], v[100:103]\n"
      "s_mul_i32 s16, s16, 393216\n"
      "ds_read_b128 a[212:215], v5 offset:0\n"
      "v_fma_f32 v226, v226, s48, v20\n"
      "v_mfma_f32_16x16x32_bf16 v[148:151], v[172:175], a[236:239], v[148:151]\n"
      "s_add_u32 s15, s15, s16\n"
      "v_exp_f32 v28, v224\n"
      "v_mfma_f32_16x16x32_bf16 v[148:151], v[168:171], a[232:235], v[148:151]\n"
      "s_add_u32 s36, s15, s14\n"
      "ds_read_b128 a[216:219], v4 offset:128\n"
      "v_fma_f32 v227, v227, s48, v20\n"
      "v_mfma_f32_16x16x32_bf16 a[132:135], v[176:179], a[232:235], a[132:135]\n"
      "s_add_u32 s37, s36, 3072\n"
      "v_exp_f32 v29, v225\n"
      "v_mfma_f32_16x16x32_bf16 a[132:135], v[180:183], a[236:239], a[132:135]\n"
      "ds_read_b128 a[220:223], v5 offset:128\n"
      "v_cvt_pk_bf16_f32 v210, v28, v29\n"
      "v_mfma_f32_16x16x32_bf16 a[180:183], v[184:187], a[236:239], a[180:183]\n"
      "v_exp_f32 v30, v226\n"
      "v_mfma_f32_16x16x32_bf16 v[104:107], v[160:163], a[240:243], v[104:107]\n"
      "ds_read_b128 a[224:227], v4 offset:256\n"
      "v_or3_b32 v22, v22, v203, v210\n"
      "v_mfma_f32_16x16x32_bf16 v[104:107], v[164:167], a[244:247], v[104:107]\n"
      "v_exp_f32 v31, v227\n"
      "v_mfma_f32_16x16x32_bf16 v[152:155], v[172:175], a[244:247], v[152:155]\n"
      "ds_read_b128 a[228:231], v5 offset:256\n"
      "v_cvt_pk_bf16_f32 v211, v30, v31\n"
      "v_mfma_f32_16x16x32_bf16 v[152:155], v[168:171], a[240:243], v[152:155]\n"
      "v_fma_f32 v228, v228, s48, v21\n"
      "v_fma_f32 v229, v229, s48, v21\n"
      "v_mfma_f32_16x16x32_bf16 a[136:139], v[176:179], a[240:243], a[136:139]\n"
      "v_exp_f32 v32, v228\n"
      "v_mfma_f32_16x16x32_bf16 a[136:139], v[180:183], a[244:247], a[136:139]\n"
      "v_exp_f32 v33, v229\n"
      "v_mfma_f32_16x16x32_bf16 a[184:187], v[184:187], a[244:247], a[184:187]\n"
      "v_fma_f32 v230, v230, s48, v21\n"
      "v_fma_f32 v231, v231, s48, v21\n"
      "v_mfma_f32_16x16x32_bf16 v[108:111], v[160:163], a[248:251], v[108:111]\n"
      "v_exp_f32 v34, v230\n"
      "v_mfma_f32_16x16x32_bf16 v[108:111], v[164:167], a[252:255], v[108:111]\n"
      "v_exp_f32 v35, v231\n"
      "v_mfma_f32_16x16x32_bf16 v[156:159], v[172:175], a[252:255], v[156:159]\n"
      "v_cvt_pk_bf16_f32 v214, v32, v33\n"
      "v_or3_b32 v22, v22, v211, v214\n"
      "v_mfma_f32_16x16x32_bf16 v[156:159], v[168:171], a[248:251], v[156:159]\n"
      "v_cvt_pk_bf16_f32 v215, v34, v35\n"
      "v_bitop3_b32 v22, v22, v215, s49 bitop3:0xa8\n"
      "v_cmp_ne_u32 vcc, 0, v22\n"
      "v_mfma_f32_16x16x32_bf16 a[140:143], v[176:179], a[248:251], a[140:143]\n"
      "s_cmp_eq_u32 s50, 0\n"
      "v_mfma_f32_16x16x32_bf16 a[140:143], v[180:183], a[252:255], a[140:143]\n"
      "v_mfma_f32_16x16x32_bf16 a[188:191], v[184:187], a[252:255], a[188:191]\n"
      "s_waitcnt lgkmcnt(0)\n"
      "s_cbranch_vccnz L_rare_Of1_3\n"
      "L_rare_Of1_3_ret:\n"
      "s_cbranch_scc1 L_last_O\n"
      "L_mid_O:\n"
      "v_mfma_f32_16x16x32_bf16 v[36:39], a[208:211], a[0:3], 0\n"
      "s_add_u32 s46, s44, s42\n"
      "ds_read_b64_tr_b16 a[232:233], v8 offset:0\n"
      "v_add_u32 v232, s41, v6\n"
      "v_mfma_f32_16x16x32_bf16 v[36:39], a[212:215], a[4:7], v[36:39]\n"
      "s_add_u32 s47, s45, s41\n"
      "ds_read_b64_tr_b16 a[234:235], v8 offset:6144\n"
      "v_add_u32 v233, s41, v7\n"
      "v_mfma_f32_16x16x32_bf16 v[36:39], a[216:219], a[8:11], v[36:39]\n"
      "ds_read_b64_tr_b16 a[236:237], v8 offset:12288\n"
      "v_add_u32 v234, s40, v12\n"
      "v_mfma_f32_16x16x32_bf16 v[36:39], a[220:223], a[12:15], v[36:39]\n"
      "s_mov_b32 m0, s46\n"
      "ds_read_b64_tr_b16 a[238:239], v8 offset:18432\n"
      "v_add_u32 v235, s40, v13\n"
      "v_mfma_f32_16x16x32_bf16 v[36:39], a[224:227], a[16:19], v[36:39]\n"
      "buffer_load_dwordx4 v1, s[20:23], s36 offen offset:0 lds\n"
      "v_mfma_f32_16x16x32_bf16 v[36:39], a[228:231], a[20:23], v[36:39]\n"
      "ds_read_b64_tr_b16 a[240:241], v9 offset:0\n"
      "v_add_u32 v236, s40, v14\n"
      "v_mfma_f32_16x16x32_bf16 v[40:43], a[228:231], a[44:47], 0\n"
      "ds_read_b64_tr_b16 a[242:243], v9 offset:6144\n"
      "v_add_u32 v237, s40, v15\n"
      "v_mfma_f32_16x16x32_bf16 v[40:43], a[224:227], a[40:43], v[40:43]\n"
      "s_mov_b32 s43, s40\n"
      "ds_read_b64_tr_b16 a[244:245], v9 offset:12288\n"
      "v_mfma_f32_16x16x32_bf16 v[40:43], a[220:223], a[36:39], v[40:43]\n"
      "s_mov_b32 s40, s41\n"
      "ds_read_b64_tr_b16 a[246:247], v9 offset:18432\n"
      "v_fma_f32 v36, v36, s48, v18\n"
      "v_mfma_f32_16x16x32_bf16 v[40:43], a[216:219], a[32:35], v[40:43]\n"
      "s_mov_b32 s41, s42\n"
      "ds_read_b64_tr_b16 a[248:249], v10 offset:0\n"
      "v_fma_f32 v37, v37, s48, v18\n"
      "v_mfma_f32_16x16x32_bf16 v[40:43], a[212:215], a[28:31], v[40:43]\n"
      "s_mov_b32 s42, s43\n"
      "ds_read_b64_tr_b16 a[250:251], v10 offset:6144\n"
      "v_fma_f32 v38, v38, s48, v18\n"
      "v_mfma_f32_16x16x32_bf16 v[40:43], a[208:211], a[24:27], v[40:43]\n"
      "ds_read_b64_tr_b16 a[252:253], v10 offset:12288\n"
      "v_fma_f32 v39, v39, s48, v18\n"
      "v_mfma_f32_16x16x32_bf16 v[44:47], a[208:211], a[48:51], 0\n"
      "ds_read_b64_tr_b16 a[254:255], v10 offset:18432\n"
      "v_mfma_f32_16x16x32_bf16 v[44:47], a[212:215], a[52:55], v[44:47]\n"
      "buffer_load_dwordx4 v2, s[20:23], s36 offen offset:1024 lds\n"
      "v_mfma_f32_16x16x32_bf16 v[44:47], a[216:219], a[56:59], v[44:47]\n"
      "buffer_load_dwordx4 v3, s[20:23], s36 offen offset:2048 lds\n"
      "v_mfma_f32_16x16x32_bf16 v[44:47], a[220:223], a[60:63], v[44:47]\n"
      "v_exp_f32 v28, v36\n"
      "v_mfma_f32_16x16x32_bf16 v[44:47], a[224:227], a[64:67], v[44:47]\n"
      "v_exp_f32 v29, v37\n"
      "v_mfma_f32_16x16x32_bf16 v[44:47], a[228:231], a[68:71], v[44:47]\n"
      "s_waitcnt lgkmcnt(0)\n"
      "v_exp_f32 v30, v38\n"
      "v_smfmac_f32_16x16x64_bf16 a[192:195], v[24:27], v[188:195], v16\n"
      "ds_read_b128 a[208:211], v4 offset:6144\n"
      "v_cvt_pk_bf16_f32 v160, v28, v29\n"
      "v_mfma_f32_16x16x32_bf16 v[64:67], v[188:191], a[232:235], v[64:67]\n"
      "v_exp_f32 v31, v39\n"
      "v_mfma_f32_16x16x32_bf16 v[64:67], v[192:195], a[236:239], v[64:67]\n"
      "ds_read_b128 a[212:215], v5 offset:6144\n"
      "v_cvt_pk_bf16_f32 v161, v30, v31\n"
      "v_mfma_f32_16x16x32_bf16 v[112:115], v[200:203], a[236:239], v[112:115]\n"
      "v_fma_f32 v40, v40, s48, v19\n"
      "v_fma_f32 v41, v41, s48, v19\n"
      "v_mfma_f32_16x16x32_bf16 v[112:115], v[196:199], a[232:235], v[112:115]\n"
      "ds_read_b128 a[216:219], v4 offset:6272\n"
      "v_fma_f32 v42, v42, s48, v19\n"
      "v_mfma_f32_16x16x32_bf16 a[96:99], v[204:207], a[232:235], a[96:99]\n"
      "v_exp_f32 v32, v40\n"
      "v_mfma_f32_16x16x32_bf16 a[96:99], v[208:211], a[236:239], a[96:99]\n"
      "ds_read_b128 a[220:223], v5 offset:6272\n"
      "v_fma_f32 v43, v43, s48, v19\n"
      "v_mfma_f32_16x16x32_bf16 a[144:147], v[212:215], a[236:239], a[144:147]\n"
      "v_exp_f32 v33, v41\n"
      "v_mfma_f32_16x16x32_bf16 v[68:71], v[188:191], a[240:243], v[68:71]\n"
      "ds_read_b128 a[224:227], v4 offset:6400\n"
      "v_cvt_pk_bf16_f32 v168, v32, v33\n"
      "v_mfma_f32_16x16x32_bf16 v[68:71], v[192:195], a[244:247], v[68:71]\n"
      "ds_read_b64_tr_b16 a[232:233], v11 offset:0\n"
      "v_or3_b32 v22, v160, v161, v168\n"
      "v_mfma_f32_16x16x32_bf16 v[116:119], v[200:203], a[244:247], v[116:119]\n"
      "ds_read_b128 a[228:231], v5 offset:6400\n"
      "v_fma_f32 v44, v44, s48, v20\n"
      "v_mfma_f32_16x16x32_bf16 v[116:119], v[196:199], a[240:243], v[116:119]\n"
      "ds_read_b64_tr_b16 a[234:235], v11 offset:6144\n"
      "v_fma_f32 v45, v45, s48, v20\n"
      "v_mfma_f32_16x16x32_bf16 a[100:103], v[204:207], a[240:243], a[100:103]\n"
      "ds_read_b64_tr_b16 a[236:237], v11 offset:12288\n"
      "v_fma_f32 v46, v46, s48, v20\n"
      "v_mfma_f32_16x16x32_bf16 a[100:103], v[208:211], a[244:247], a[100:103]\n"
      "ds_read_b64_tr_b16 a[238:239], v11 offset:18432\n"
      "v_fma_f32 v47, v47, s48, v20\n"
      "v_mfma_f32_16x16x32_bf16 a[148:151], v[212:215], a[244:247], a[148:151]\n"
      "v_exp_f32 v34, v42\n"
      "v_mfma_f32_16x16x32_bf16 v[72:75], v[188:191], a[248:251], v[72:75]\n"
      "v_exp_f32 v35, v43\n"
      "v_mfma_f32_16x16x32_bf16 v[72:75], v[192:195], a[252:255], v[72:75]\n"
      "ds_read_b64_tr_b16 a[240:241], v8 offset:128\n"
      "v_cvt_pk_bf16_f32 v169, v34, v35\n"
      "v_mfma_f32_16x16x32_bf16 v[120:123], v[200:203], a[252:255], v[120:123]\n"
      "v_exp_f32 v28, v44\n"
      "v_mfma_f32_16x16x32_bf16 v[120:123], v[196:199], a[248:251], v[120:123]\n"
      "v_exp_f32 v29, v45\n"
      "v_mfma_f32_16x16x32_bf16 a[104:107], v[204:207], a[248:251], a[104:107]\n"
      "ds_read_b64_tr_b16 a[242:243], v8 offset:6272\n"
      "v_cvt_pk_bf16_f32 v176, v28, v29\n"
      "v_mfma_f32_16x16x32_bf16 a[104:107], v[208:211], a[252:255], a[104:107]\n"
      "ds_read_b64_tr_b16 a[244:245], v8 offset:12416\n"
      "v_or3_b32 v22, v22, v169, v176\n"
      "v_mfma_f32_16x16x32_bf16 a[152:155], v[212:215], a[252:255], a[152:155]\n"
      "s_waitcnt lgkmcnt(6)\n"
      "v_exp_f32 v30, v46\n"
      "v_mfma_f32_16x16x32_bf16 v[216:219], a[208:211], a[0:3], 0\n"
      "v_exp_f32 v31, v47\n"
      "v_mfma_f32_16x16x32_bf16 v[216:219], a[212:215], a[4:7], v[216:219]\n"
      "ds_read_b64_tr_b16 a[246:247], v8 offset:18560\n"
      "v_cvt_pk_bf16_f32 v177, v30, v31\n"
      "v_mfma_f32_16x16x32_bf16 v[216:219], a[216:219], a[8:11], v[216:219]\n"
      "ds_read_b64_tr_b16 a[248:249], v9 offset:128\n"
      "v_mfma_f32_16x16x32_bf16 v[216:219], a[220:223], a[12:15], v[216:219]\n"
      "s_add_u32 m0, s46, 3072\n"
      "ds_read_b64_tr_b16 a[250:251], v9 offset:6272\n"
      "v_mfma_f32_16x16x32_bf16 v[216:219], a[224:227], a[16:19], v[216:219]\n"
      "buffer_load_dwordx4 v1, s[20:23], s37 offen offset:0 lds\n"
      "v_mfma_f32_16x16x32_bf16 v[216:219], a[228:231], a[20:23], v[216:219]\n"
      "buffer_load_dwordx4 v2, s[20:23], s37 offen offset:1024 lds\n"
      "v_mfma_f32_16x16x32_bf16 v[220:223], a[228:231], a[44:47], 0\n"
      "buffer_load_dwordx4 v3, s[20:23], s37 offen offset:2048 lds\n"
      "v_mfma_f32_16x16x32_bf16 v[220:223], a[224:227], a[40:43], v[220:223]\n"
      "s_nop 0\n"
      "ds_read_b64_tr_b16 a[252:253], v9 offset:12416\n"
      "v_mfma_f32_16x16x32_bf16 v[220:223], a[220:223], a[36:39], v[220:223]\n"
      "ds_read_b64_tr_b16 a[254:255], v9 offset:18560\n"
      "v_fma_f32 v216, v216, s48, v18\n"
      "v_mfma_f32_16x16x32_bf16 v[220:223], a[216:219], a[32:35], v[220:223]\n"
      "v_exp_f32 v32, v216\n"
      "v_mfma_f32_16x16x32_bf16 v[220:223], a[212:215], a[28:31], v[220:223]\n"
      "v_fma_f32 v217, v217, s48, v18\n"
      "v_fma_f32 v218, v218, s48, v18\n"
      "v_mfma_f32_16x16x32_bf16 v[220:223], a[208:211], a[24:27], v[220:223]\n"
      "v_exp_f32 v33, v217\n"
      "v_mfma_f32_16x16x32_bf16 v[224:227], a[208:211], a[48:51], 0\n"
      "v_exp_f32 v34, v218\n"
      "v_mfma_f32_16x16x32_bf16 v[224:227], a[212:215], a[52:55], v[224:227]\n"
      "v_fma_f32 v219, v219, s48, v18\n"
      "v_cvt_pk_bf16_f32 v162, v32, v33\n"
      "v_mfma_f32_16x16x32_bf16 v[224:227], a[216:219], a[56:59], v[224:227]\n"
      "v_exp_f32 v35, v219\n"
      "v_mfma_f32_16x16x32_bf16 v[224:227], a[220:223], a[60:63], v[224:227]\n"
      "v_or3_b32 v22, v22, v177, v162\n"
      "v_cvt_pk_bf16_f32 v163, v34, v35\n"
      "v_mfma_f32_16x16x32_bf16 v[224:227], a[224:227], a[64:67], v[224:227]\n"
      "v_fma_f32 v220, v220, s48, v19\n"
      "v_fma_f32 v221, v221, s48, v19\n"
      "v_mfma_f32_16x16x32_bf16 v[224:227], a[228:231], a[68:71], v[224:227]\n"
      "s_waitcnt lgkmcnt(0)\n"
      "v_exp_f32 v28, v220\n"
      "v_smfmac_f32_16x16x64_bf16 a[196:199], v[24:27], v[196:203], v16\n"
      "ds_read_b128 a[208:211], v4 offset:12288\n"
      "v_fma_f32 v222, v222, s48, v19\n"
      "v_mfma_f32_16x16x32_bf16 v[76:79], v[188:191], a[232:235], v[76:79]\n"
      "v_exp_f32 v29, v221\n"
      "v_mfma_f32_16x16x32_bf16 v[76:79], v[192:195], a[236:239], v[76:79]\n"
      "ds_read_b128 a[212:215], v5 offset:12288\n"
      "v_fma_f32 v223, v223, s48, v19\n"
      "v_mfma_f32_16x16x32_bf16 v[124:127], v[200:203], a[236:239], v[124:127]\n"
      "v_exp_f32 v30, v222\n"
      "v_mfma_f32_16x16x32_bf16 v[124:127], v[196:199], a[232:235], v[124:127]\n"
      "ds_read_b128 a[216:219], v4 offset:12416\n"
      "v_cvt_pk_bf16_f32 v170, v28, v29\n"
      "v_mfma_f32_16x16x32_bf16 a[108:111], v[204:207], a[232:235], a[108:111]\n"
      "v_exp_f32 v31, v223\n"
      "v_mfma_f32_16x16x32_bf16 a[108:111], v[208:211], a[236:239], a[108:111]\n"
      "ds_read_b128 a[220:223], v5 offset:12416\n"
      "v_or3_b32 v22, v22, v163, v170\n"
      "v_mfma_f32_16x16x32_bf16 a[156:159], v[212:215], a[236:239], a[156:159]\n"
      "v_cvt_pk_bf16_f32 v171, v30, v31\n"
      "v_fma_f32 v224, v224, s48, v20\n"
      "v_mfma_f32_16x16x32_bf16 v[80:83], v[188:191], a[240:243], v[80:83]\n"
      "ds_read_b128 a[224:227], v4 offset:12544\n"
      "v_fma_f32 v225, v225, s48, v20\n"
      "v_mfma_f32_16x16x32_bf16 v[80:83], v[192:195], a[244:247], v[80:83]\n"
      "v_exp_f32 v32, v224\n"
      "v_mfma_f32_16x16x32_bf16 v[128:131], v[200:203], a[244:247], v[128:131]\n"
      "ds_read_b128 a[228:231], v5 offset:12544\n"
      "v_fma_f32 v226, v226, s48, v20\n"
      "v_mfma_f32_16x16x32_bf16 v[128:131], v[196:199], a[240:243], v[128:131]\n"
      "v_exp_f32 v33, v225\n"
      "v_mfma_f32_16x16x32_bf16 a[112:115], v[204:207], a[240:243], a[112:115]\n"
      "v_exp_f32 v34, v226\n"
      "v_mfma_f32_16x16x32_bf16 a[112:115], v[208:211], a[244:247], a[112:115]\n"
      "v_fma_f32 v227, v227, s48, v20\n"
      "v_cvt_pk_bf16_f32 v178, v32, v33\n"
      "v_mfma_f32_16x16x32_bf16 a[160:163], v[212:215], a[244:247], a[160:163]\n"
      "v_exp_f32 v35, v227\n"
      "v_mfma_f32_16x16x32_bf16 v[84:87], v[188:191], a[248:251], v[84:87]\n"
      "v_or3_b32 v22, v22, v171, v178\n"
      "v_cvt_pk_bf16_f32 v179, v34, v35\n"
      "v_mfma_f32_16x16x32_bf16 v[84:87], v[192:195], a[252:255], v[84:87]\n"
      "v_bitop3_b32 v22, v22, v179, s49 bitop3:0xa8\n"
      "v_cmp_ne_u32 vcc, 0, v22\n"
      "v_mfma_f32_16x16x32_bf16 v[132:135], v[200:203], a[252:255], v[132:135]\n"
      "v_mfma_f32_16x16x32_bf16 v[132:135], v[196:199], a[248:251], v[132:135]\n"
      "v_mfma_f32_16x16x32_bf16 a[116:119], v[204:207], a[248:251], a[116:119]\n"
      "s_cbranch_vccnz L_rare_Of0_1\n"
      "L_rare_Of0_1_ret:\n"
      "v_mfma_f32_16x16x32_bf16 a[116:119], v[208:211], a[252:255], a[116:119]\n"
      "v_mfma_f32_16x16x32_bf16 a[164:167], v[212:215], a[252:255], a[164:167]\n"
      "s_waitcnt lgkmcnt(0)\n"
      "v_mfma_f32_16x16x32_bf16 v[36:39], a[208:211], a[0:3], 0\n"
      "ds_read_b64_tr_b16 a[232:233], v10 offset:128\n"
      "v_mfma_f32_16x16x32_bf16 v[36:39], a[212:215], a[4:7], v[36:39]\n"
      "ds_read_b64_tr_b16 a[234:235], v10 offset:6272\n"
      "v_mfma_f32_16x16x32_bf16 v[36:39], a[216:219], a[8:11], v[36:39]\n"
      "ds_read_b64_tr_b16 a[236:237], v10 offset:12416\n"
      "v_mfma_f32_16x16x32_bf16 v[36:39], a[220:223], a[12:15], v[36:39]\n"
      "s_mov_b32 m0, s47\n"
      "ds_read_b64_tr_b16 a[238:239], v10 offset:18560\n"
      "v_mfma_f32_16x16x32_bf16 v[36:39], a[224:227], a[16:19], v[36:39]\n"
      "buffer_load_dwordx4 v1, s[24:27], s38 offen offset:0 lds\n"
      "v_mfma_f32_16x16x32_bf16 v[36:39], a[228:231], a[20:23], v[36:39]\n"
      "buffer_load_dwordx4 v2, s[24:27], s38 offen offset:1024 lds\n"
      "v_mfma_f32_16x16x32_bf16 v[40:43], a[228:231], a[44:47], 0\n"
      "buffer_load_dwordx4 v3, s[24:27], s38 offen offset:2048 lds\n"
      "v_mfma_f32_16x16x32_bf16 v[40:43], a[224:227], a[40:43], v[40:43]\n"
      "s_nop 0\n"
      "ds_read_b64_tr_b16 a[240:241], v11 offset:128\n"
      "v_mfma_f32_16x16x32_bf16 v[40:43], a[220:223], a[36:39], v[40:43]\n"
      "ds_read_b64_tr_b16 a[242:243], v11 offset:6272\n"
      "v_fma_f32 v36, v36, s48, v18\n"
      "v_mfma_f32_16x16x32_bf16 v[40:43], a[216:219], a[32:35], v[40:43]\n"
      "ds_read_b64_tr_b16 a[244:245], v11 offset:12416\n"
      "v_fma_f32 v37, v37, s48, v18\n"
      "v_mfma_f32_16x16x32_bf16 v[40:43], a[212:215], a[28:31], v[40:43]\n"
      "ds_read_b64_tr_b16 a[246:247], v11 offset:18560\n"
      "v_fma_f32 v38, v38, s48, v18\n"
      "v_mfma_f32_16x16x32_bf16 v[40:43], a[208:211], a[24:27], v[40:43]\n"
      "ds_read_b64_tr_b16 a[248:249], v8 offset:256\n"
      "v_fma_f32 v39, v39, s48, v18\n"
      "v_mfma_f32_16x16x32_bf16 v[44:47], a[208:211], a[48:51], 0\n"
      "v_exp_f32 v28, v36\n"
      "v_mfma_f32_16x16x32_bf16 v[44:47], a[212:215], a[52:55], v[44:47]\n"
      "v_exp_f32 v29, v37\n"
      "v_mfma_f32_16x16x32_bf16 v[44:47], a[216:219], a[56:59], v[44:47]\n"
      "ds_read_b64_tr_b16 a[250:251], v8 offset:6400\n"
      "v_cvt_pk_bf16_f32 v164, v28, v29\n"
      "v_mfma_f32_16x16x32_bf16 v[44:47], a[220:223], a[60:63], v[44:47]\n"
      "ds_read_b64_tr_b16 a[252:253], v8 offset:12544\n"
      "v_fma_f32 v40, v40, s48, v19\n"
      "v_mfma_f32_16x16x32_bf16 v[44:47], a[224:227], a[64:67], v[44:47]\n"
      "ds_read_b64_tr_b16 a[254:255], v8 offset:18688\n"
      "v_fma_f32 v41, v41, s48, v19\n"
      "v_mfma_f32_16x16x32_bf16 v[44:47], a[228:231], a[68:71], v[44:47]\n"
      "v_exp_f32 v30, v38\n"
      "v_mfma_f32_16x16x32_bf16 v[48:51], a[228:231], a[92:95], 0\n"
      "v_exp_f32 v31, v39\n"
      "v_mfma_f32_16x16x32_bf16 v[48:51], a[224:227], a[88:91], v[48:51]\n"
      "v_exp_f32 v32, v40\n"
      "v_mfma_f32_16x16x32_bf16 v[48:51], a[220:223], a[84:87], v[48:51]\n"
      "v_exp_f32 v33, v41\n"
      "v_mfma_f32_16x16x32_bf16 v[48:51], a[216:219], a[80:83], v[48:51]\n"
      "v_cvt_pk_bf16_f32 v165, v30, v31\n"
      "v_fma_f32 v42, v42, s48, v19\n"
      "v_mfma_f32_16x16x32_bf16 v[48:51], a[212:215], a[76:79], v[48:51]\n"
      "v_exp_f32 v34, v42\n"
      "v_mfma_f32_16x16x32_bf16 v[48:51], a[208:211], a[72:75], v[48:51]\n"
      "s_waitcnt lgkmcnt(0)\n"
      "v_fma_f32 v43, v43, s48, v19\n"
      "v_cvt_pk_bf16_f32 v172, v32, v33\n"
      "v_smfmac_f32_16x16x64_bf16 a[200:203], v[24:27], v[204:211], v16\n"
      "ds_read_b128 a[208:211], v4 offset:18432\n"
      "v_or3_b32 v22, v164, v165, v172\n"
      "v_mfma_f32_16x16x32_bf16 v[88:91], v[188:191], a[232:235], v[88:91]\n"
      "v_exp_f32 v35, v43\n"
      "v_mfma_f32_16x16x32_bf16 v[88:91], v[192:195], a[236:239], v[88:91]\n"
      "ds_read_b128 a[212:215], v5 offset:18432\n"
      "v_cvt_pk_bf16_f32 v173, v34, v35\n"
      "v_mfma_f32_16x16x32_bf16 v[136:139], v[200:203], a[236:239], v[136:139]\n"
      "v_fma_f32 v44, v44, s48, v20\n"
      "v_fma_f32 v45, v45, s48, v20\n"
      "v_mfma_f32_16x16x32_bf16 v[136:139], v[196:199], a[232:235], v[136:139]\n"
      "ds_read_b128 a[216:219], v4 offset:18560\n"
      "v_fma_f32 v46, v46, s48, v20\n"
      "v_mfma_f32_16x16x32_bf16 a[120:123], v[204:207], a[232:235], a[120:123]\n"
      "v_exp_f32 v28, v44\n"
      "v_mfma_f32_16x16x32_bf16 a[120:123], v[208:211], a[236:239], a[120:123]\n"
      "ds_read_b128 a[220:223], v5 offset:18560\n"
      "v_fma_f32 v47, v47, s48, v20\n"
      "v_mfma_f32_16x16x32_bf16 a[168:171], v[212:215], a[236:239], a[168:171]\n"
      "v_exp_f32 v29, v45\n"
      "v_mfma_f32_16x16x32_bf16 v[92:95], v[188:191], a[240:243], v[92:95]\n"
      "ds_read_b128 a[224:227], v4 offset:18688\n"
      "v_cvt_pk_bf16_f32 v180, v28, v29\n"
      "v_mfma_f32_16x16x32_bf16 v[92:95], v[192:195], a[244:247], v[92:95]\n"
      "ds_read_b64_tr_b16 a[232:233], v9 offset:256\n"
      "v_or3_b32 v22, v22, v173, v180\n"
      "v_mfma_f32_16x16x32_bf16 v[140:143], v[200:203], a[244:247], v[140:143]\n"
      "ds_read_b128 a[228:231], v5 offset:18688\n"
      "v_fma_f32 v48, v48, s48, v21\n"
      "v_mfma_f32_16x16x32_bf16 v[140:143], v[196:199], a[240:243], v[140:143]\n"
      "ds_read_b64_tr_b16 a[234:235], v9 offset:6400\n"
      "v_fma_f32 v49, v49, s48, v21\n"
      "v_mfma_f32_16x16x32_bf16 a[124:127], v[204:207], a[240:243], a[124:127]\n"
      "ds_read_b64_tr_b16 a[236:237], v9 offset:12544\n"
      "v_fma_f32 v50, v50, s48, v21\n"
      "v_mfma_f32_16x16x32_bf16 a[124:127], v[208:211], a[244:247], a[124:127]\n"
      "ds_read_b64_tr_b16 a[238:239], v9 offset:18688\n"
      "v_fma_f32 v51, v51, s48, v21\n"
      "v_mfma_f32_16x16x32_bf16 a[172:175], v[212:215], a[244:247], a[172:175]\n"
      "v_exp_f32 v30, v46\n"
      "v_mfma_f32_16x16x32_bf16 v[96:99], v[188:191], a[248:251], v[96:99]\n"
      "v_exp_f32 v31, v47\n"
      "v_mfma_f32_16x16x32_bf16 v[96:99], v[192:195], a[252:255], v[96:99]\n"
      "ds_read_b64_tr_b16 a[240:241], v10 offset:256\n"
      "v_cvt_pk_bf16_f32 v181, v30, v31\n"
      "v_mfma_f32_16x16x32_bf16 v[144:147], v[200:203], a[252:255], v[144:147]\n"
      "v_exp_f32 v32, v48\n"
      "v_mfma_f32_16x16x32_bf16 v[144:147], v[196:199], a[248:251], v[144:147]\n"
      "v_exp_f32 v33, v49\n"
      "v_mfma_f32_16x16x32_bf16 a[128:131], v[204:207], a[248:251], a[128:131]\n"
      "ds_read_b64_tr_b16 a[242:243], v10 offset:6400\n"
      "v_cvt_pk_bf16_f32 v184, v32, v33\n"
      "v_mfma_f32_16x16x32_bf16 a[128:131], v[208:211], a[252:255], a[128:131]\n"
      "ds_read_b64_tr_b16 a[244:245], v10 offset:12544\n"
      "v_or3_b32 v22, v22, v181, v184\n"
      "v_mfma_f32_16x16x32_bf16 a[176:179], v[212:215], a[252:255], a[176:179]\n"
      "s_waitcnt lgkmcnt(6)\n"
      "v_exp_f32 v34, v50\n"
      "v_mfma_f32_16x16x32_bf16 v[216:219], a[208:211], a[0:3], 0\n"
      "v_exp_f32 v35, v51\n"
      "v_mfma_f32_16x16x32_bf16 v[216:219], a[212:215], a[4:7], v[216:219]\n"
      "ds_read_b64_tr_b16 a[246:247], v10 offset:18688\n"
      "v_cvt_pk_bf16_f32 v185, v34, v35\n"
      "v_mfma_f32_16x16x32_bf16 v[216:219], a[216:219], a[8:11], v[216:219]\n"
      "ds_read_b64_tr_b16 a[248:249], v11 offset:256\n"
      "v_mfma_f32_16x16x32_bf16 v[216:219], a[220:223], a[12:15], v[216:219]\n"
      "s_add_u32 m0, s47, 3072\n"
      "ds_read_b64_tr_b16 a[250:251], v11 offset:6400\n"
      "v_mfma_f32_16x16x32_bf16 v[216:219], a[224:227], a[16:19], v[216:219]\n"
      "buffer_load_dwordx4 v1, s[24:27], s39 offen offset:0 lds\n"
      "v_mfma_f32_16x16x32_bf16 v[216:219], a[228:231], a[20:23], v[216:219]\n"
      "buffer_load_dwordx4 v2, s[24:27], s39 offen offset:1024 lds\n"
      "v_mfma_f32_16x16x32_bf16 v[220:223], a[228:231], a[44:47], 0\n"
      "buffer_load_dwordx4 v3, s[24:27], s39 offen offset:2048 lds\n"
      "v_mfma_f32_16x16x32_bf16 v[220:223], a[224:227], a[40:43], v[220:223]\n"
      "s_nop 0\n"
      "ds_read_b64_tr_b16 a[252:253], v11 offset:12544\n"
      "v_mfma_f32_16x16x32_bf16 v[220:223], a[220:223], a[36:39], v[220:223]\n"
      "ds_read_b64_tr_b16 a[254:255], v11 offset:18688\n"
      "v_fma_f32 v216, v216, s48, v18\n"
      "v_mfma_f32_16x16x32_bf16 v[220:223], a[216:219], a[32:35], v[220:223]\n"
      "v_exp_f32 v28, v216\n"
      "v_mfma_f32_16x16x32_bf16 v[220:223], a[212:215], a[28:31], v[220:223]\n"
      "v_fma_f32 v217, v217, s48, v18\n"
      "v_fma_f32 v218, v218, s48, v18\n"
      "v_mfma_f32_16x16x32_bf16 v[220:223], a[208:211], a[24:27], v[220:223]\n"
      "v_exp_f32 v29, v217\n"
      "v_mfma_f32_16x16x32_bf16 v[224:227], a[208:211], a[48:51], 0\n"
      "v_exp_f32 v30, v218\n"
      "v_mfma_f32_16x16x32_bf16 v[224:227], a[212:215], a[52:55], v[224:227]\n"
      "v_fma_f32 v219, v219, s48, v18\n"
      "v_cvt_pk_bf16_f32 v166, v28, v29\n"
      "v_mfma_f32_16x16x32_bf16 v[224:227], a[216:219], a[56:59], v[224:227]\n"
      "v_exp_f32 v31, v219\n"
      "v_mfma_f32_16x16x32_bf16 v[224:227], a[220:223], a[60:63], v[224:227]\n"
      "v_or3_b32 v22, v22, v185, v166\n"
      "v_cvt_pk_bf16_f32 v167, v30, v31\n"
      "v_mfma_f32_16x16x32_bf16 v[224:227], a[224:227], a[64:67], v[224:227]\n"
      "v_fma_f32 v220, v220, s48, v19\n"
      "v_fma_f32 v221, v221, s48, v19\n"
      "v_mfma_f32_16x16x32_bf16 v[224:227], a[228:231], a[68:71], v[224:227]\n"
      "s_mov_b32 s38, s36\n"
      "v_exp_f32 v32, v220\n"
      "v_mfma_f32_16x16x32_bf16 v[228:231], a[228:231], a[92:95], 0\n"
      "s_mov_b32 s39, s37\n"
      "v_exp_f32 v33, v221\n"
      "v_mfma_f32_16x16x32_bf16 v[228:231], a[224:227], a[88:91], v[228:231]\n"
      "s_add_u32 s12, s12, 1\n"
      "v_fma_f32 v222, v222, s48, v19\n"
      "v_fma_f32 v223, v223, s48, v19\n"
      "v_mfma_f32_16x16x32_bf16 v[228:231], a[220:223], a[84:87], v[228:231]\n"
      "s_add_u32 s15, s13, s12\n"
      "v_exp_f32 v34, v222\n"
      "v_mfma_f32_16x16x32_bf16 v[228:231], a[216:219], a[80:83], v[228:231]\n"
      "s_and_b32 s15, s15, 15\n"
      "v_exp_f32 v35, v223\n"
      "v_mfma_f32_16x16x32_bf16 v[228:231], a[212:215], a[76:79], v[228:231]\n"
      "s_waitcnt vmcnt(12) lgkmcnt(0)\n"
      "v_cvt_pk_bf16_f32 v174, v32, v33\n"
      "v_or3_b32 v22, v22, v167, v174\n"
      "v_mfma_f32_16x16x32_bf16 v[228:231], a[208:211], a[72:75], v[228:231]\n"
      "s_barrier\n"
      "v_cvt_pk_bf16_f32 v175, v34, v35\n"
      "v_fma_f32 v224, v224, s48, v20\n"
      "v_mfma_f32_16x16x32_bf16 a[204:207], v[24:27], v[212:215], a[204:207]\n"
      "s_mul_i32 s15, s15, 24576\n"
      "ds_read_b128 a[208:211], v232 offset:0\n"
      "v_fma_f32 v225, v225, s48, v20\n"
      "v_mfma_f32_16x16x32_bf16 v[100:103], v[188:191], a[232:235], v[100:103]\n"
      "s_lshr_b32 s16, s12, 4\n"
      "v_exp_f32 v28, v224\n"
      "v_mfma_f32_16x16x32_bf16 v[100:103], v[192:195], a[236:239], v[100:103]\n"
      "s_mul_i32 s16, s16, 393216\n"
      "ds_read_b128 a[212:215], v233 offset:0\n"
      "v_fma_f32 v226, v226, s48, v20\n"
      "v_mfma_f32_16x16x32_bf16 v[148:151], v[200:203], a[236:239], v[148:151]\n"
      "s_add_u32 s15, s15, s16\n"
      "v_exp_f32 v29, v225\n"
      "v_mfma_f32_16x16x32_bf16 v[148:151], v[196:199], a[232:235], v[148:151]\n"
      "s_add_u32 s36, s15, s14\n"
      "ds_read_b128 a[216:219], v232 offset:128\n"
      "v_fma_f32 v227, v227, s48, v20\n"
      "v_mfma_f32_16x16x32_bf16 a[132:135], v[204:207], a[232:235], a[132:135]\n"
      "s_add_u32 s37, s36, 3072\n"
      "v_exp_f32 v30, v226\n"
      "v_mfma_f32_16x16x32_bf16 a[132:135], v[208:211], a[236:239], a[132:135]\n"
      "ds_read_b128 a[220:223], v233 offset:128\n"
      "v_cvt_pk_bf16_f32 v182, v28, v29\n"
      "v_mfma_f32_16x16x32_bf16 a[180:183], v[212:215], a[236:239], a[180:183]\n"
      "v_exp_f32 v31, v227\n"
      "v_mfma_f32_16x16x32_bf16 v[104:107], v[188:191], a[240:243], v[104:107]\n"
      "ds_read_b128 a[224:227], v232 offset:256\n"
      "v_or3_b32 v22, v22, v175, v182\n"
      "v_mfma_f32_16x16x32_bf16 v[104:107], v[192:195], a[244:247], v[104:107]\n"
      "v_cvt_pk_bf16_f32 v183, v30, v31\n"
      "v_fma_f32 v228, v228, s48, v21\n"
      "v_mfma_f32_16x16x32_bf16 v[152:155], v[200:203], a[244:247], v[152:155]\n"
      "ds_read_b128 a[228:231], v233 offset:256\n"
      "v_fma_f32 v229, v229, s48, v21\n"
      "v_mfma_f32_16x16x32_bf16 v[152:155], v[196:199], a[240:243], v[152:155]\n"
      "v_exp_f32 v32, v228\n"
      "v_mfma_f32_16x16x32_bf16 a[136:139], v[204:207], a[240:243], a[136:139]\n"
      "v_exp_f32 v33, v229\n"
      "v_mfma_f32_16x16x32_bf16 a[136:139], v[208:211], a[244:247], a[136:139]\n"
      "v_fma_f32 v230, v230, s48, v21\n"
      "v_fma_f32 v231, v231, s48, v21\n"
      "v_mfma_f32_16x16x32_bf16 a[184:187], v[212:215], a[244:247], a[184:187]\n"
      "v_exp_f32 v34, v230\n"
      "v_mfma_f32_16x16x32_bf16 v[108:111], v[188:191], a[248:251], v[108:111]\n"
      "v_exp_f32 v35, v231\n"
      "v_mfma_f32_16x16x32_bf16 v[108:111], v[192:195], a[252:255], v[108:111]\n"
      "s_cmp_eq_u32 s50, s86\n"
      "s_cbranch_scc1 L_hook_O\n"
      "L_hook_O_ret:\n"
      "v_cvt_pk_bf16_f32 v186, v32, v33\n"
      "v_mfma_f32_16x16x32_bf16 v[156:159], v[200:203], a[252:255], v[156:159]\n"
      "v_or3_b32 v22, v22, v183, v186\n"
      "v_cvt_pk_bf16_f32 v187, v34, v35\n"
      "v_mfma_f32_16x16x32_bf16 v[156:159], v[196:199], a[248:251], v[156:159]\n"
      "s_sub_u32 s50, s50, 1\n"
      "v_bitop3_b32 v22, v22, v187, s49 bitop3:0xa8\n"
      "v_cmp_ne_u32 vcc, 0, v22\n"
      "v_mfma_f32_16x16x32_bf16 a[140:143], v[204:207], a[248:251], a[140:143]\n"
      "v_mfma_f32_16x16x32_bf16 a[140:143], v[208:211], a[252:255], a[140:143]\n"
      "v_mfma_f32_16x16x32_bf16 a[188:191], v[212:215], a[252:255], a[188:191]\n"
      "s_waitcnt lgkmcnt(0)\n"
      "s_cbranch_vccnz L_rare_Of0_3\n"
      "L_rare_Of0_3_ret:\n"
      "s_branch L_loop_O\n"
      "L_last_O:\n"
      "s_cmp_lg_u32 s51, 0\n"
      "s_cbranch_scc1 L_fin_O\n"
      "ds_read_b64_tr_b16 a[232:233], v8 offset:0\n"
      "ds_read_b64_tr_b16 a[234:235], v8 offset:6144\n"
      "ds_read_b64_tr_b16 a[236:237], v8 offset:12288\n"
      "ds_read_b64_tr_b16 a[238:239], v8 offset:18432\n"
      "ds_read_b64_tr_b16 a[240:241], v9 offset:0\n"
      "ds_read_b64_tr_b16 a[242:243], v9 offset:6144\n"
      "ds_read_b64_tr_b16 a[244:245], v9 offset:12288\n"
      "ds_read_b64_tr_b16 a[246:247], v9 offset:18432\n"
      "ds_read_b64_tr_b16 a[248:249], v10 offset:0\n"
      "ds_read_b64_tr_b16 a[250:251], v10 offset:6144\n"
      "ds_read_b64_tr_b16 a[252:253], v10 offset:12288\n"
      "ds_read_b64_tr_b16 a[254:255], v10 offset:18432\n"
      "s_mov_b32 s20, s90\n"
      "s_mov_b32 s24, s93\n"
      "s_mov_b32 s21, s91\n"
      "s_mov_b32 s25, s94\n"
      "s_mov_b32 s22, s92\n"
      "s_mov_b32 s26, s95\n"
      "s_mov_b32 s36, s84\n"
      "s_add_u32 s37, s84, 3072\n"
      "s_mov_b32 s38, s85\n"
      "s_add_u32 s39, s85, 3072\n"
      "s_add_u32 s46, s44, s41\n"
      "s_add_u32 s47, s45, s40\n"
      "s_sub_u32 s63, s46, s65\n"
      "s_sub_u32 s64, s47, s65\n"
      "s_add_u32 s82, s45, s41\n"
      "s_sub_u32 s83, s82, s65\n"
      "v_smfmac_f32_16x16x64_bf16 a[192:195], v[24:27], v[188:195], v16\n"
      "s_add_u32 m0, s82, 0\n"
      "v_add_u32 v216, s63, v6\n"
      "v_add_u32 v217, s63, v7\n"
      "v_smfmac_f32_16x16x64_bf16 a[196:199], v[24:27], v[196:203], v16\n"
      "s_add_u32 s43, s66, 0\n"
      "v_add_u32 v218, s64, v6\n"
      "v_add_u32 v219, s64, v7\n"
      "v_smfmac_f32_16x16x64_bf16 a[200:203], v[24:27], v[204:211], v16\n"
      "buffer_load_dwordx4 v1, s[28:31], s43 offen offset:0 nt lds\n"
      "v_mfma_f32_16x16x32_bf16 a[204:207], v[24:27], v[212:215], a[204:207]\n"
      "s_waitcnt lgkmcnt(8)\n"
      "buffer_load_dwordx4 v2, s[28:31], s43 offen offset:1024 nt lds\n"
      "v_mfma_f32_16x16x32_bf16 v[64:67], v[188:191], a[232:235], v[64:67]\n"
      "buffer_load_dwordx4 v3, s[28:31], s43 offen offset:2048 nt lds\n"
      "v_mfma_f32_16x16x32_bf16 v[64:67], v[192:195], a[236:239], v[64:67]\n"
      "s_add_u32 m0, s82, 3072\n"
      "v_add_u32 v220, s89, v6\n"
      "v_add_u32 v221, s89, v7\n"
      "v_mfma_f32_16x16x32_bf16 v[112:115], v[200:203], a[236:239], v[112:115]\n"
      "s_add_u32 s43, s66, 3072\n"
      "v_add_u32 v222, s83, v6\n"
      "v_add_u32 v223, s83, v7\n"
      "v_mfma_f32_16x16x32_bf16 v[112:115], v[196:199], a[232:235], v[112:115]\n"
      "buffer_load_dwordx4 v1, s[28:31], s43 offen offset:0 nt lds\n"
      "v_mfma_f32_16x16x32_bf16 a[96:99], v[204:207], a[232:235], a[96:99]\n"
      "buffer_load_dwordx4 v2, s[28:31], s43 offen offset:1024 nt lds\n"
      "v_mfma_f32_16x16x32_bf16 a[96:99], v[208:211], a[236:239], a[96:99]\n"
      "buffer_load_dwordx4 v3, s[28:31], s43 offen offset:2048 nt lds\n"
      "v_mfma_f32_16x16x32_bf16 a[144:147], v[212:215], a[236:239], a[144:147]\n"
      "s_waitcnt lgkmcnt(4)\n"
      "ds_read_b64_tr_b16 a[232:233], v11 offset:0\n"
      "v_accvgpr_read_b32 v238, a192\n"
      "v_mfma_f32_16x16x32_bf16 v[68:71], v[188:191], a[240:243], v[68:71]\n"
      "ds_read_b64_tr_b16 a[234:235], v11 offset:6144\n"
      "ds_read_b64_tr_b16 a[236:237], v11 offset:12288\n"
      "v_mfma_f32_16x16x32_bf16 v[68:71], v[192:195], a[244:247], v[68:71]\n"
      "ds_read_b64_tr_b16 a[238:239], v11 offset:18432\n"
      "v_accvgpr_read_b32 v242, a196\n"
      "v_mfma_f32_16x16x32_bf16 v[116:119], v[200:203], a[244:247], v[116:119]\n"
      "v_accvgpr_read_b32 v246, a200\n"
      "v_accvgpr_read_b32 v239, a193\n"
      "v_mfma_f32_16x16x32_bf16 v[116:119], v[196:199], a[240:243], v[116:119]\n"
      "v_accvgpr_read_b32 v243, a197\n"
      "v_accvgpr_read_b32 v247, a201\n"
      "v_mfma_f32_16x16x32_bf16 a[100:103], v[204:207], a[240:243], a[100:103]\n"
      "s_lshl_b32 s63, s61, 9\n"
      "v_mfma_f32_16x16x32_bf16 a[100:103], v[208:211], a[244:247], a[100:103]\n"
      "s_add_u32 s63, s63, s65\n"
      "v_mfma_f32_16x16x32_bf16 a[148:151], v[212:215], a[244:247], a[148:151]\n"
      "s_waitcnt lgkmcnt(4)\n"
      "ds_read_b64_tr_b16 a[240:241], v8 offset:128\n"
      "ds_read_b64_tr_b16 a[242:243], v8 offset:6272\n"
      "v_mfma_f32_16x16x32_bf16 v[72:75], v[188:191], a[248:251], v[72:75]\n"
      "s_add_u32 s63, s63, 161792\n"
      "ds_read_b64_tr_b16 a[244:245], v8 offset:12416\n"
      "ds_read_b64_tr_b16 a[246:247], v8 offset:18560\n"
      "v_mfma_f32_16x16x32_bf16 v[72:75], v[192:195], a[252:255], v[72:75]\n"
      "v_and_b32 v236, 15, v0\n"
      "v_lshl_add_u32 v236, v236, 2, s63\n"
      "v_mfma_f32_16x16x32_bf16 v[120:123], v[200:203], a[252:255], v[120:123]\n"
      "s_waitcnt vmcnt(6)\n"
      "v_and_b32 v237, 48, v0\n"
      "v_add_u32 v237, s63, v237\n"
      "v_mfma_f32_16x16x32_bf16 v[120:123], v[196:199], a[248:251], v[120:123]\n"
      "ds_read_b128 a[24:27], v216 offset:0\n"
      "v_add_f32 v238, v238, v239\n"
      "v_mfma_f32_16x16x32_bf16 a[104:107], v[204:207], a[248:251], a[104:107]\n"
      "ds_read_b128 a[28:31], v217 offset:0\n"
      "v_add_f32 v242, v242, v243\n"
      "v_mfma_f32_16x16x32_bf16 a[104:107], v[208:211], a[252:255], a[104:107]\n"
      "ds_read_b128 a[32:35], v216 offset:128\n"
      "v_add_f32 v246, v246, v247\n"
      "v_mfma_f32_16x16x32_bf16 a[152:155], v[212:215], a[252:255], a[152:155]\n"
      "s_waitcnt lgkmcnt(7)\n"
      "ds_read_b64_tr_b16 a[248:249], v9 offset:128\n"
      "ds_read_b64_tr_b16 a[250:251], v9 offset:6272\n"
      "v_mfma_f32_16x16x32_bf16 v[76:79], v[188:191], a[232:235], v[76:79]\n"
      "s_add_u32 s82, s45, s41\n"
      "ds_read_b64_tr_b16 a[252:253], v9 offset:12416\n"
      "ds_read_b64_tr_b16 a[254:255], v9 offset:18560\n"
      "v_mfma_f32_16x16x32_bf16 v[76:79], v[192:195], a[236:239], v[76:79]\n"
      "v_rcp_f32 v238, v238\n"
      "v_mfma_f32_16x16x32_bf16 v[124:127], v[200:203], a[236:239], v[124:127]\n"
      "ds_read_b128 a[36:39], v217 offset:128\n"
      "v_mfma_f32_16x16x32_bf16 v[124:127], v[196:199], a[232:235], v[124:127]\n"
      "ds_read_b128 a[40:43], v216 offset:256\n"
      "v_mfma_f32_16x16x32_bf16 a[108:111], v[204:207], a[232:235], a[108:111]\n"
      "ds_read_b128 a[44:47], v217 offset:256\n"
      "v_mfma_f32_16x16x32_bf16 a[108:111], v[208:211], a[236:239], a[108:111]\n"
      "ds_read_b128 a[48:51], v218 offset:0\n"
      "v_mfma_f32_16x16x32_bf16 a[156:159], v[212:215], a[236:239], a[156:159]\n"
      "s_waitcnt lgkmcnt(11)\n"
      "ds_read_b128 a[52:55], v219 offset:0\n"
      "ds_read_b64_tr_b16 a[232:233], v10 offset:128\n"
      "v_mfma_f32_16x16x32_bf16 v[80:83], v[188:191], a[240:243], v[80:83]\n"
      "ds_read_b64_tr_b16 a[234:235], v10 offset:6272\n"
      "ds_read_b64_tr_b16 a[236:237], v10 offset:12416\n"
      "v_mfma_f32_16x16x32_bf16 v[80:83], v[192:195], a[244:247], v[80:83]\n"
      "ds_read_b64_tr_b16 a[238:239], v10 offset:18560\n"
      "ds_read_b128 a[56:59], v218 offset:128\n"
      "v_mfma_f32_16x16x32_bf16 v[128:131], v[200:203], a[244:247], v[128:131]\n"
      "ds_read_b128 a[60:63], v219 offset:128\n"
      "v_mfma_f32_16x16x32_bf16 v[128:131], v[196:199], a[240:243], v[128:131]\n"
      "ds_read_b128 a[64:67], v218 offset:256\n"
      "v_mfma_f32_16x16x32_bf16 a[112:115], v[204:207], a[240:243], a[112:115]\n"
      "ds_read_b128 a[68:71], v219 offset:256\n"
      "v_mfma_f32_16x16x32_bf16 a[112:115], v[208:211], a[244:247], a[112:115]\n"
      "ds_read_b128 a[72:75], v220 offset:0\n"
      "v_mfma_f32_16x16x32_bf16 a[160:163], v[212:215], a[244:247], a[160:163]\n"
      "s_waitcnt lgkmcnt(14)\n"
      "ds_read_b128 a[76:79], v221 offset:0\n"
      "ds_read_b64_tr_b16 a[240:241], v11 offset:128\n"
      "v_mfma_f32_16x16x32_bf16 v[84:87], v[188:191], a[248:251], v[84:87]\n"
      "ds_read_b128 a[80:83], v220 offset:128\n"
      "ds_read_b64_tr_b16 a[242:243], v11 offset:6272\n"
      "v_mfma_f32_16x16x32_bf16 v[84:87], v[192:195], a[252:255], v[84:87]\n"
      "ds_read_b64_tr_b16 a[244:245], v11 offset:12416\n"
      "ds_read_b64_tr_b16 a[246:247], v11 offset:18560\n"
      "ds_read_b128 a[84:87], v221 offset:128\n"
      "v_mfma_f32_16x16x32_bf16 v[132:135], v[200:203], a[252:255], v[132:135]\n"
      "ds_read_b128 a[88:91], v220 offset:256\n"
      "v_mfma_f32_16x16x32_bf16 v[132:135], v[196:199], a[248:251], v[132:135]\n"
      "ds_read_b128 a[92:95], v221 offset:256\n"
      "v_mfma_f32_16x16x32_bf16 a[116:119], v[204:207], a[248:251], a[116:119]\n"
      "v_rcp_f32 v242, v242\n"
      "v_mfma_f32_16x16x32_bf16 a[116:119], v[208:211], a[252:255], a[116:119]\n"
      "v_rcp_f32 v246, v246\n"
      "v_mfma_f32_16x16x32_bf16 a[164:167], v[212:215], a[252:255], a[164:167]\n"
      "s_waitcnt lgkmcnt(14)\n"
      "ds_read_b64_tr_b16 a[248:249], v8 offset:256\n"
      "v_add_u32 v232, s82, v251\n"
      "v_mfma_f32_16x16x32_bf16 v[88:91], v[188:191], a[232:235], v[88:91]\n"
      "s_waitcnt lgkmcnt(1)\n"
      "ds_read_b64_tr_b16 a[250:251], v8 offset:6400\n"
      "ds_read_b64_tr_b16 a[252:253], v8 offset:12544\n"
      "v_mfma_f32_16x16x32_bf16 v[88:91], v[192:195], a[236:239], v[88:91]\n"
      "s_mov_b32 m0, s46\n"
      "ds_read_b64_tr_b16 a[254:255], v8 offset:18688\n"
      "ds_write_b32 v236, v238 offset:0\n"
      "v_mfma_f32_16x16x32_bf16 v[136:139], v[200:203], a[236:239], v[136:139]\n"
      "buffer_load_dwordx4 v1, s[20:23], s36 offen offset:0 lds\n"
      "v_mfma_f32_16x16x32_bf16 v[136:139], v[196:199], a[232:235], v[136:139]\n"
      "ds_write_b32 v236, v242 offset:64\n"
      "ds_write_b32 v236, v246 offset:128\n"
      "v_mfma_f32_16x16x32_bf16 a[120:123], v[204:207], a[232:235], a[120:123]\n"
      "buffer_load_dwordx4 v2, s[20:23], s36 offen offset:1024 lds\n"
      "v_mfma_f32_16x16x32_bf16 a[120:123], v[208:211], a[236:239], a[120:123]\n"
      "ds_read_b128 v[238:241], v237 offset:0\n"
      "ds_read_b128 v[242:245], v237 offset:64\n"
      "v_mfma_f32_16x16x32_bf16 a[168:171], v[212:215], a[236:239], a[168:171]\n"
      "s_waitcnt lgkmcnt(9)\n"
      "buffer_load_dwordx4 v3, s[20:23], s36 offen offset:2048 lds\n"
      "v_mfma_f32_16x16x32_bf16 v[92:95], v[188:191], a[240:243], v[92:95]\n"
      "s_add_u32 m0, s46, 3072\n"
      "ds_read_b64_tr_b16 a[232:233], v9 offset:256\n"
      "ds_read_b64_tr_b16 a[234:235], v9 offset:6400\n"
      "v_mfma_f32_16x16x32_bf16 v[92:95], v[192:195], a[244:247], v[92:95]\n"
      "ds_read_b64_tr_b16 a[236:237], v9 offset:12544\n"
      "ds_read_b64_tr_b16 a[238:239], v9 offset:18688\n"
      "v_mfma_f32_16x16x32_bf16 v[140:143], v[200:203], a[244:247], v[140:143]\n"
      "buffer_load_dwordx4 v1, s[20:23], s37 offen offset:0 lds\n"
      "v_mfma_f32_16x16x32_bf16 v[140:143], v[196:199], a[240:243], v[140:143]\n"
      "ds_read_b128 v[246:249], v237 offset:128\n"
      "v_add_u32 v234, s82, v17\n"
      "v_mfma_f32_16x16x32_bf16 a[124:127], v[204:207], a[240:243], a[124:127]\n"
      "buffer_load_dwordx4 v2, s[20:23], s37 offen offset:1024 lds\n"
      "v_mfma_f32_16x16x32_bf16 a[124:127], v[208:211], a[244:247], a[124:127]\n"
      "v_xor_b32 v233, 32, v232\n"
      "v_xor_b32 v235, 32, v234\n"
      "v_mfma_f32_16x16x32_bf16 a[172:175], v[212:215], a[244:247], a[172:175]\n"
      "s_waitcnt lgkmcnt(10)\n"
      "buffer_load_dwordx4 v3, s[20:23], s37 offen offset:2048 lds\n"
      "v_mfma_f32_16x16x32_bf16 v[96:99], v[188:191], a[248:251], v[96:99]\n"
      "s_mov_b32 m0, s47\n"
      "ds_read_b64_tr_b16 a[240:241], v10 offset:256\n"
      "ds_read_b64_tr_b16 a[242:243], v10 offset:6400\n"
      "v_mfma_f32_16x16x32_bf16 v[96:99], v[192:195], a[252:255], v[96:99]\n"
      "ds_read_b64_tr_b16 a[244:245], v10 offset:12544\n"
      "ds_read_b64_tr_b16 a[246:247], v10 offset:18688\n"
      "v_mfma_f32_16x16x32_bf16 v[144:147], v[200:203], a[252:255], v[144:147]\n"
      "buffer_load_dwordx4 v1, s[24:27], s38 offen offset:0 lds\n"
      "v_mfma_f32_16x16x32_bf16 v[144:147], v[196:199], a[248:251], v[144:147]\n"
      "v_lshlrev_b32 v23, 4, v0\n"
      "v_mov_b32 v28, 0\n"
      "v_mfma_f32_16x16x32_bf16 a[128:131], v[204:207], a[248:251], a[128:131]\n"
      "buffer_load_dwordx4 v2, s[24:27], s38 offen offset:1024 lds\n"
      "v_mfma_f32_16x16x32_bf16 a[128:131], v[208:211], a[252:255], a[128:131]\n"
      "v_mov_b32 v29, 0\n"
      "v_mov_b32 v30, 0\n"
      "v_mfma_f32_16x16x32_bf16 a[176:179], v[212:215], a[252:255], a[176:179]\n"
      "s_waitcnt lgkmcnt(5)\n"
      "buffer_load_dwordx4 v3, s[24:27], s38 offen offset:2048 lds\n"
      "v_mfma_f32_16x16x32_bf16 v[100:103], v[188:191], a[232:235], v[100:103]\n"
      "s_add_u32 m0, s47, 3072\n"
      "ds_read_b64_tr_b16 a[248:249], v11 offset:256\n"
      "ds_read_b64_tr_b16 a[250:251], v11 offset:6400\n"
      "v_mfma_f32_16x16x32_bf16 v[100:103], v[192:195], a[236:239], v[100:103]\n"
      "ds_read_b64_tr_b16 a[252:253], v11 offset:12544\n"
      "ds_read_b64_tr_b16 a[254:255], v11 offset:18688\n"
      "v_mfma_f32_16x16x32_bf16 v[148:151], v[200:203], a[236:239], v[148:151]\n"
      "buffer_load_dwordx4 v1, s[24:27], s39 offen offset:0 lds\n"
      "v_mfma_f32_16x16x32_bf16 v[148:151], v[196:199], a[232:235], v[148:151]\n"
      "v_mov_b32 v31, 0\n"
      "v_mfma_f32_16x16x32_bf16 a[132:135], v[204:207], a[232:235], a[132:135]\n"
      "s_add_u32 s63, s44, s42\n"
      "buffer_load_dwordx4 v2, s[24:27], s39 offen offset:1024 lds\n"
      "v_mfma_f32_16x16x32_bf16 a[132:135], v[208:211], a[236:239], a[132:135]\n"
      "s_sub_u32 s64, s63, 6144\n"
      "v_add_u32 v52, s64, v23\n"
      "v_add_u32 v23, s63, v23\n"
      "v_mfma_f32_16x16x32_bf16 a[180:183], v[212:215], a[236:239], a[180:183]\n"
      "s_waitcnt lgkmcnt(4)\n"
      "buffer_load_dwordx4 v3, s[24:27], s39 offen offset:2048 lds\n"
      "v_mfma_f32_16x16x32_bf16 v[104:107], v[188:191], a[240:243], v[104:107]\n"
      "s_mov_b32 s38, s36\n"
      "v_mfma_f32_16x16x32_bf16 v[104:107], v[192:195], a[244:247], v[104:107]\n"
      "s_mov_b32 s39, s37\n"
      "v_mfma_f32_16x16x32_bf16 v[152:155], v[200:203], a[244:247], v[152:155]\n"
      "s_add_u32 s15, s13, s12\n"
      "v_mfma_f32_16x16x32_bf16 v[152:155], v[196:199], a[240:243], v[152:155]\n"
      "s_and_b32 s15, s15, 15\n"
      "v_mfma_f32_16x16x32_bf16 a[136:139], v[204:207], a[240:243], a[136:139]\n"
      "s_mul_i32 s15, s15, 24576\n"
      "v_mfma_f32_16x16x32_bf16 a[136:139], v[208:211], a[244:247], a[136:139]\n"
      "s_lshr_b32 s16, s12, 4\n"
      "v_mfma_f32_16x16x32_bf16 a[184:187], v[212:215], a[244:247], a[184:187]\n"
      "s_waitcnt lgkmcnt(0)\n"
      "s_waitcnt vmcnt(12)\n"
      "v_mfma_f32_16x16x32_bf16 v[108:111], v[188:191], a[248:251], v[108:111]\n"
      "s_mul_i32 s16, s16, 393216\n"
      "ds_read_b128 a[0:3], v222 offset:0\n"
      "v_mfma_f32_16x16x32_bf16 v[108:111], v[192:195], a[252:255], v[108:111]\n"
      "s_add_u32 s15, s15, s16\n"
      "ds_read_b128 a[4:7], v223 offset:0\n"
      "v_mfma_f32_16x16x32_bf16 v[156:159], v[200:203], a[252:255], v[156:159]\n"
      "s_add_u32 s36, s15, s14\n"
      "ds_read_b128 a[8:11], v222 offset:128\n"
      "v_mfma_f32_16x16x32_bf16 v[156:159], v[196:199], a[248:251], v[156:159]\n"
      "s_add_u32 s37, s36, 3072\n"
      "ds_read_b128 a[12:15], v223 offset:128\n"
      "v_mfma_f32_16x16x32_bf16 a[140:143], v[204:207], a[248:251], a[140:143]\n"
      "s_lshl_b32 s63, s61, 9\n"
      "s_add_u32 s63, s63, s65\n"
      "ds_read_b128 a[16:19], v222 offset:256\n"
      "v_mfma_f32_16x16x32_bf16 a[140:143], v[208:211], a[252:255], a[140:143]\n"
      "s_add_u32 s63, s63, 147456\n"
      "ds_read_b128 a[20:23], v223 offset:256\n"
      "v_lshlrev_b32 v53, 3, v0\n"
      "v_mfma_f32_16x16x32_bf16 a[188:191], v[212:215], a[252:255], a[188:191]\n"
      "v_add_u32 v53, s63, v53\n"
      "v_mov_b32 v54, v21\n"
      "v_accvgpr_read_b32 v55, a204\n"
      "v_xor_b32 v58, 512, v53\n"
      "s_waitcnt lgkmcnt(6)\n"
      "v_pk_mul_f32 v[64:65], v[64:65], v[238:239]\n"
      "v_pk_mul_f32 v[66:67], v[66:67], v[240:241]\n"
      "v_pk_mul_f32 v[68:69], v[68:69], v[238:239]\n"
      "v_pk_mul_f32 v[70:71], v[70:71], v[240:241]\n"
      "v_pk_mul_f32 v[72:73], v[72:73], v[238:239]\n"
      "v_pk_mul_f32 v[74:75], v[74:75], v[240:241]\n"
      "v_pk_mul_f32 v[76:77], v[76:77], v[238:239]\n"
      "v_pk_mul_f32 v[78:79], v[78:79], v[240:241]\n"
      "ds_write_b64 v53, v[54:55] offset:0\n"
      "ds_write_b128 v52, a[144:147] offset:0\n"
      "v_cvt_pk_bf16_f32 v176, v64, v68\n"
      "v_cvt_pk_bf16_f32 v177, v72, v76\n"
      "v_cvt_pk_bf16_f32 v178, v65, v69\n"
      "v_cvt_pk_bf16_f32 v179, v73, v77\n"
      "v_cvt_pk_bf16_f32 v180, v66, v70\n"
      "v_cvt_pk_bf16_f32 v181, v74, v78\n"
      "v_cvt_pk_bf16_f32 v182, v67, v71\n"
      "v_cvt_pk_bf16_f32 v183, v75, v79\n"
      "ds_write_b128 v52, a[148:151] offset:1024\n"
      "v_pk_mul_f32 v[80:81], v[80:81], v[238:239]\n"
      "v_pk_mul_f32 v[82:83], v[82:83], v[240:241]\n"
      "v_pk_mul_f32 v[84:85], v[84:85], v[238:239]\n"
      "ds_write_b128 v52, a[152:155] offset:2048\n"
      "ds_write_b128 v52, a[156:159] offset:3072\n"
      "v_pk_mul_f32 v[86:87], v[86:87], v[240:241]\n"
      "v_pk_mul_f32 v[88:89], v[88:89], v[238:239]\n"
      "v_pk_mul_f32 v[90:91], v[90:91], v[240:241]\n"
      "ds_write_b64 v232, v[176:177] offset:0\n"
      "ds_write_b64 v232, v[178:179] offset:128\n"
      "v_pk_mul_f32 v[92:93], v[92:93], v[238:239]\n"
      "v_pk_mul_f32 v[94:95], v[94:95], v[240:241]\n"
      "v_mfma_f32_32x32x16_bf16 v[64:79], v[28:31], v[28:31], 0\n"
      "v_cvt_pk_bf16_f32 v184, v80, v84\n"
      "ds_write_b64 v233, v[180:181] offset:256\n"
      "ds_write_b64 v233, v[182:183] offset:384\n"
      "v_cvt_pk_bf16_f32 v185, v88, v92\n"
      "v_cvt_pk_bf16_f32 v186, v81, v85\n"
      "v_cvt_pk_bf16_f32 v187, v89, v93\n"
      "ds_read_b64_tr_b16 v[192:193], v234 offset:0\n"
      "ds_read_b64_tr_b16 v[194:195], v235 offset:0\n"
      "v_cvt_pk_bf16_f32 v188, v82, v86\n"
      "v_cvt_pk_bf16_f32 v189, v90, v94\n"
      "v_cvt_pk_bf16_f32 v190, v83, v87\n"
      "v_cvt_pk_bf16_f32 v191, v91, v95\n"
      "ds_read_b64_tr_b16 v[196:197], v234 offset:1024\n"
      "ds_read_b64_tr_b16 v[198:199], v235 offset:1024\n"
      "v_pk_mul_f32 v[96:97], v[96:97], v[238:239]\n"
      "v_pk_mul_f32 v[98:99], v[98:99], v[240:241]\n"
      "v_pk_mul_f32 v[100:101], v[100:101], v[238:239]\n"
      "v_pk_mul_f32 v[102:103], v[102:103], v[240:241]\n"
      "ds_write_b128 v52, a[160:163] offset:4096\n"
      "ds_write_b128 v52, a[164:167] offset:5120\n"
      "v_pk_mul_f32 v[104:105], v[104:105], v[238:239]\n"
      "v_pk_mul_f32 v[106:107], v[106:107], v[240:241]\n"
      "v_pk_mul_f32 v[108:109], v[108:109], v[238:239]\n"
      "v_pk_mul_f32 v[110:111], v[110:111], v[240:241]\n"
      "ds_write_b64 v232, v[184:185] offset:2048\n"
      "ds_write_b64 v232, v[186:187] offset:2176\n"
      "v_mfma_f32_32x32x16_bf16 v[80:95], v[28:31], v[28:31], 0\n"
      "v_cvt_pk_bf16_f32 v176, v96, v100\n"
      "s_waitcnt lgkmcnt(4)\n"
      "v_cvt_pk_bf16_f32 v177, v104, v108\n"
      "ds_write_b64 v233, v[188:189] offset:2304\n"
      "ds_write_b64 v233, v[190:191] offset:2432\n"
      "buffer_store_dwordx4 v[192:195], v250, s[72:75], s52 offen offset:0\n"
      "v_cvt_pk_bf16_f32 v178, v97, v101\n"
      "v_cvt_pk_bf16_f32 v179, v105, v109\n"
      "v_cvt_pk_bf16_f32 v180, v98, v102\n"
      "ds_read_b64_tr_b16 v[200:201], v234 offset:2048\n"
      "ds_read_b64_tr_b16 v[202:203], v235 offset:2048\n"
      "buffer_store_dwordx4 v[196:199], v250, s[72:75], s52 offen offset:3072\n"
      "v_cvt_pk_bf16_f32 v181, v106, v110\n"
      "v_cvt_pk_bf16_f32 v182, v99, v103\n"
      "v_cvt_pk_bf16_f32 v183, v107, v111\n"
      "ds_read_b64_tr_b16 v[204:205], v234 offset:3072\n"
      "ds_read_b64_tr_b16 v[206:207], v235 offset:3072\n"
      "v_pk_mul_f32 v[112:113], v[112:113], v[242:243]\n"
      "v_pk_mul_f32 v[114:115], v[114:115], v[244:245]\n"
      "v_pk_mul_f32 v[116:117], v[116:117], v[242:243]\n"
      "v_pk_mul_f32 v[118:119], v[118:119], v[244:245]\n"
      "v_pk_mul_f32 v[120:121], v[120:121], v[242:243]\n"
      "ds_write_b64 v232, v[176:177] offset:4096\n"
      "ds_write_b64 v232, v[178:179] offset:4224\n"
      "v_pk_mul_f32 v[122:123], v[122:123], v[244:245]\n"
      "v_pk_mul_f32 v[124:125], v[124:125], v[242:243]\n"
      "v_pk_mul_f32 v[126:127], v[126:127], v[244:245]\n"
      "v_mfma_f32_32x32x16_bf16 v[96:111], v[28:31], v[28:31], 0\n"
      "v_cvt_pk_bf16_f32 v184, v112, v116\n"
      "ds_write_b64 v233, v[180:181] offset:4352\n"
      "ds_write_b64 v233, v[182:183] offset:4480\n"
      "s_waitcnt lgkmcnt(4)\n"
      "v_cvt_pk_bf16_f32 v185, v120, v124\n"
      "buffer_store_dwordx4 v[200:203], v250, s[72:75], s52 offen offset:128\n"
      "v_cvt_pk_bf16_f32 v186, v113, v117\n"
      "v_cvt_pk_bf16_f32 v187, v121, v125\n"
      "ds_read_b64_tr_b16 v[208:209], v234 offset:4096\n"
      "ds_read_b64_tr_b16 v[210:211], v235 offset:4096\n"
      "v_cvt_pk_bf16_f32 v188, v114, v118\n"
      "buffer_store_dwordx4 v[204:207], v250, s[72:75], s52 offen offset:3200\n"
      "v_cvt_pk_bf16_f32 v189, v122, v126\n"
      "v_cvt_pk_bf16_f32 v190, v115, v119\n"
      "v_cvt_pk_bf16_f32 v191, v123, v127\n"
      "ds_read_b64_tr_b16 v[212:213], v234 offset:5120\n"
      "ds_read_b64_tr_b16 v[214:215], v235 offset:5120\n"
      "v_pk_mul_f32 v[128:129], v[128:129], v[242:243]\n"
      "v_pk_mul_f32 v[130:131], v[130:131], v[244:245]\n"
      "v_pk_mul_f32 v[132:133], v[132:133], v[242:243]\n"
      "v_pk_mul_f32 v[134:135], v[134:135], v[244:245]\n"
      "v_pk_mul_f32 v[136:137], v[136:137], v[242:243]\n"
      "ds_write_b64 v232, v[184:185] offset:0\n"
      "ds_write_b64 v232, v[186:187] offset:128\n"
      "v_pk_mul_f32 v[138:139], v[138:139], v[244:245]\n"
      "v_pk_mul_f32 v[140:141], v[140:141], v[242:243]\n"
      "v_pk_mul_f32 v[142:143], v[142:143], v[244:245]\n"
      "v_mfma_f32_32x32x16_bf16 v[112:127], v[28:31], v[28:31], 0\n"
      "v_cvt_pk_bf16_f32 v176, v128, v132\n"
      "ds_write_b64 v233, v[188:189] offset:256\n"
      "ds_write_b64 v233, v[190:191] offset:384\n"
      "s_waitcnt lgkmcnt(4)\n"
      "v_cvt_pk_bf16_f32 v177, v136, v140\n"
      "buffer_store_dwordx4 v[208:211], v250, s[72:75], s52 offen offset:256\n"
      "v_cvt_pk_bf16_f32 v178, v129, v133\n"
      "v_cvt_pk_bf16_f32 v179, v137, v141\n"
      "ds_read_b64_tr_b16 v[192:193], v234 offset:0\n"
      "ds_read_b64_tr_b16 v[194:195], v235 offset:0\n"
      "v_cvt_pk_bf16_f32 v180, v130, v134\n"
      "buffer_store_dwordx4 v[212:215], v250, s[72:75], s52 offen offset:3328\n"
      "v_cvt_pk_bf16_f32 v181, v138, v142\n"
      "v_cvt_pk_bf16_f32 v182, v131, v135\n"
      "v_cvt_pk_bf16_f32 v183, v139, v143\n"
      "ds_read_b64_tr_b16 v[196:197], v234 offset:1024\n"
      "ds_read_b64_tr_b16 v[198:199], v235 offset:1024\n"
      "s_waitcnt lgkmcnt(8)\n"
      "s_barrier\n"
      "v_pk_mul_f32 v[144:145], v[144:145], v[242:243]\n"
      "v_pk_mul_f32 v[146:147], v[146:147], v[244:245]\n"
      "v_pk_mul_f32 v[148:149], v[148:149], v[242:243]\n"
      "ds_write_b64 v232, v[176:177] offset:2048\n"
      "ds_write_b64 v232, v[178:179] offset:2176\n"
      "v_pk_mul_f32 v[150:151], v[150:151], v[244:245]\n"
      "v_pk_mul_f32 v[152:153], v[152:153], v[242:243]\n"
      "v_pk_mul_f32 v[154:155], v[154:155], v[244:245]\n"
      "ds_write_b64 v233, v[180:181] offset:2304\n"
      "ds_write_b64 v233, v[182:183] offset:2432\n"
      "v_pk_mul_f32 v[156:157], v[156:157], v[242:243]\n"
      "v_pk_mul_f32 v[158:159], v[158:159], v[244:245]\n"
      "v_mfma_f32_32x32x16_bf16 v[128:143], v[28:31], v[28:31], 0\n"
      "v_cvt_pk_bf16_f32 v184, v144, v148\n"
      "ds_read_b64_tr_b16 v[200:201], v234 offset:2048\n"
      "ds_read_b64_tr_b16 v[202:203], v235 offset:2048\n"
      "s_waitcnt lgkmcnt(6)\n"
      "v_cvt_pk_bf16_f32 v185, v152, v156\n"
      "buffer_store_dwordx4 v[192:195], v250, s[72:75], s53 offen offset:0\n"
      "ds_read_b64_tr_b16 v[204:205], v234 offset:3072\n"
      "ds_read_b64_tr_b16 v[206:207], v235 offset:3072\n"
      "v_cvt_pk_bf16_f32 v186, v145, v149\n"
      "v_cvt_pk_bf16_f32 v187, v153, v157\n"
      "v_cvt_pk_bf16_f32 v188, v146, v150\n"
      "ds_read_b64 v[56:57], v58 offset:0\n"
      "ds_read_b128 v[36:39], v23 offset:0\n"
      "buffer_store_dwordx4 v[196:199], v250, s[72:75], s53 offen offset:3072\n"
      "v_cvt_pk_bf16_f32 v189, v154, v158\n"
      "v_cvt_pk_bf16_f32 v190, v147, v151\n"
      "v_cvt_pk_bf16_f32 v191, v155, v159\n"
      "ds_read_b128 v[40:43], v23 offset:1024\n"
      "ds_read_b128 v[44:47], v23 offset:2048\n"
      "v_mfma_f32_32x32x16_bf16 v[144:159], v[28:31], v[28:31], 0\n"
      "v_accvgpr_read_b32 v160, a96\n"
      "v_accvgpr_read_b32 v161, a97\n"
      "v_accvgpr_read_b32 v162, a98\n"
      "v_accvgpr_read_b32 v163, a99\n"
      "v_accvgpr_read_b32 v164, a100\n"
      "ds_write_b64 v232, v[184:185] offset:4096\n"
      "ds_write_b64 v232, v[186:187] offset:4224\n"
      "v_accvgpr_read_b32 v165, a101\n"
      "v_accvgpr_read_b32 v166, a102\n"
      "v_accvgpr_read_b32 v167, a103\n"
      "v_accvgpr_read_b32 v168, a104\n"
      "v_accvgpr_read_b32 v169, a105\n"
      "v_accvgpr_read_b32 v170, a106\n"
      "ds_write_b64 v233, v[188:189] offset:4352\n"
      "ds_write_b64 v233, v[190:191] offset:4480\n"
      "v_accvgpr_read_b32 v171, a107\n"
      "v_accvgpr_read_b32 v172, a108\n"
      "v_accvgpr_read_b32 v173, a109\n"
      "v_accvgpr_read_b32 v174, a110\n"
      "v_accvgpr_read_b32 v175, a111\n"
      "v_pk_mul_f32 v[160:161], v[160:161], v[246:247]\n"
      "ds_read_b64_tr_b16 v[208:209], v234 offset:4096\n"
      "ds_read_b64_tr_b16 v[210:211], v235 offset:4096\n"
      "v_pk_mul_f32 v[162:163], v[162:163], v[248:249]\n"
      "v_pk_mul_f32 v[164:165], v[164:165], v[246:247]\n"
      "v_pk_mul_f32 v[166:167], v[166:167], v[248:249]\n"
      "v_pk_mul_f32 v[168:169], v[168:169], v[246:247]\n"
      "v_pk_mul_f32 v[170:171], v[170:171], v[248:249]\n"
      "v_pk_mul_f32 v[172:173], v[172:173], v[246:247]\n"
      "ds_read_b64_tr_b16 v[212:213], v234 offset:5120\n"
      "ds_read_b64_tr_b16 v[214:215], v235 offset:5120\n"
      "v_pk_mul_f32 v[174:175], v[174:175], v[248:249]\n"
      "v_cvt_pk_bf16_f32 v176, v160, v164\n"
      "s_waitcnt lgkmcnt(12)\n"
      "v_cvt_pk_bf16_f32 v177, v168, v172\n"
      "buffer_store_dwordx4 v[200:203], v250, s[72:75], s53 offen offset:128\n"
      "v_cvt_pk_bf16_f32 v178, v161, v165\n"
      "ds_read_b128 v[48:51], v23 offset:3072\n"
      "ds_read_b128 v[216:219], v23 offset:4096\n"
      "v_cvt_pk_bf16_f32 v179, v169, v173\n"
      "v_cvt_pk_bf16_f32 v180, v162, v166\n"
      "buffer_store_dwordx4 v[204:207], v250, s[72:75], s53 offen offset:3200\n"
      "v_cvt_pk_bf16_f32 v181, v170, v174\n"
      "v_cvt_pk_bf16_f32 v182, v163, v167\n"
      "v_cvt_pk_bf16_f32 v183, v171, v175\n"
      "ds_read_b128 v[220:223], v23 offset:5120\n"
      "v_mfma_f32_32x32x16_bf16 a[96:111], v[28:31], v[28:31], 0\n"
      "v_accvgpr_read_b32 v160, a112\n"
      "v_accvgpr_read_b32 v161, a113\n"
      "v_accvgpr_read_b32 v162, a114\n"
      "v_accvgpr_read_b32 v163, a115\n"
      "v_accvgpr_read_b32 v164, a116\n"
      "v_accvgpr_read_b32 v165, a117\n"
      "v_accvgpr_read_b32 v166, a118\n"
      "v_accvgpr_read_b32 v167, a119\n"
      "ds_write_b64 v232, v[176:177] offset:0\n"
      "ds_write_b64 v232, v[178:179] offset:128\n"
      "v_accvgpr_read_b32 v168, a120\n"
      "v_accvgpr_read_b32 v169, a121\n"
      "v_accvgpr_read_b32 v170, a122\n"
      "v_accvgpr_read_b32 v171, a123\n"
      "v_accvgpr_read_b32 v172, a124\n"
      "v_accvgpr_read_b32 v173, a125\n"
      "v_accvgpr_read_b32 v174, a126\n"
      "v_accvgpr_read_b32 v175, a127\n"
      "v_pk_mul_f32 v[160:161], v[160:161], v[246:247]\n"
      "ds_write_b64 v233, v[180:181] offset:256\n"
      "ds_write_b64 v233, v[182:183] offset:384\n"
      "v_pk_mul_f32 v[162:163], v[162:163], v[248:249]\n"
      "v_pk_mul_f32 v[164:165], v[164:165], v[246:247]\n"
      "v_pk_mul_f32 v[166:167], v[166:167], v[248:249]\n"
      "v_pk_mul_f32 v[168:169], v[168:169], v[246:247]\n"
      "v_pk_mul_f32 v[170:171], v[170:171], v[248:249]\n"
      "v_pk_mul_f32 v[172:173], v[172:173], v[246:247]\n"
      "v_pk_mul_f32 v[174:175], v[174:175], v[248:249]\n"
      "v_cvt_pk_bf16_f32 v184, v160, v164\n"
      "s_waitcnt lgkmcnt(7)\n"
      "ds_read_b64_tr_b16 v[192:193], v234 offset:0\n"
      "ds_read_b64_tr_b16 v[194:195], v235 offset:0\n"
      "v_cvt_pk_bf16_f32 v185, v168, v172\n"
      "buffer_store_dwordx4 v[208:211], v250, s[72:75], s53 offen offset:256\n"
      "v_cvt_pk_bf16_f32 v186, v161, v165\n"
      "v_cvt_pk_bf16_f32 v187, v169, v173\n"
      "v_cvt_pk_bf16_f32 v188, v162, v166\n"
      "buffer_store_dwordx4 v[212:215], v250, s[72:75], s53 offen offset:3328\n"
      "v_cvt_pk_bf16_f32 v189, v170, v174\n"
      "v_cvt_pk_bf16_f32 v190, v163, v167\n"
      "v_cvt_pk_bf16_f32 v191, v171, v175\n"
      "ds_read_b64_tr_b16 v[196:197], v234 offset:1024\n"
      "ds_read_b64_tr_b16 v[198:199], v235 offset:1024\n"
      "s_waitcnt lgkmcnt(11)\n"
      "v_min_f32 v59, v54, v56\n"
      "v_sub_f32 v52, v59, v54\n"
      "v_sub_f32 v53, v59, v56\n"
      "v_exp_f32 v52, v52\n"
      "v_exp_f32 v53, v53\n"
      "v_mul_f32 v55, v55, v52\n"
      "v_fma_f32 v55, v57, v53, v55\n"
      "v_rcp_f32 v55, v55\n"
      "s_nop 0\n"
      "v_mul_f32 v52, v52, v55\n"
      "v_mul_f32 v53, v53, v55\n"
      "v_mfma_f32_32x32x16_bf16 a[112:127], v[28:31], v[28:31], 0\n"
      "v_accvgpr_read_b32 v160, a128\n"
      "v_accvgpr_read_b32 v161, a129\n"
      "v_accvgpr_read_b32 v162, a130\n"
      "v_accvgpr_read_b32 v163, a131\n"
      "v_accvgpr_read_b32 v164, a132\n"
      "ds_write_b32 v236, v52 offset:192\n"
      "ds_write_b32 v236, v53 offset:256\n"
      "v_accvgpr_read_b32 v165, a133\n"
      "v_accvgpr_read_b32 v166, a134\n"
      "v_accvgpr_read_b32 v167, a135\n"
      "v_accvgpr_read_b32 v168, a136\n"
      "v_accvgpr_read_b32 v169, a137\n"
      "v_accvgpr_read_b32 v170, a138\n"
      "ds_read_b128 v[56:59], v237 offset:192\n"
      "ds_read_b128 v[60:63], v237 offset:256\n"
      "v_accvgpr_read_b32 v171, a139\n"
      "v_accvgpr_read_b32 v172, a140\n"
      "v_accvgpr_read_b32 v173, a141\n"
      "v_accvgpr_read_b32 v174, a142\n"
      "v_accvgpr_read_b32 v175, a143\n"
      "v_pk_mul_f32 v[160:161], v[160:161], v[246:247]\n"
      "ds_write_b64 v232, v[184:185] offset:2048\n"
      "ds_write_b64 v232, v[186:187] offset:2176\n"
      "v_pk_mul_f32 v[162:163], v[162:163], v[248:249]\n"
      "v_pk_mul_f32 v[164:165], v[164:165], v[246:247]\n"
      "v_pk_mul_f32 v[166:167], v[166:167], v[248:249]\n"
      "v_pk_mul_f32 v[168:169], v[168:169], v[246:247]\n"
      "v_pk_mul_f32 v[170:171], v[170:171], v[248:249]\n"
      "v_pk_mul_f32 v[172:173], v[172:173], v[246:247]\n"
      "ds_write_b64 v233, v[188:189] offset:2304\n"
      "ds_write_b64 v233, v[190:191] offset:2432\n"
      "v_pk_mul_f32 v[174:175], v[174:175], v[248:249]\n"
      "v_cvt_pk_bf16_f32 v176, v160, v164\n"
      "s_waitcnt lgkmcnt(8)\n"
      "v_cvt_pk_bf16_f32 v177, v168, v172\n"
      "buffer_store_dwordx4 v[192:195], v250, s[72:75], s54 offen offset:0\n"
      "v_cvt_pk_bf16_f32 v178, v161, v165\n"
      "ds_read_b64_tr_b16 v[200:201], v234 offset:2048\n"
      "ds_read_b64_tr_b16 v[202:203], v235 offset:2048\n"
      "v_cvt_pk_bf16_f32 v179, v169, v173\n"
      "v_cvt_pk_bf16_f32 v180, v162, v166\n"
      "buffer_store_dwordx4 v[196:199], v250, s[72:75], s54 offen offset:3072\n"
      "v_cvt_pk_bf16_f32 v181, v170, v174\n"
      "v_cvt_pk_bf16_f32 v182, v163, v167\n"
      "v_cvt_pk_bf16_f32 v183, v171, v175\n"
      "ds_read_b64_tr_b16 v[204:205], v234 offset:3072\n"
      "ds_read_b64_tr_b16 v[206:207], v235 offset:3072\n"
      "v_mfma_f32_32x32x16_bf16 a[128:143], v[28:31], v[28:31], 0\n"
      "v_mfma_f32_32x32x16_bf16 a[192:207], v[28:31], v[28:31], 0\n"
      "s_waitcnt lgkmcnt(8)\n"
      "v_accvgpr_read_b32 v160, a176\n"
      "v_accvgpr_read_b32 v161, a177\n"
      "v_accvgpr_read_b32 v162, a178\n"
      "v_accvgpr_read_b32 v163, a179\n"
      "v_accvgpr_read_b32 v164, a180\n"
      "v_accvgpr_read_b32 v165, a181\n"
      "v_accvgpr_read_b32 v166, a182\n"
      "v_accvgpr_read_b32 v167, a183\n"
      "ds_write_b64 v232, v[176:177] offset:4096\n"
      "ds_write_b64 v232, v[178:179] offset:4224\n"
      "v_accvgpr_read_b32 v168, a184\n"
      "v_accvgpr_read_b32 v169, a185\n"
      "v_accvgpr_read_b32 v170, a186\n"
      "v_accvgpr_read_b32 v171, a187\n"
      "v_accvgpr_read_b32 v172, a188\n"
      "v_accvgpr_read_b32 v173, a189\n"
      "v_accvgpr_read_b32 v174, a190\n"
      "v_accvgpr_read_b32 v175, a191\n"
      "v_pk_mul_f32 v[160:161], v[160:161], v[56:57]\n"
      "v_pk_mul_f32 v[162:163], v[162:163], v[58:59]\n"
      "v_pk_mul_f32 v[164:165], v[164:165], v[56:57]\n"
      "v_pk_mul_f32 v[166:167], v[166:167], v[58:59]\n"
      "ds_write_b64 v233, v[180:181] offset:4352\n"
      "ds_write_b64 v233, v[182:183] offset:4480\n"
      "v_pk_mul_f32 v[168:169], v[168:169], v[56:57]\n"
      "v_pk_mul_f32 v[170:171], v[170:171], v[58:59]\n"
      "v_pk_mul_f32 v[172:173], v[172:173], v[56:57]\n"
      "v_pk_mul_f32 v[174:175], v[174:175], v[58:59]\n"
      "v_pk_fma_f32 v[160:161], v[44:45], v[60:61], v[160:161]\n"
      "v_pk_fma_f32 v[162:163], v[46:47], v[62:63], v[162:163]\n"
      "v_pk_fma_f32 v[164:165], v[48:49], v[60:61], v[164:165]\n"
      "v_pk_fma_f32 v[166:167], v[50:51], v[62:63], v[166:167]\n"
      "v_pk_fma_f32 v[168:169], v[216:217], v[60:61], v[168:169]\n"
      "v_pk_fma_f32 v[170:171], v[218:219], v[62:63], v[170:171]\n"
      "v_pk_fma_f32 v[172:173], v[220:221], v[60:61], v[172:173]\n"
      "ds_read_b64_tr_b16 v[208:209], v234 offset:4096\n"
      "ds_read_b64_tr_b16 v[210:211], v235 offset:4096\n"
      "v_pk_fma_f32 v[174:175], v[222:223], v[62:63], v[174:175]\n"
      "v_cvt_pk_bf16_f32 v184, v160, v164\n"
      "s_waitcnt lgkmcnt(6)\n"
      "v_cvt_pk_bf16_f32 v185, v168, v172\n"
      "buffer_store_dwordx4 v[200:203], v250, s[72:75], s54 offen offset:128\n"
      "v_cvt_pk_bf16_f32 v186, v161, v165\n"
      "v_cvt_pk_bf16_f32 v187, v169, v173\n"
      "v_cvt_pk_bf16_f32 v188, v162, v166\n"
      "buffer_store_dwordx4 v[204:207], v250, s[72:75], s54 offen offset:3200\n"
      "v_cvt_pk_bf16_f32 v189, v170, v174\n"
      "v_cvt_pk_bf16_f32 v190, v163, v167\n"
      "v_cvt_pk_bf16_f32 v191, v171, v175\n"
      "ds_read_b64_tr_b16 v[212:213], v234 offset:5120\n"
      "ds_read_b64_tr_b16 v[214:215], v235 offset:5120\n"
      "v_accvgpr_read_b32 v160, a168\n"
      "v_accvgpr_read_b32 v161, a169\n"
      "v_accvgpr_read_b32 v162, a170\n"
      "v_accvgpr_read_b32 v163, a171\n"
      "v_accvgpr_read_b32 v164, a172\n"
      "v_accvgpr_read_b32 v165, a173\n"
      "v_accvgpr_read_b32 v166, a174\n"
      "ds_write_b64 v232, v[184:185] offset:0\n"
      "ds_write_b64 v232, v[186:187] offset:128\n"
      "v_accvgpr_read_b32 v167, a175\n"
      "v_and_b32 v54, 2, v0\n"
      "v_cmp_ne_u32 vcc, 0, v54\n"
      "v_mov_b32 v54, 0x7fff0000\n"
      "s_nop 0\n"
      "v_cndmask_b32 v54, v54, v250, vcc\n"
      "v_pk_mul_f32 v[160:161], v[160:161], v[56:57]\n"
      "v_pk_mul_f32 v[162:163], v[162:163], v[58:59]\n"
      "v_pk_mul_f32 v[164:165], v[164:165], v[56:57]\n"
      "ds_write_b64 v233, v[188:189] offset:256\n"
      "ds_write_b64 v233, v[190:191] offset:384\n"
      "v_pk_mul_f32 v[166:167], v[166:167], v[58:59]\n"
      "v_pk_fma_f32 v[160:161], v[36:37], v[60:61], v[160:161]\n"
      "v_pk_fma_f32 v[162:163], v[38:39], v[62:63], v[162:163]\n"
      "v_pk_fma_f32 v[164:165], v[40:41], v[60:61], v[164:165]\n"
      "v_pk_fma_f32 v[166:167], v[42:43], v[62:63], v[166:167]\n"
      "v_cvt_pk_bf16_f32 v176, v160, v164\n"
      "s_waitcnt lgkmcnt(4)\n"
      "v_cvt_pk_bf16_f32 v177, v160, v164\n"
      "ds_read_b64_tr_b16 v[192:193], v234 offset:0\n"
      "ds_read_b64_tr_b16 v[194:195], v235 offset:0\n"
      "buffer_store_dwordx4 v[208:211], v250, s[72:75], s54 offen offset:256\n"
      "v_cvt_pk_bf16_f32 v178, v161, v165\n"
      "v_cvt_pk_bf16_f32 v179, v161, v165\n"
      "v_cvt_pk_bf16_f32 v180, v162, v166\n"
      "buffer_store_dwordx4 v[212:215], v250, s[72:75], s54 offen offset:3328\n"
      "v_cvt_pk_bf16_f32 v181, v162, v166\n"
      "v_cvt_pk_bf16_f32 v182, v163, v167\n"
      "v_cvt_pk_bf16_f32 v183, v163, v167\n"
      "ds_read_b64_tr_b16 v[196:197], v234 offset:1024\n"
      "ds_read_b64_tr_b16 v[198:199], v235 offset:1024\n"
      "ds_write_b64 v232, v[176:177] offset:2048\n"
      "ds_write_b64 v232, v[178:179] offset:2176\n"
      "ds_write_b64 v233, v[180:181] offset:2304\n"
      "ds_write_b64 v233, v[182:183] offset:2432\n"
      "s_waitcnt lgkmcnt(4)\n"
      "buffer_store_dwordx4 v[192:195], v250, s[72:75], s55 offen offset:256\n"
      "buffer_store_dwordx4 v[196:199], v250, s[72:75], s55 offen offset:3328\n"
      "v_mfma_f32_32x32x16_bf16 a[144:159], v[28:31], v[28:31], 0\n"
      "v_mfma_f32_32x32x16_bf16 a[160:175], v[28:31], v[28:31], 0\n"
      "v_mfma_f32_32x32x16_bf16 a[176:191], v[28:31], v[28:31], 0\n"
      "ds_read_b64_tr_b16 v[200:201], v234 offset:2048\n"
      "ds_read_b64_tr_b16 v[202:203], v235 offset:2048\n"
      "ds_read_b64_tr_b16 v[204:205], v234 offset:3072\n"
      "ds_read_b64_tr_b16 v[206:207], v235 offset:3072\n"
      "s_waitcnt lgkmcnt(0)\n"
      "buffer_store_dwordx4 v[200:203], v54, s[72:75], s55 offen offset:128\n"
      "buffer_store_dwordx4 v[204:207], v54, s[72:75], s55 offen offset:3200\n"
      "s_mov_b32 s51, 1\n"
      "s_mov_b32 s43, s52\n"
      "s_mov_b32 s52, s66\n"
      "s_mov_b32 s66, s43\n"
      "s_mov_b32 s43, s53\n"
      "s_mov_b32 s53, s67\n"
      "s_mov_b32 s67, s43\n"
      "s_mov_b32 s43, s54\n"
      "s_mov_b32 s54, s68\n"
      "s_mov_b32 s68, s43\n"
      "s_mov_b32 s43, s55\n"
      "s_mov_b32 s55, s69\n"
      "s_mov_b32 s69, s43\n"
      "s_mov_b32 s86, -1\n"
      "s_mov_b32 s50, 7\n"
      "s_mov_b32 s35, 0\n"
      "v_mfma_f32_16x16x32_bf16 v[36:39], a[208:211], a[0:3], 0\n"
      "s_add_u32 s46, s44, s42\n"
      "ds_read_b128 a[232:235], v4 offset:6144\n"
      "v_add_u32 v232, s41, v6\n"
      "v_mfma_f32_16x16x32_bf16 v[36:39], a[212:215], a[4:7], v[36:39]\n"
      "s_add_u32 s47, s45, s41\n"
      "ds_read_b128 a[236:239], v5 offset:6144\n"
      "v_add_u32 v233, s41, v7\n"
      "v_mfma_f32_16x16x32_bf16 v[36:39], a[216:219], a[8:11], v[36:39]\n"
      "ds_read_b128 a[240:243], v4 offset:6272\n"
      "ds_read_b128 a[244:247], v5 offset:6272\n"
      "v_mfma_f32_16x16x32_bf16 v[36:39], a[220:223], a[12:15], v[36:39]\n"
      "s_mov_b32 m0, s46\n"
      "ds_read_b128 a[248:251], v4 offset:6400\n"
      "ds_read_b128 a[252:255], v5 offset:6400\n"
      "v_mfma_f32_16x16x32_bf16 v[36:39], a[224:227], a[16:19], v[36:39]\n"
      "buffer_load_dwordx4 v1, s[20:23], s36 offen offset:0 lds\n"
      "v_mfma_f32_16x16x32_bf16 v[36:39], a[228:231], a[20:23], v[36:39]\n"
      "buffer_load_dwordx4 v2, s[20:23], s36 offen offset:1024 lds\n"
      "v_mfma_f32_16x16x32_bf16 v[40:43], a[228:231], a[44:47], 0\n"
      "buffer_load_dwordx4 v3, s[20:23], s36 offen offset:2048 lds\n"
      "v_mfma_f32_16x16x32_bf16 v[40:43], a[224:227], a[40:43], v[40:43]\n"
      "s_nop 0\n"
      "v_add_u32 v234, s40, v12\n"
      "v_add_u32 v235, s40, v13\n"
      "v_mfma_f32_16x16x32_bf16 v[40:43], a[220:223], a[36:39], v[40:43]\n"
      "v_max3_f32 v52, v36, v37, v38\n"
      "v_max_f32 v52, v52, v39\n"
      "v_mfma_f32_16x16x32_bf16 v[40:43], a[216:219], a[32:35], v[40:43]\n"
      "v_mov_b32 v53, v52\n"
      "s_nop 1\n"
      "v_permlane32_swap_b32 v52, v53\n"
      "v_mfma_f32_16x16x32_bf16 v[40:43], a[212:215], a[28:31], v[40:43]\n"
      "v_max_f32 v52, v52, v53\n"
      "v_mov_b32 v53, v52\n"
      "v_mfma_f32_16x16x32_bf16 v[40:43], a[208:211], a[24:27], v[40:43]\n"
      "s_nop 0\n"
      "v_permlane16_swap_b32 v52, v53\n"
      "v_max_f32 v52, v52, v53\n"
      "v_mfma_f32_16x16x32_bf16 v[44:47], a[208:211], a[48:51], 0\n"
      "v_mul_f32 v52, s62, v52\n"
      "v_add_f32 v18, 0xc1800000, v52\n"
      "v_mfma_f32_16x16x32_bf16 v[44:47], a[212:215], a[52:55], v[44:47]\n"
      "v_fma_f32 v36, v36, s48, v18\n"
      "v_fma_f32 v37, v37, s48, v18\n"
      "v_mfma_f32_16x16x32_bf16 v[44:47], a[216:219], a[56:59], v[44:47]\n"
      "v_exp_f32 v28, v36\n"
      "v_mfma_f32_16x16x32_bf16 v[44:47], a[220:223], a[60:63], v[44:47]\n"
      "v_exp_f32 v29, v37\n"
      "v_mfma_f32_16x16x32_bf16 v[44:47], a[224:227], a[64:67], v[44:47]\n"
      "v_fma_f32 v38, v38, s48, v18\n"
      "v_fma_f32 v39, v39, s48, v18\n"
      "v_mfma_f32_16x16x32_bf16 v[44:47], a[228:231], a[68:71], v[44:47]\n"
      "s_waitcnt lgkmcnt(0)\n"
      "v_exp_f32 v30, v38\n"
      "v_mfma_f32_16x16x32_bf16 v[216:219], a[232:235], a[0:3], 0\n"
      "ds_read_b128 a[208:211], v4 offset:12288\n"
      "v_cvt_pk_bf16_f32 v160, v28, v29\n"
      "v_mfma_f32_16x16x32_bf16 v[216:219], a[236:239], a[4:7], v[216:219]\n"
      "ds_read_b128 a[212:215], v5 offset:12288\n"
      "v_max3_f32 v54, v40, v41, v42\n"
      "v_mfma_f32_16x16x32_bf16 v[216:219], a[240:243], a[8:11], v[216:219]\n"
      "ds_read_b128 a[216:219], v4 offset:12416\n"
      "ds_read_b128 a[220:223], v5 offset:12416\n"
      "v_mfma_f32_16x16x32_bf16 v[216:219], a[244:247], a[12:15], v[216:219]\n"
      "s_add_u32 m0, s46, 3072\n"
      "ds_read_b128 a[224:227], v4 offset:12544\n"
      "ds_read_b128 a[228:231], v5 offset:12544\n"
      "v_mfma_f32_16x16x32_bf16 v[216:219], a[248:251], a[16:19], v[216:219]\n"
      "buffer_load_dwordx4 v1, s[20:23], s37 offen offset:0 lds\n"
      "v_mfma_f32_16x16x32_bf16 v[216:219], a[252:255], a[20:23], v[216:219]\n"
      "buffer_load_dwordx4 v2, s[20:23], s37 offen offset:1024 lds\n"
      "v_mfma_f32_16x16x32_bf16 v[220:223], a[252:255], a[44:47], 0\n"
      "buffer_load_dwordx4 v3, s[20:23], s37 offen offset:2048 lds\n"
      "v_mfma_f32_16x16x32_bf16 v[220:223], a[248:251], a[40:43], v[220:223]\n"
      "v_exp_f32 v31, v39\n"
      "v_mfma_f32_16x16x32_bf16 v[220:223], a[244:247], a[36:39], v[220:223]\n"
      "v_cvt_pk_bf16_f32 v161, v30, v31\n"
      "v_max_f32 v54, v54, v43\n"
      "v_mfma_f32_16x16x32_bf16 v[220:223], a[240:243], a[32:35], v[220:223]\n"
      "v_mov_b32 v55, v54\n"
      "s_nop 1\n"
      "v_permlane32_swap_b32 v54, v55\n"
      "v_mfma_f32_16x16x32_bf16 v[220:223], a[236:239], a[28:31], v[220:223]\n"
      "v_max_f32 v54, v54, v55\n"
      "v_mov_b32 v55, v54\n"
      "v_mfma_f32_16x16x32_bf16 v[220:223], a[232:235], a[24:27], v[220:223]\n"
      "s_nop 0\n"
      "v_permlane16_swap_b32 v54, v55\n"
      "v_max_f32 v54, v54, v55\n"
      "v_mfma_f32_16x16x32_bf16 v[224:227], a[232:235], a[48:51], 0\n"
      "v_mul_f32 v54, s62, v54\n"
      "v_add_f32 v19, 0xc1800000, v54\n"
      "v_mfma_f32_16x16x32_bf16 v[224:227], a[236:239], a[52:55], v[224:227]\n"
      "v_fma_f32 v40, v40, s48, v19\n"
      "v_fma_f32 v41, v41, s48, v19\n"
      "v_mfma_f32_16x16x32_bf16 v[224:227], a[240:243], a[56:59], v[224:227]\n"
      "v_fma_f32 v42, v42, s48, v19\n"
      "v_fma_f32 v43, v43, s48, v19\n"
      "v_exp_f32 v32, v40\n"
      "v_exp_f32 v33, v41\n"
      "v_exp_f32 v34, v42\n"
      "v_exp_f32 v35, v43\n"
      "v_cvt_pk_bf16_f32 v168, v32, v33\n"
      "v_or3_b32 v22, v160, v161, v168\n"
      "v_cvt_pk_bf16_f32 v169, v34, v35\n"
      "v_max3_f32 v56, v44, v45, v46\n"
      "v_max_f32 v56, v56, v47\n"
      "v_mov_b32 v57, v56\n"
      "s_nop 1\n"
      "v_permlane32_swap_b32 v56, v57\n"
      "v_max_f32 v56, v56, v57\n"
      "v_mov_b32 v57, v56\n"
      "s_nop 1\n"
      "v_permlane16_swap_b32 v56, v57\n"
      "v_max_f32 v56, v56, v57\n"
      "v_mul_f32 v56, s62, v56\n"
      "v_add_f32 v20, 0xc1800000, v56\n"
      "v_fma_f32 v44, v44, s48, v20\n"
      "v_fma_f32 v45, v45, s48, v20\n"
      "v_fma_f32 v46, v46, s48, v20\n"
      "v_fma_f32 v47, v47, s48, v20\n"
      "v_exp_f32 v28, v44\n"
      "v_exp_f32 v29, v45\n"
      "v_exp_f32 v30, v46\n"
      "v_exp_f32 v31, v47\n"
      "v_cvt_pk_bf16_f32 v176, v28, v29\n"
      "v_or3_b32 v22, v22, v169, v176\n"
      "v_cvt_pk_bf16_f32 v177, v30, v31\n"
      "v_bitop3_b32 v22, v22, v177, s49 bitop3:0xa8\n"
      "v_cmp_ne_u32 vcc, 0, v22\n"
      "v_mfma_f32_16x16x32_bf16 v[224:227], a[244:247], a[60:63], v[224:227]\n"
      "v_add_u32 v236, s40, v14\n"
      "v_add_u32 v237, s40, v15\n"
      "v_mfma_f32_16x16x32_bf16 v[224:227], a[248:251], a[64:67], v[224:227]\n"
      "s_mov_b32 s43, s40\n"
      "v_mfma_f32_16x16x32_bf16 v[224:227], a[252:255], a[68:71], v[224:227]\n"
      "s_waitcnt lgkmcnt(0)\n"
      "s_cbranch_vccnz L_rare_Oa_0\n"
      "L_rare_Oa_0_ret:\n"
      "v_fma_f32 v216, v216, s48, v18\n"
      "v_mfma_f32_16x16x32_bf16 v[36:39], a[208:211], a[0:3], 0\n"
      "s_mov_b32 s40, s41\n"
      "ds_read_b128 a[232:235], v4 offset:18432\n"
      "v_fma_f32 v217, v217, s48, v18\n"
      "v_mfma_f32_16x16x32_bf16 v[36:39], a[212:215], a[4:7], v[36:39]\n"
      "s_mov_b32 s41, s42\n"
      "ds_read_b128 a[236:239], v5 offset:18432\n"
      "v_fma_f32 v218, v218, s48, v18\n"
      "v_mfma_f32_16x16x32_bf16 v[36:39], a[216:219], a[8:11], v[36:39]\n"
      "s_mov_b32 s42, s43\n"
      "ds_read_b128 a[240:243], v4 offset:18560\n"
      "ds_read_b128 a[244:247], v5 offset:18560\n"
      "v_mfma_f32_16x16x32_bf16 v[36:39], a[220:223], a[12:15], v[36:39]\n"
      "s_mov_b32 m0, s47\n"
      "ds_read_b128 a[248:251], v4 offset:18688\n"
      "ds_read_b128 a[252:255], v5 offset:18688\n"
      "v_mfma_f32_16x16x32_bf16 v[36:39], a[224:227], a[16:19], v[36:39]\n"
      "buffer_load_dwordx4 v1, s[24:27], s38 offen offset:0 lds\n"
      "v_mfma_f32_16x16x32_bf16 v[36:39], a[228:231], a[20:23], v[36:39]\n"
      "buffer_load_dwordx4 v2, s[24:27], s38 offen offset:1024 lds\n"
      "v_mfma_f32_16x16x32_bf16 v[40:43], a[228:231], a[44:47], 0\n"
      "buffer_load_dwordx4 v3, s[24:27], s38 offen offset:2048 lds\n"
      "v_mfma_f32_16x16x32_bf16 v[40:43], a[224:227], a[40:43], v[40:43]\n"
      "v_exp_f32 v32, v216\n"
      "v_mfma_f32_16x16x32_bf16 v[40:43], a[220:223], a[36:39], v[40:43]\n"
      "v_exp_f32 v33, v217\n"
      "v_mfma_f32_16x16x32_bf16 v[40:43], a[216:219], a[32:35], v[40:43]\n"
      "v_exp_f32 v34, v218\n"
      "v_mfma_f32_16x16x32_bf16 v[40:43], a[212:215], a[28:31], v[40:43]\n"
      "v_fma_f32 v219, v219, s48, v18\n"
      "v_cvt_pk_bf16_f32 v162, v32, v33\n"
      "v_mfma_f32_16x16x32_bf16 v[40:43], a[208:211], a[24:27], v[40:43]\n"
      "v_exp_f32 v35, v219\n"
      "v_mfma_f32_16x16x32_bf16 v[44:47], a[208:211], a[48:51], 0\n"
      "v_cvt_pk_bf16_f32 v163, v34, v35\n"
      "v_fma_f32 v220, v220, s48, v19\n"
      "v_mfma_f32_16x16x32_bf16 v[44:47], a[212:215], a[52:55], v[44:47]\n"
      "v_exp_f32 v28, v220\n"
      "v_mfma_f32_16x16x32_bf16 v[44:47], a[216:219], a[56:59], v[44:47]\n"
      "v_fma_f32 v221, v221, s48, v19\n"
      "v_fma_f32 v222, v222, s48, v19\n"
      "v_mfma_f32_16x16x32_bf16 v[44:47], a[220:223], a[60:63], v[44:47]\n"
      "v_exp_f32 v29, v221\n"
      "v_mfma_f32_16x16x32_bf16 v[44:47], a[224:227], a[64:67], v[44:47]\n"
      "v_exp_f32 v30, v222\n"
      "v_mfma_f32_16x16x32_bf16 v[44:47], a[228:231], a[68:71], v[44:47]\n"
      "v_fma_f32 v223, v223, s48, v19\n"
      "v_cvt_pk_bf16_f32 v170, v28, v29\n"
      "v_mfma_f32_16x16x32_bf16 v[48:51], a[228:231], a[92:95], 0\n"
      "v_exp_f32 v31, v223\n"
      "v_mfma_f32_16x16x32_bf16 v[48:51], a[224:227], a[88:91], v[48:51]\n"
      "v_or3_b32 v22, v162, v163, v170\n"
      "v_cvt_pk_bf16_f32 v171, v30, v31\n"
      "v_mfma_f32_16x16x32_bf16 v[48:51], a[220:223], a[84:87], v[48:51]\n"
      "v_fma_f32 v224, v224, s48, v20\n"
      "v_fma_f32 v225, v225, s48, v20\n"
      "v_fma_f32 v226, v226, s48, v20\n"
      "v_fma_f32 v227, v227, s48, v20\n"
      "v_exp_f32 v32, v224\n"
      "v_exp_f32 v33, v225\n"
      "v_exp_f32 v34, v226\n"
      "v_exp_f32 v35, v227\n"
      "v_cvt_pk_bf16_f32 v178, v32, v33\n"
      "v_or3_b32 v22, v22, v171, v178\n"
      "v_cvt_pk_bf16_f32 v179, v34, v35\n"
      "v_bitop3_b32 v22, v22, v179, s49 bitop3:0xa8\n"
      "v_cmp_ne_u32 vcc, 0, v22\n"
      "v_mfma_f32_16x16x32_bf16 v[48:51], a[216:219], a[80:83], v[48:51]\n"
      "v_mfma_f32_16x16x32_bf16 v[48:51], a[212:215], a[76:79], v[48:51]\n"
      "v_mfma_f32_16x16x32_bf16 v[48:51], a[208:211], a[72:75], v[48:51]\n"
      "s_waitcnt lgkmcnt(0)\n"
      "s_cbranch_vccnz L_rare_Oa_1\n"
      "L_rare_Oa_1_ret:\n"
      "v_fma_f32 v36, v36, s48, v18\n"
      "v_mfma_f32_16x16x32_bf16 v[216:219], a[232:235], a[0:3], 0\n"
      "v_exp_f32 v28, v36\n"
      "v_mfma_f32_16x16x32_bf16 v[216:219], a[236:239], a[4:7], v[216:219]\n"
      "v_fma_f32 v37, v37, s48, v18\n"
      "v_fma_f32 v38, v38, s48, v18\n"
      "v_mfma_f32_16x16x32_bf16 v[216:219], a[240:243], a[8:11], v[216:219]\n"
      "v_exp_f32 v29, v37\n"
      "v_mfma_f32_16x16x32_bf16 v[216:219], a[244:247], a[12:15], v[216:219]\n"
      "s_add_u32 m0, s47, 3072\n"
      "v_exp_f32 v30, v38\n"
      "v_mfma_f32_16x16x32_bf16 v[216:219], a[248:251], a[16:19], v[216:219]\n"
      "buffer_load_dwordx4 v1, s[24:27], s39 offen offset:0 lds\n"
      "v_mfma_f32_16x16x32_bf16 v[216:219], a[252:255], a[20:23], v[216:219]\n"
      "buffer_load_dwordx4 v2, s[24:27], s39 offen offset:1024 lds\n"
      "v_mfma_f32_16x16x32_bf16 v[220:223], a[252:255], a[44:47], 0\n"
      "buffer_load_dwordx4 v3, s[24:27], s39 offen offset:2048 lds\n"
      "v_mfma_f32_16x16x32_bf16 v[220:223], a[248:251], a[40:43], v[220:223]\n"
      "s_mov_b32 s38, s36\n"
      "v_fma_f32 v39, v39, s48, v18\n"
      "v_cvt_pk_bf16_f32 v164, v28, v29\n"
      "v_mfma_f32_16x16x32_bf16 v[220:223], a[244:247], a[36:39], v[220:223]\n"
      "s_mov_b32 s39, s37\n"
      "v_exp_f32 v31, v39\n"
      "v_mfma_f32_16x16x32_bf16 v[220:223], a[240:243], a[32:35], v[220:223]\n"
      "s_add_u32 s12, s12, 1\n"
      "v_cvt_pk_bf16_f32 v165, v30, v31\n"
      "v_fma_f32 v40, v40, s48, v19\n"
      "v_mfma_f32_16x16x32_bf16 v[220:223], a[236:239], a[28:31], v[220:223]\n"
      "s_waitcnt vmcnt(34)\n"
      "v_exp_f32 v32, v40\n"
      "v_mfma_f32_16x16x32_bf16 v[220:223], a[232:235], a[24:27], v[220:223]\n"
      "s_barrier\n"
      "ds_read_b128 a[208:211], v232 offset:0\n"
      "v_fma_f32 v41, v41, s48, v19\n"
      "v_mfma_f32_16x16x32_bf16 v[224:227], a[232:235], a[48:51], 0\n"
      "s_add_u32 s15, s13, s12\n"
      "ds_read_b128 a[212:215], v233 offset:0\n"
      "v_fma_f32 v42, v42, s48, v19\n"
      "v_mfma_f32_16x16x32_bf16 v[224:227], a[236:239], a[52:55], v[224:227]\n"
      "s_and_b32 s15, s15, 15\n"
      "ds_read_b128 a[216:219], v232 offset:128\n"
      "ds_read_b128 a[220:223], v233 offset:128\n"
      "v_mfma_f32_16x16x32_bf16 v[224:227], a[240:243], a[56:59], v[224:227]\n"
      "s_mul_i32 s15, s15, 24576\n"
      "ds_read_b128 a[224:227], v232 offset:256\n"
      "ds_read_b128 a[228:231], v233 offset:256\n"
      "v_mfma_f32_16x16x32_bf16 v[224:227], a[244:247], a[60:63], v[224:227]\n"
      "s_lshr_b32 s16, s12, 4\n"
      "v_exp_f32 v33, v41\n"
      "v_mfma_f32_16x16x32_bf16 v[224:227], a[248:251], a[64:67], v[224:227]\n"
      "s_mul_i32 s16, s16, 393216\n"
      "s_add_u32 s15, s15, s16\n"
      "s_add_u32 s36, s15, s14\n"
      "s_add_u32 s37, s36, 3072\n"
      "v_mfma_f32_16x16x32_bf16 v[224:227], a[252:255], a[68:71], v[224:227]\n"
      "v_exp_f32 v34, v42\n"
      "v_mfma_f32_16x16x32_bf16 v[228:231], a[252:255], a[92:95], 0\n"
      "v_fma_f32 v43, v43, s48, v19\n"
      "v_cvt_pk_bf16_f32 v172, v32, v33\n"
      "v_mfma_f32_16x16x32_bf16 v[228:231], a[248:251], a[88:91], v[228:231]\n"
      "v_or3_b32 v22, v164, v165, v172\n"
      "v_fma_f32 v44, v44, s48, v20\n"
      "v_mfma_f32_16x16x32_bf16 v[228:231], a[244:247], a[84:87], v[228:231]\n"
      "v_exp_f32 v35, v43\n"
      "s_nop 0\n"
      "v_cvt_pk_bf16_f32 v173, v34, v35\n"
      "v_fma_f32 v45, v45, s48, v20\n"
      "v_fma_f32 v46, v46, s48, v20\n"
      "v_fma_f32 v47, v47, s48, v20\n"
      "v_exp_f32 v28, v44\n"
      "v_exp_f32 v29, v45\n"
      "v_exp_f32 v30, v46\n"
      "v_exp_f32 v31, v47\n"
      "v_cvt_pk_bf16_f32 v180, v28, v29\n"
      "v_or3_b32 v22, v22, v173, v180\n"
      "v_cvt_pk_bf16_f32 v181, v30, v31\n"
      "v_max3_f32 v58, v48, v49, v50\n"
      "v_max_f32 v58, v58, v51\n"
      "v_mov_b32 v59, v58\n"
      "s_nop 1\n"
      "v_permlane32_swap_b32 v58, v59\n"
      "v_max_f32 v58, v58, v59\n"
      "v_mov_b32 v59, v58\n"
      "s_nop 1\n"
      "v_permlane16_swap_b32 v58, v59\n"
      "v_max_f32 v58, v58, v59\n"
      "v_mul_f32 v58, s62, v58\n"
      "v_add_f32 v21, 0xc1800000, v58\n"
      "v_fma_f32 v48, v48, s48, v21\n"
      "v_fma_f32 v49, v49, s48, v21\n"
      "v_fma_f32 v50, v50, s48, v21\n"
      "v_fma_f32 v51, v51, s48, v21\n"
      "v_exp_f32 v32, v48\n"
      "v_exp_f32 v33, v49\n"
      "v_exp_f32 v34, v50\n"
      "v_exp_f32 v35, v51\n"
      "v_cvt_pk_bf16_f32 v184, v32, v33\n"
      "v_or3_b32 v22, v22, v181, v184\n"
      "v_cvt_pk_bf16_f32 v185, v34, v35\n"
      "v_bitop3_b32 v22, v22, v185, s49 bitop3:0xa8\n"
      "v_cmp_ne_u32 vcc, 0, v22\n"
      "v_mfma_f32_16x16x32_bf16 v[228:231], a[240:243], a[80:83], v[228:231]\n"
      "v_mfma_f32_16x16x32_bf16 v[228:231], a[236:239], a[76:79], v[228:231]\n"
      "v_mfma_f32_16x16x32_bf16 v[228:231], a[232:235], a[72:75], v[228:231]\n"
      "s_waitcnt lgkmcnt(0)\n"
      "s_cbranch_vccnz L_rare_Oa_2\n"
      "L_rare_Oa_2_ret:\n"
      "v_fma_f32 v216, v216, s48, v18\n"
      "v_fma_f32 v217, v217, s48, v18\n"
      "v_fma_f32 v218, v218, s48, v18\n"
      "v_fma_f32 v219, v219, s48, v18\n"
      "v_exp_f32 v28, v216\n"
      "v_exp_f32 v29, v217\n"
      "v_exp_f32 v30, v218\n"
      "v_exp_f32 v31, v219\n"
      "v_cvt_pk_bf16_f32 v166, v28, v29\n"
      "v_cvt_pk_bf16_f32 v167, v30, v31\n"
      "v_fma_f32 v220, v220, s48, v19\n"
      "v_fma_f32 v221, v221, s48, v19\n"
      "v_fma_f32 v222, v222, s48, v19\n"
      "v_fma_f32 v223, v223, s48, v19\n"
      "v_exp_f32 v32, v220\n"
      "v_exp_f32 v33, v221\n"
      "v_exp_f32 v34, v222\n"
      "v_exp_f32 v35, v223\n"
      "v_cvt_pk_bf16_f32 v174, v32, v33\n"
      "v_or3_b32 v22, v166, v167, v174\n"
      "v_cvt_pk_bf16_f32 v175, v34, v35\n"
      "v_fma_f32 v224, v224, s48, v20\n"
      "v_fma_f32 v225, v225, s48, v20\n"
      "v_fma_f32 v226, v226, s48, v20\n"
      "v_fma_f32 v227, v227, s48, v20\n"
      "v_exp_f32 v28, v224\n"
      "v_exp_f32 v29, v225\n"
      "v_exp_f32 v30, v226\n"
      "v_exp_f32 v31, v227\n"
      "v_cvt_pk_bf16_f32 v182, v28, v29\n"
      "v_or3_b32 v22, v22, v175, v182\n"
      "v_cvt_pk_bf16_f32 v183, v30, v31\n"
      "v_fma_f32 v228, v228, s48, v21\n"
      "v_fma_f32 v229, v229, s48, v21\n"
      "v_fma_f32 v230, v230, s48, v21\n"
      "v_fma_f32 v231, v231, s48, v21\n"
      "v_exp_f32 v32, v228\n"
      "v_exp_f32 v33, v229\n"
      "v_exp_f32 v34, v230\n"
      "v_exp_f32 v35, v231\n"
      "v_cvt_pk_bf16_f32 v186, v32, v33\n"
      "v_or3_b32 v22, v22, v183, v186\n"
      "v_cvt_pk_bf16_f32 v187, v34, v35\n"
      "v_bitop3_b32 v22, v22, v187, s49 bitop3:0xa8\n"
      "v_cmp_ne_u32 vcc, 0, v22\n"
      "s_nop 1\n"
      "s_cbranch_vccnz L_rare_Oa_3\n"
      "L_rare_Oa_3_ret:\n"
      "s_branch L_loop_O\n"
      "L_fin_O:\n"
      "ds_read_b64_tr_b16 a[232:233], v8 offset:0\n"
      "ds_read_b64_tr_b16 a[234:235], v8 offset:6144\n"
      "ds_read_b64_tr_b16 a[236:237], v8 offset:12288\n"
      "ds_read_b64_tr_b16 a[238:239], v8 offset:18432\n"
      "ds_read_b64_tr_b16 a[240:241], v9 offset:0\n"
      "ds_read_b64_tr_b16 a[242:243], v9 offset:6144\n"
      "ds_read_b64_tr_b16 a[244:245], v9 offset:12288\n"
      "ds_read_b64_tr_b16 a[246:247], v9 offset:18432\n"
      "ds_read_b64_tr_b16 a[248:249], v10 offset:0\n"
      "ds_read_b64_tr_b16 a[250:251], v10 offset:6144\n"
      "ds_read_b64_tr_b16 a[252:253], v10 offset:12288\n"
      "ds_read_b64_tr_b16 a[254:255], v10 offset:18432\n"
      "v_smfmac_f32_16x16x64_bf16 a[192:195], v[24:27], v[188:195], v16\n"
      "v_smfmac_f32_16x16x64_bf16 a[196:199], v[24:27], v[196:203], v16\n"
      "v_smfmac_f32_16x16x64_bf16 a[200:203], v[24:27], v[204:211], v16\n"
      "v_mfma_f32_16x16x32_bf16 a[204:207], v[24:27], v[212:215], a[204:207]\n"
      "s_waitcnt lgkmcnt(8)\n"
      "v_mfma_f32_16x16x32_bf16 v[64:67], v[188:191], a[232:235], v[64:67]\n"
      "v_mfma_f32_16x16x32_bf16 v[64:67], v[192:195], a[236:239], v[64:67]\n"
      "v_mfma_f32_16x16x32_bf16 v[112:115], v[200:203], a[236:239], v[112:115]\n"
      "v_mfma_f32_16x16x32_bf16 v[112:115], v[196:199], a[232:235], v[112:115]\n"
      "v_accvgpr_read_b32 v238, a192\n"
      "v_accvgpr_read_b32 v242, a196\n"
      "v_mfma_f32_16x16x32_bf16 a[96:99], v[204:207], a[232:235], a[96:99]\n"
      "v_accvgpr_read_b32 v246, a200\n"
      "v_accvgpr_read_b32 v239, a193\n"
      "v_mfma_f32_16x16x32_bf16 a[96:99], v[208:211], a[236:239], a[96:99]\n"
      "v_accvgpr_read_b32 v243, a197\n"
      "v_accvgpr_read_b32 v247, a201\n"
      "v_mfma_f32_16x16x32_bf16 a[144:147], v[212:215], a[236:239], a[144:147]\n"
      "s_waitcnt lgkmcnt(4)\n"
      "ds_read_b64_tr_b16 a[232:233], v11 offset:0\n"
      "ds_read_b64_tr_b16 a[234:235], v11 offset:6144\n"
      "v_mfma_f32_16x16x32_bf16 v[68:71], v[188:191], a[240:243], v[68:71]\n"
      "s_lshl_b32 s63, s61, 9\n"
      "ds_read_b64_tr_b16 a[236:237], v11 offset:12288\n"
      "ds_read_b64_tr_b16 a[238:239], v11 offset:18432\n"
      "v_mfma_f32_16x16x32_bf16 v[68:71], v[192:195], a[244:247], v[68:71]\n"
      "s_add_u32 s63, s63, s65\n"
      "v_mfma_f32_16x16x32_bf16 v[116:119], v[200:203], a[244:247], v[116:119]\n"
      "s_add_u32 s63, s63, 161792\n"
      "v_and_b32 v236, 15, v0\n"
      "v_lshl_add_u32 v236, v236, 2, s63\n"
      "v_mfma_f32_16x16x32_bf16 v[116:119], v[196:199], a[240:243], v[116:119]\n"
      "v_and_b32 v237, 48, v0\n"
      "v_add_u32 v237, s63, v237\n"
      "v_mfma_f32_16x16x32_bf16 a[100:103], v[204:207], a[240:243], a[100:103]\n"
      "v_add_f32 v238, v238, v239\n"
      "v_add_f32 v242, v242, v243\n"
      "v_mfma_f32_16x16x32_bf16 a[100:103], v[208:211], a[244:247], a[100:103]\n"
      "v_add_f32 v246, v246, v247\n"
      "v_mfma_f32_16x16x32_bf16 a[148:151], v[212:215], a[244:247], a[148:151]\n"
      "s_waitcnt lgkmcnt(4)\n"
      "ds_read_b64_tr_b16 a[240:241], v8 offset:128\n"
      "ds_read_b64_tr_b16 a[242:243], v8 offset:6272\n"
      "v_mfma_f32_16x16x32_bf16 v[72:75], v[188:191], a[248:251], v[72:75]\n"
      "s_add_u32 s82, s45, s41\n"
      "ds_read_b64_tr_b16 a[244:245], v8 offset:12416\n"
      "ds_read_b64_tr_b16 a[246:247], v8 offset:18560\n"
      "v_mfma_f32_16x16x32_bf16 v[72:75], v[192:195], a[252:255], v[72:75]\n"
      "v_rcp_f32 v238, v238\n"
      "v_mfma_f32_16x16x32_bf16 v[120:123], v[200:203], a[252:255], v[120:123]\n"
      "v_rcp_f32 v242, v242\n"
      "v_mfma_f32_16x16x32_bf16 v[120:123], v[196:199], a[248:251], v[120:123]\n"
      "v_rcp_f32 v246, v246\n"
      "v_mfma_f32_16x16x32_bf16 a[104:107], v[204:207], a[248:251], a[104:107]\n"
      "v_add_u32 v232, s82, v251\n"
      "v_mfma_f32_16x16x32_bf16 a[104:107], v[208:211], a[252:255], a[104:107]\n"
      "ds_write_b32 v236, v238 offset:0\n"
      "ds_write_b32 v236, v242 offset:64\n"
      "v_mfma_f32_16x16x32_bf16 a[152:155], v[212:215], a[252:255], a[152:155]\n"
      "s_waitcnt lgkmcnt(6)\n"
      "ds_read_b64_tr_b16 a[248:249], v9 offset:128\n"
      "ds_read_b64_tr_b16 a[250:251], v9 offset:6272\n"
      "v_mfma_f32_16x16x32_bf16 v[76:79], v[188:191], a[232:235], v[76:79]\n"
      "ds_read_b64_tr_b16 a[252:253], v9 offset:12416\n"
      "ds_read_b64_tr_b16 a[254:255], v9 offset:18560\n"
      "v_mfma_f32_16x16x32_bf16 v[76:79], v[192:195], a[236:239], v[76:79]\n"
      "ds_write_b32 v236, v246 offset:128\n"
      "ds_read_b128 v[238:241], v237 offset:0\n"
      "v_mfma_f32_16x16x32_bf16 v[124:127], v[200:203], a[236:239], v[124:127]\n"
      "ds_read_b128 v[242:245], v237 offset:64\n"
      "ds_read_b128 v[246:249], v237 offset:128\n"
      "v_mfma_f32_16x16x32_bf16 v[124:127], v[196:199], a[232:235], v[124:127]\n"
      "v_add_u32 v234, s82, v17\n"
      "v_xor_b32 v233, 32, v232\n"
      "v_mfma_f32_16x16x32_bf16 a[108:111], v[204:207], a[232:235], a[108:111]\n"
      "v_xor_b32 v235, 32, v234\n"
      "v_lshlrev_b32 v23, 4, v0\n"
      "v_mfma_f32_16x16x32_bf16 a[108:111], v[208:211], a[236:239], a[108:111]\n"
      "s_waitcnt lgkmcnt(0)\n"
      "v_mul_f32 v64, v64, v238\n"
      "v_mul_f32 v65, v65, v239\n"
      "v_mfma_f32_16x16x32_bf16 a[156:159], v[212:215], a[236:239], a[156:159]\n"
      "s_waitcnt lgkmcnt(0)\n"
      "ds_read_b64_tr_b16 a[232:233], v10 offset:128\n"
      "v_mul_f32 v66, v66, v240\n"
      "v_mfma_f32_16x16x32_bf16 v[80:83], v[188:191], a[240:243], v[80:83]\n"
      "ds_read_b64_tr_b16 a[234:235], v10 offset:6272\n"
      "ds_read_b64_tr_b16 a[236:237], v10 offset:12416\n"
      "v_mfma_f32_16x16x32_bf16 v[80:83], v[192:195], a[244:247], v[80:83]\n"
      "ds_read_b64_tr_b16 a[238:239], v10 offset:18560\n"
      "v_mul_f32 v67, v67, v241\n"
      "v_mfma_f32_16x16x32_bf16 v[128:131], v[200:203], a[244:247], v[128:131]\n"
      "v_mul_f32 v68, v68, v238\n"
      "v_mul_f32 v69, v69, v239\n"
      "v_mfma_f32_16x16x32_bf16 v[128:131], v[196:199], a[240:243], v[128:131]\n"
      "v_mul_f32 v70, v70, v240\n"
      "v_mul_f32 v71, v71, v241\n"
      "v_mfma_f32_16x16x32_bf16 a[112:115], v[204:207], a[240:243], a[112:115]\n"
      "v_mul_f32 v72, v72, v238\n"
      "v_mul_f32 v73, v73, v239\n"
      "v_mfma_f32_16x16x32_bf16 a[112:115], v[208:211], a[244:247], a[112:115]\n"
      "v_mul_f32 v74, v74, v240\n"
      "v_mul_f32 v75, v75, v241\n"
      "v_mfma_f32_16x16x32_bf16 a[160:163], v[212:215], a[244:247], a[160:163]\n"
      "s_waitcnt lgkmcnt(4)\n"
      "ds_read_b64_tr_b16 a[240:241], v11 offset:128\n"
      "v_mul_f32 v76, v76, v238\n"
      "v_mfma_f32_16x16x32_bf16 v[84:87], v[188:191], a[248:251], v[84:87]\n"
      "ds_read_b64_tr_b16 a[242:243], v11 offset:6272\n"
      "ds_read_b64_tr_b16 a[244:245], v11 offset:12416\n"
      "v_mfma_f32_16x16x32_bf16 v[84:87], v[192:195], a[252:255], v[84:87]\n"
      "ds_read_b64_tr_b16 a[246:247], v11 offset:18560\n"
      "v_mul_f32 v77, v77, v239\n"
      "v_mfma_f32_16x16x32_bf16 v[132:135], v[200:203], a[252:255], v[132:135]\n"
      "v_mul_f32 v78, v78, v240\n"
      "v_mul_f32 v79, v79, v241\n"
      "v_mfma_f32_16x16x32_bf16 v[132:135], v[196:199], a[248:251], v[132:135]\n"
      "v_cvt_pk_bf16_f32 v176, v64, v68\n"
      "v_cvt_pk_bf16_f32 v177, v72, v76\n"
      "v_mfma_f32_16x16x32_bf16 a[116:119], v[204:207], a[248:251], a[116:119]\n"
      "v_cvt_pk_bf16_f32 v178, v65, v69\n"
      "v_cvt_pk_bf16_f32 v179, v73, v77\n"
      "v_mfma_f32_16x16x32_bf16 a[116:119], v[208:211], a[252:255], a[116:119]\n"
      "v_cvt_pk_bf16_f32 v180, v66, v70\n"
      "v_cvt_pk_bf16_f32 v181, v74, v78\n"
      "v_mfma_f32_16x16x32_bf16 a[164:167], v[212:215], a[252:255], a[164:167]\n"
      "s_waitcnt lgkmcnt(4)\n"
      "ds_read_b64_tr_b16 a[248:249], v8 offset:256\n"
      "v_cvt_pk_bf16_f32 v182, v67, v71\n"
      "v_mfma_f32_16x16x32_bf16 v[88:91], v[188:191], a[232:235], v[88:91]\n"
      "ds_read_b64_tr_b16 a[250:251], v8 offset:6400\n"
      "ds_read_b64_tr_b16 a[252:253], v8 offset:12544\n"
      "v_mfma_f32_16x16x32_bf16 v[88:91], v[192:195], a[236:239], v[88:91]\n"
      "ds_read_b64_tr_b16 a[254:255], v8 offset:18688\n"
      "v_cvt_pk_bf16_f32 v183, v75, v79\n"
      "v_mfma_f32_16x16x32_bf16 v[136:139], v[200:203], a[236:239], v[136:139]\n"
      "ds_write_b64 v232, v[176:177] offset:0\n"
      "ds_write_b64 v232, v[178:179] offset:128\n"
      "v_mfma_f32_16x16x32_bf16 v[136:139], v[196:199], a[232:235], v[136:139]\n"
      "ds_write_b64 v233, v[180:181] offset:256\n"
      "ds_write_b64 v233, v[182:183] offset:384\n"
      "v_mfma_f32_16x16x32_bf16 a[120:123], v[204:207], a[232:235], a[120:123]\n"
      "ds_read_b64_tr_b16 v[216:217], v234 offset:0\n"
      "ds_read_b64_tr_b16 v[218:219], v235 offset:0\n"
      "v_mfma_f32_16x16x32_bf16 a[120:123], v[208:211], a[236:239], a[120:123]\n"
      "ds_read_b64_tr_b16 v[220:221], v234 offset:1024\n"
      "ds_read_b64_tr_b16 v[222:223], v235 offset:1024\n"
      "v_mfma_f32_16x16x32_bf16 a[168:171], v[212:215], a[236:239], a[168:171]\n"
      "s_waitcnt lgkmcnt(12)\n"
      "ds_read_b64_tr_b16 a[232:233], v9 offset:256\n"
      "v_mul_f32 v112, v112, v242\n"
      "v_mfma_f32_16x16x32_bf16 v[92:95], v[188:191], a[240:243], v[92:95]\n"
      "ds_read_b64_tr_b16 a[234:235], v9 offset:6400\n"
      "ds_read_b64_tr_b16 a[236:237], v9 offset:12544\n"
      "v_mfma_f32_16x16x32_bf16 v[92:95], v[192:195], a[244:247], v[92:95]\n"
      "ds_read_b64_tr_b16 a[238:239], v9 offset:18688\n"
      "v_mul_f32 v113, v113, v243\n"
      "v_mfma_f32_16x16x32_bf16 v[140:143], v[200:203], a[244:247], v[140:143]\n"
      "v_mul_f32 v114, v114, v244\n"
      "v_mul_f32 v115, v115, v245\n"
      "v_mfma_f32_16x16x32_bf16 v[140:143], v[196:199], a[240:243], v[140:143]\n"
      "v_mul_f32 v116, v116, v242\n"
      "v_mul_f32 v117, v117, v243\n"
      "v_mfma_f32_16x16x32_bf16 a[124:127], v[204:207], a[240:243], a[124:127]\n"
      "v_mul_f32 v118, v118, v244\n"
      "v_mul_f32 v119, v119, v245\n"
      "v_mfma_f32_16x16x32_bf16 a[124:127], v[208:211], a[244:247], a[124:127]\n"
      "v_mul_f32 v120, v120, v242\n"
      "v_mul_f32 v121, v121, v243\n"
      "v_mfma_f32_16x16x32_bf16 a[172:175], v[212:215], a[244:247], a[172:175]\n"
      "s_waitcnt lgkmcnt(12)\n"
      "ds_read_b64_tr_b16 a[240:241], v10 offset:256\n"
      "v_mul_f32 v122, v122, v244\n"
      "v_mfma_f32_16x16x32_bf16 v[96:99], v[188:191], a[248:251], v[96:99]\n"
      "s_waitcnt lgkmcnt(5)\n"
      "ds_read_b64_tr_b16 a[242:243], v10 offset:6400\n"
      "ds_read_b64_tr_b16 a[244:245], v10 offset:12544\n"
      "v_mfma_f32_16x16x32_bf16 v[96:99], v[192:195], a[252:255], v[96:99]\n"
      "ds_read_b64_tr_b16 a[246:247], v10 offset:18688\n"
      "v_mul_f32 v123, v123, v245\n"
      "v_mfma_f32_16x16x32_bf16 v[144:147], v[200:203], a[252:255], v[144:147]\n"
      "buffer_store_dwordx4 v[216:219], v250, s[72:75], s52 offen offset:0 sc0 sc1 nt\n"
      "v_mfma_f32_16x16x32_bf16 v[144:147], v[196:199], a[248:251], v[144:147]\n"
      "buffer_store_dwordx4 v[220:223], v250, s[72:75], s52 offen offset:3072 sc0 sc1 nt\n"
      "v_mfma_f32_16x16x32_bf16 a[128:131], v[204:207], a[248:251], a[128:131]\n"
      "v_mul_f32 v124, v124, v242\n"
      "v_mul_f32 v125, v125, v243\n"
      "v_mfma_f32_16x16x32_bf16 a[128:131], v[208:211], a[252:255], a[128:131]\n"
      "v_mul_f32 v126, v126, v244\n"
      "v_mul_f32 v127, v127, v245\n"
      "v_mfma_f32_16x16x32_bf16 a[176:179], v[212:215], a[252:255], a[176:179]\n"
      "s_waitcnt lgkmcnt(4)\n"
      "ds_read_b64_tr_b16 a[248:249], v11 offset:256\n"
      "v_cvt_pk_bf16_f32 v36, v112, v116\n"
      "v_mfma_f32_16x16x32_bf16 v[100:103], v[188:191], a[232:235], v[100:103]\n"
      "ds_read_b64_tr_b16 a[250:251], v11 offset:6400\n"
      "ds_read_b64_tr_b16 a[252:253], v11 offset:12544\n"
      "v_mfma_f32_16x16x32_bf16 v[100:103], v[192:195], a[236:239], v[100:103]\n"
      "ds_read_b64_tr_b16 a[254:255], v11 offset:18688\n"
      "v_cvt_pk_bf16_f32 v37, v120, v124\n"
      "v_mfma_f32_16x16x32_bf16 v[148:151], v[200:203], a[236:239], v[148:151]\n"
      "v_cvt_pk_bf16_f32 v38, v113, v117\n"
      "v_cvt_pk_bf16_f32 v39, v121, v125\n"
      "v_mfma_f32_16x16x32_bf16 v[148:151], v[196:199], a[232:235], v[148:151]\n"
      "v_cvt_pk_bf16_f32 v40, v114, v118\n"
      "v_cvt_pk_bf16_f32 v41, v122, v126\n"
      "v_mfma_f32_16x16x32_bf16 a[132:135], v[204:207], a[232:235], a[132:135]\n"
      "v_cvt_pk_bf16_f32 v42, v115, v119\n"
      "v_cvt_pk_bf16_f32 v43, v123, v127\n"
      "v_mfma_f32_16x16x32_bf16 a[132:135], v[208:211], a[236:239], a[132:135]\n"
      "ds_write_b64 v232, v[36:37] offset:2048\n"
      "ds_write_b64 v232, v[38:39] offset:2176\n"
      "v_mfma_f32_16x16x32_bf16 a[180:183], v[212:215], a[236:239], a[180:183]\n"
      "s_waitcnt lgkmcnt(6)\n"
      "ds_write_b64 v233, v[40:41] offset:2304\n"
      "ds_write_b64 v233, v[42:43] offset:2432\n"
      "v_mfma_f32_16x16x32_bf16 v[104:107], v[188:191], a[240:243], v[104:107]\n"
      "ds_read_b64_tr_b16 v[224:225], v234 offset:2048\n"
      "ds_read_b64_tr_b16 v[226:227], v235 offset:2048\n"
      "v_mfma_f32_16x16x32_bf16 v[104:107], v[192:195], a[244:247], v[104:107]\n"
      "ds_read_b64_tr_b16 v[228:229], v234 offset:3072\n"
      "ds_read_b64_tr_b16 v[230:231], v235 offset:3072\n"
      "v_mfma_f32_16x16x32_bf16 v[152:155], v[200:203], a[244:247], v[152:155]\n"
      "v_accvgpr_read_b32 v160, a96\n"
      "v_accvgpr_read_b32 v161, a97\n"
      "v_mfma_f32_16x16x32_bf16 v[152:155], v[196:199], a[240:243], v[152:155]\n"
      "v_accvgpr_read_b32 v162, a98\n"
      "v_accvgpr_read_b32 v163, a99\n"
      "v_mfma_f32_16x16x32_bf16 a[136:139], v[204:207], a[240:243], a[136:139]\n"
      "v_accvgpr_read_b32 v164, a100\n"
      "v_accvgpr_read_b32 v165, a101\n"
      "v_mfma_f32_16x16x32_bf16 a[136:139], v[208:211], a[244:247], a[136:139]\n"
      "v_accvgpr_read_b32 v166, a102\n"
      "v_accvgpr_read_b32 v167, a103\n"
      "v_mfma_f32_16x16x32_bf16 a[184:187], v[212:215], a[244:247], a[184:187]\n"
      "s_waitcnt lgkmcnt(8)\n"
      "v_accvgpr_read_b32 v168, a104\n"
      "v_accvgpr_read_b32 v169, a105\n"
      "v_mfma_f32_16x16x32_bf16 v[108:111], v[188:191], a[248:251], v[108:111]\n"
      "v_accvgpr_read_b32 v170, a106\n"
      "v_accvgpr_read_b32 v171, a107\n"
      "v_mfma_f32_16x16x32_bf16 v[108:111], v[192:195], a[252:255], v[108:111]\n"
      "v_accvgpr_read_b32 v172, a108\n"
      "v_accvgpr_read_b32 v173, a109\n"
      "v_mfma_f32_16x16x32_bf16 v[156:159], v[200:203], a[252:255], v[156:159]\n"
      "s_waitcnt lgkmcnt(0)\n"
      "v_accvgpr_read_b32 v174, a110\n"
      "v_accvgpr_read_b32 v175, a111\n"
      "v_mfma_f32_16x16x32_bf16 v[156:159], v[196:199], a[248:251], v[156:159]\n"
      "buffer_store_dwordx4 v[224:227], v250, s[72:75], s53 offen offset:0 sc0 sc1 nt\n"
      "v_mfma_f32_16x16x32_bf16 a[140:143], v[204:207], a[248:251], a[140:143]\n"
      "buffer_store_dwordx4 v[228:231], v250, s[72:75], s53 offen offset:3072 sc0 sc1 nt\n"
      "v_mfma_f32_16x16x32_bf16 a[140:143], v[208:211], a[252:255], a[140:143]\n"
      "v_mul_f32 v160, v160, v246\n"
      "v_mul_f32 v161, v161, v247\n"
      "v_mfma_f32_16x16x32_bf16 a[188:191], v[212:215], a[252:255], a[188:191]\n"
      "v_mul_f32 v162, v162, v248\n"
      "v_mul_f32 v163, v163, v249\n"
      "v_mul_f32 v164, v164, v246\n"
      "v_mul_f32 v165, v165, v247\n"
      "v_mul_f32 v166, v166, v248\n"
      "v_mul_f32 v167, v167, v249\n"
      "v_mul_f32 v168, v168, v246\n"
      "v_mul_f32 v169, v169, v247\n"
      "v_mul_f32 v170, v170, v248\n"
      "v_mul_f32 v171, v171, v249\n"
      "v_mul_f32 v172, v172, v246\n"
      "v_mul_f32 v173, v173, v247\n"
      "v_mul_f32 v174, v174, v248\n"
      "v_mul_f32 v175, v175, v249\n"
      "v_cvt_pk_bf16_f32 v176, v160, v164\n"
      "v_cvt_pk_bf16_f32 v177, v168, v172\n"
      "v_cvt_pk_bf16_f32 v178, v161, v165\n"
      "v_cvt_pk_bf16_f32 v179, v169, v173\n"
      "v_cvt_pk_bf16_f32 v180, v162, v166\n"
      "v_cvt_pk_bf16_f32 v181, v170, v174\n"
      "v_cvt_pk_bf16_f32 v182, v163, v167\n"
      "v_cvt_pk_bf16_f32 v183, v171, v175\n"
      "ds_write_b64 v232, v[176:177] offset:4096\n"
      "ds_write_b64 v232, v[178:179] offset:4224\n"
      "ds_write_b64 v233, v[180:181] offset:4352\n"
      "ds_write_b64 v233, v[182:183] offset:4480\n"
      "ds_read_b64_tr_b16 v[44:45], v234 offset:4096\n"
      "ds_read_b64_tr_b16 v[46:47], v235 offset:4096\n"
      "ds_read_b64_tr_b16 v[48:49], v234 offset:5120\n"
      "ds_read_b64_tr_b16 v[50:51], v235 offset:5120\n"
      "s_add_u32 s63, s44, s42\n"
      "s_sub_u32 s64, s63, 6144\n"
      "v_add_u32 v52, s64, v23\n"
      "v_add_u32 v23, s63, v23\n"
      "s_lshl_b32 s63, s61, 9\n"
      "s_add_u32 s63, s63, s65\n"
      "s_add_u32 s63, s63, 147456\n"
      "v_lshlrev_b32 v53, 3, v0\n"
      "v_add_u32 v53, s63, v53\n"
      "v_mov_b32 v54, v21\n"
      "v_accvgpr_read_b32 v55, a204\n"
      "v_xor_b32 v58, 512, v53\n"
      "v_pk_mul_f32 v[80:81], v[80:81], v[238:239]\n"
      "v_pk_mul_f32 v[82:83], v[82:83], v[240:241]\n"
      "v_pk_mul_f32 v[84:85], v[84:85], v[238:239]\n"
      "v_pk_mul_f32 v[86:87], v[86:87], v[240:241]\n"
      "v_pk_mul_f32 v[88:89], v[88:89], v[238:239]\n"
      "v_pk_mul_f32 v[90:91], v[90:91], v[240:241]\n"
      "v_pk_mul_f32 v[92:93], v[92:93], v[238:239]\n"
      "v_pk_mul_f32 v[94:95], v[94:95], v[240:241]\n"
      "s_waitcnt lgkmcnt(0)\n"
      "buffer_store_dwordx4 v[44:47], v250, s[72:75], s54 offen offset:0 sc0 sc1 nt\n"
      "buffer_store_dwordx4 v[48:51], v250, s[72:75], s54 offen offset:3072 sc0 sc1 nt\n"
      "ds_write_b64 v53, v[54:55] offset:0\n"
      "ds_write_b128 v52, a[144:147] offset:0\n"
      "v_cvt_pk_bf16_f32 v176, v80, v84\n"
      "v_cvt_pk_bf16_f32 v177, v88, v92\n"
      "v_cvt_pk_bf16_f32 v178, v81, v85\n"
      "v_cvt_pk_bf16_f32 v179, v89, v93\n"
      "v_cvt_pk_bf16_f32 v180, v82, v86\n"
      "v_cvt_pk_bf16_f32 v181, v90, v94\n"
      "v_cvt_pk_bf16_f32 v182, v83, v87\n"
      "v_cvt_pk_bf16_f32 v183, v91, v95\n"
      "ds_write_b128 v52, a[148:151] offset:1024\n"
      "v_pk_mul_f32 v[96:97], v[96:97], v[238:239]\n"
      "v_pk_mul_f32 v[98:99], v[98:99], v[240:241]\n"
      "v_pk_mul_f32 v[100:101], v[100:101], v[238:239]\n"
      "ds_write_b128 v52, a[152:155] offset:2048\n"
      "ds_write_b128 v52, a[156:159] offset:3072\n"
      "v_pk_mul_f32 v[102:103], v[102:103], v[240:241]\n"
      "v_pk_mul_f32 v[104:105], v[104:105], v[238:239]\n"
      "v_pk_mul_f32 v[106:107], v[106:107], v[240:241]\n"
      "ds_write_b64 v232, v[176:177] offset:0\n"
      "ds_write_b64 v232, v[178:179] offset:128\n"
      "v_pk_mul_f32 v[108:109], v[108:109], v[238:239]\n"
      "v_pk_mul_f32 v[110:111], v[110:111], v[240:241]\n"
      "v_cvt_pk_bf16_f32 v184, v96, v100\n"
      "ds_write_b64 v233, v[180:181] offset:256\n"
      "ds_write_b64 v233, v[182:183] offset:384\n"
      "v_cvt_pk_bf16_f32 v185, v104, v108\n"
      "v_cvt_pk_bf16_f32 v186, v97, v101\n"
      "v_cvt_pk_bf16_f32 v187, v105, v109\n"
      "ds_read_b64_tr_b16 v[192:193], v234 offset:0\n"
      "ds_read_b64_tr_b16 v[194:195], v235 offset:0\n"
      "v_cvt_pk_bf16_f32 v188, v98, v102\n"
      "v_cvt_pk_bf16_f32 v189, v106, v110\n"
      "v_cvt_pk_bf16_f32 v190, v99, v103\n"
      "v_cvt_pk_bf16_f32 v191, v107, v111\n"
      "ds_read_b64_tr_b16 v[196:197], v234 offset:1024\n"
      "ds_read_b64_tr_b16 v[198:199], v235 offset:1024\n"
      "v_pk_mul_f32 v[128:129], v[128:129], v[242:243]\n"
      "v_pk_mul_f32 v[130:131], v[130:131], v[244:245]\n"
      "v_pk_mul_f32 v[132:133], v[132:133], v[242:243]\n"
      "ds_write_b128 v52, a[160:163] offset:4096\n"
      "ds_write_b128 v52, a[164:167] offset:5120\n"
      "v_pk_mul_f32 v[134:135], v[134:135], v[244:245]\n"
      "v_pk_mul_f32 v[136:137], v[136:137], v[242:243]\n"
      "v_pk_mul_f32 v[138:139], v[138:139], v[244:245]\n"
      "v_pk_mul_f32 v[140:141], v[140:141], v[242:243]\n"
      "ds_write_b64 v232, v[184:185] offset:2048\n"
      "ds_write_b64 v232, v[186:187] offset:2176\n"
      "v_pk_mul_f32 v[142:143], v[142:143], v[244:245]\n"
      "v_cvt_pk_bf16_f32 v176, v128, v132\n"
      "s_waitcnt lgkmcnt(4)\n"
      "v_cvt_pk_bf16_f32 v177, v136, v140\n"
      "ds_write_b64 v233, v[188:189] offset:2304\n"
      "ds_write_b64 v233, v[190:191] offset:2432\n"
      "buffer_store_dwordx4 v[192:195], v250, s[72:75], s52 offen offset:128 sc0 sc1 nt\n"
      "v_cvt_pk_bf16_f32 v178, v129, v133\n"
      "v_cvt_pk_bf16_f32 v179, v137, v141\n"
      "v_cvt_pk_bf16_f32 v180, v130, v134\n"
      "ds_read_b64_tr_b16 v[200:201], v234 offset:2048\n"
      "ds_read_b64_tr_b16 v[202:203], v235 offset:2048\n"
      "buffer_store_dwordx4 v[196:199], v250, s[72:75], s52 offen offset:3200 sc0 sc1 nt\n"
      "v_cvt_pk_bf16_f32 v181, v138, v142\n"
      "v_cvt_pk_bf16_f32 v182, v131, v135\n"
      "v_cvt_pk_bf16_f32 v183, v139, v143\n"
      "ds_read_b64_tr_b16 v[204:205], v234 offset:3072\n"
      "ds_read_b64_tr_b16 v[206:207], v235 offset:3072\n"
      "v_pk_mul_f32 v[144:145], v[144:145], v[242:243]\n"
      "v_pk_mul_f32 v[146:147], v[146:147], v[244:245]\n"
      "v_pk_mul_f32 v[148:149], v[148:149], v[242:243]\n"
      "v_pk_mul_f32 v[150:151], v[150:151], v[244:245]\n"
      "ds_write_b64 v232, v[176:177] offset:4096\n"
      "ds_write_b64 v232, v[178:179] offset:4224\n"
      "v_pk_mul_f32 v[152:153], v[152:153], v[242:243]\n"
      "v_pk_mul_f32 v[154:155], v[154:155], v[244:245]\n"
      "v_pk_mul_f32 v[156:157], v[156:157], v[242:243]\n"
      "v_pk_mul_f32 v[158:159], v[158:159], v[244:245]\n"
      "v_cvt_pk_bf16_f32 v184, v144, v148\n"
      "ds_write_b64 v233, v[180:181] offset:4352\n"
      "ds_write_b64 v233, v[182:183] offset:4480\n"
      "s_waitcnt lgkmcnt(4)\n"
      "v_cvt_pk_bf16_f32 v185, v152, v156\n"
      "buffer_store_dwordx4 v[200:203], v250, s[72:75], s52 offen offset:256 sc0 sc1 nt\n"
      "v_cvt_pk_bf16_f32 v186, v145, v149\n"
      "v_cvt_pk_bf16_f32 v187, v153, v157\n"
      "ds_read_b64_tr_b16 v[208:209], v234 offset:4096\n"
      "ds_read_b64_tr_b16 v[210:211], v235 offset:4096\n"
      "v_cvt_pk_bf16_f32 v188, v146, v150\n"
      "buffer_store_dwordx4 v[204:207], v250, s[72:75], s52 offen offset:3328 sc0 sc1 nt\n"
      "v_cvt_pk_bf16_f32 v189, v154, v158\n"
      "v_cvt_pk_bf16_f32 v190, v147, v151\n"
      "v_cvt_pk_bf16_f32 v191, v155, v159\n"
      "ds_read_b64_tr_b16 v[212:213], v234 offset:5120\n"
      "ds_read_b64_tr_b16 v[214:215], v235 offset:5120\n"
      "s_waitcnt lgkmcnt(8)\n"
      "s_barrier\n"
      "v_accvgpr_read_b32 v160, a112\n"
      "v_accvgpr_read_b32 v161, a113\n"
      "v_accvgpr_read_b32 v162, a114\n"
      "v_accvgpr_read_b32 v163, a115\n"
      "v_accvgpr_read_b32 v164, a116\n"
      "ds_write_b64 v232, v[184:185] offset:0\n"
      "ds_write_b64 v232, v[186:187] offset:128\n"
      "v_accvgpr_read_b32 v165, a117\n"
      "v_accvgpr_read_b32 v166, a118\n"
      "v_accvgpr_read_b32 v167, a119\n"
      "v_accvgpr_read_b32 v168, a120\n"
      "v_accvgpr_read_b32 v169, a121\n"
      "v_accvgpr_read_b32 v170, a122\n"
      "ds_write_b64 v233, v[188:189] offset:256\n"
      "ds_write_b64 v233, v[190:191] offset:384\n"
      "v_accvgpr_read_b32 v171, a123\n"
      "v_accvgpr_read_b32 v172, a124\n"
      "v_accvgpr_read_b32 v173, a125\n"
      "v_accvgpr_read_b32 v174, a126\n"
      "v_accvgpr_read_b32 v175, a127\n"
      "v_pk_mul_f32 v[160:161], v[160:161], v[246:247]\n"
      "ds_read_b64_tr_b16 v[192:193], v234 offset:0\n"
      "ds_read_b64_tr_b16 v[194:195], v235 offset:0\n"
      "v_pk_mul_f32 v[162:163], v[162:163], v[248:249]\n"
      "v_pk_mul_f32 v[164:165], v[164:165], v[246:247]\n"
      "v_pk_mul_f32 v[166:167], v[166:167], v[248:249]\n"
      "v_pk_mul_f32 v[168:169], v[168:169], v[246:247]\n"
      "v_pk_mul_f32 v[170:171], v[170:171], v[248:249]\n"
      "v_pk_mul_f32 v[172:173], v[172:173], v[246:247]\n"
      "ds_read_b64_tr_b16 v[196:197], v234 offset:1024\n"
      "ds_read_b64_tr_b16 v[198:199], v235 offset:1024\n"
      "v_pk_mul_f32 v[174:175], v[174:175], v[248:249]\n"
      "v_cvt_pk_bf16_f32 v176, v160, v164\n"
      "s_waitcnt lgkmcnt(8)\n"
      "v_cvt_pk_bf16_f32 v177, v168, v172\n"
      "buffer_store_dwordx4 v[208:211], v250, s[72:75], s53 offen offset:128 sc0 sc1 nt\n"
      "v_cvt_pk_bf16_f32 v178, v161, v165\n"
      "ds_read_b64 v[56:57], v58 offset:0\n"
      "ds_read_b128 v[36:39], v23 offset:0\n"
      "v_cvt_pk_bf16_f32 v179, v169, v173\n"
      "v_cvt_pk_bf16_f32 v180, v162, v166\n"
      "buffer_store_dwordx4 v[212:215], v250, s[72:75], s53 offen offset:3200 sc0 sc1 nt\n"
      "v_cvt_pk_bf16_f32 v181, v170, v174\n"
      "v_cvt_pk_bf16_f32 v182, v163, v167\n"
      "v_cvt_pk_bf16_f32 v183, v171, v175\n"
      "ds_read_b128 v[40:43], v23 offset:1024\n"
      "ds_read_b128 v[44:47], v23 offset:2048\n"
      "s_waitcnt lgkmcnt(3)\n"
      "v_min_f32 v59, v54, v56\n"
      "v_sub_f32 v52, v59, v54\n"
      "v_sub_f32 v53, v59, v56\n"
      "v_exp_f32 v52, v52\n"
      "v_exp_f32 v53, v53\n"
      "v_mul_f32 v55, v55, v52\n"
      "v_fma_f32 v55, v57, v53, v55\n"
      "v_rcp_f32 v55, v55\n"
      "s_nop 0\n"
      "v_mul_f32 v52, v52, v55\n"
      "v_mul_f32 v53, v53, v55\n"
      "v_accvgpr_read_b32 v160, a128\n"
      "v_accvgpr_read_b32 v161, a129\n"
      "v_accvgpr_read_b32 v162, a130\n"
      "v_accvgpr_read_b32 v163, a131\n"
      "ds_write_b32 v236, v52 offset:192\n"
      "ds_write_b32 v236, v53 offset:256\n"
      "v_accvgpr_read_b32 v164, a132\n"
      "v_accvgpr_read_b32 v165, a133\n"
      "v_accvgpr_read_b32 v166, a134\n"
      "v_accvgpr_read_b32 v167, a135\n"
      "ds_read_b128 v[56:59], v237 offset:192\n"
      "ds_read_b128 v[60:63], v237 offset:256\n"
      "v_accvgpr_read_b32 v168, a136\n"
      "v_accvgpr_read_b32 v169, a137\n"
      "v_accvgpr_read_b32 v170, a138\n"
      "v_accvgpr_read_b32 v171, a139\n"
      "v_accvgpr_read_b32 v172, a140\n"
      "ds_write_b64 v232, v[176:177] offset:2048\n"
      "ds_write_b64 v232, v[178:179] offset:2176\n"
      "v_accvgpr_read_b32 v173, a141\n"
      "v_accvgpr_read_b32 v174, a142\n"
      "v_accvgpr_read_b32 v175, a143\n"
      "v_pk_mul_f32 v[160:161], v[160:161], v[246:247]\n"
      "ds_write_b64 v233, v[180:181] offset:2304\n"
      "ds_write_b64 v233, v[182:183] offset:2432\n"
      "v_pk_mul_f32 v[162:163], v[162:163], v[248:249]\n"
      "v_pk_mul_f32 v[164:165], v[164:165], v[246:247]\n"
      "v_pk_mul_f32 v[166:167], v[166:167], v[248:249]\n"
      "v_pk_mul_f32 v[168:169], v[168:169], v[246:247]\n"
      "ds_read_b64_tr_b16 v[200:201], v234 offset:2048\n"
      "ds_read_b64_tr_b16 v[202:203], v235 offset:2048\n"
      "v_pk_mul_f32 v[170:171], v[170:171], v[248:249]\n"
      "v_pk_mul_f32 v[172:173], v[172:173], v[246:247]\n"
      "v_pk_mul_f32 v[174:175], v[174:175], v[248:249]\n"
      "v_cvt_pk_bf16_f32 v184, v160, v164\n"
      "s_waitcnt lgkmcnt(13)\n"
      "ds_read_b64_tr_b16 v[204:205], v234 offset:3072\n"
      "ds_read_b64_tr_b16 v[206:207], v235 offset:3072\n"
      "v_cvt_pk_bf16_f32 v185, v168, v172\n"
      "buffer_store_dwordx4 v[192:195], v250, s[72:75], s53 offen offset:256 sc0 sc1 nt\n"
      "v_cvt_pk_bf16_f32 v186, v161, v165\n"
      "v_cvt_pk_bf16_f32 v187, v169, v173\n"
      "ds_read_b128 v[48:51], v23 offset:3072\n"
      "ds_read_b128 v[216:219], v23 offset:4096\n"
      "v_cvt_pk_bf16_f32 v188, v162, v166\n"
      "buffer_store_dwordx4 v[196:199], v250, s[72:75], s53 offen offset:3328 sc0 sc1 nt\n"
      "v_cvt_pk_bf16_f32 v189, v170, v174\n"
      "v_cvt_pk_bf16_f32 v190, v163, v167\n"
      "v_cvt_pk_bf16_f32 v191, v171, v175\n"
      "ds_read_b128 v[220:223], v23 offset:5120\n"
      "s_waitcnt lgkmcnt(0)\n"
      "v_accvgpr_read_b32 v160, a176\n"
      "v_accvgpr_read_b32 v161, a177\n"
      "v_accvgpr_read_b32 v162, a178\n"
      "v_accvgpr_read_b32 v163, a179\n"
      "v_accvgpr_read_b32 v164, a180\n"
      "v_accvgpr_read_b32 v165, a181\n"
      "v_accvgpr_read_b32 v166, a182\n"
      "v_accvgpr_read_b32 v167, a183\n"
      "v_accvgpr_read_b32 v168, a184\n"
      "v_accvgpr_read_b32 v169, a185\n"
      "ds_write_b64 v232, v[184:185] offset:4096\n"
      "ds_write_b64 v232, v[186:187] offset:4224\n"
      "v_accvgpr_read_b32 v170, a186\n"
      "v_accvgpr_read_b32 v171, a187\n"
      "v_accvgpr_read_b32 v172, a188\n"
      "v_accvgpr_read_b32 v173, a189\n"
      "v_accvgpr_read_b32 v174, a190\n"
      "v_accvgpr_read_b32 v175, a191\n"
      "v_pk_mul_f32 v[160:161], v[160:161], v[56:57]\n"
      "v_pk_mul_f32 v[162:163], v[162:163], v[58:59]\n"
      "v_pk_mul_f32 v[164:165], v[164:165], v[56:57]\n"
      "v_pk_mul_f32 v[166:167], v[166:167], v[58:59]\n"
      "v_pk_mul_f32 v[168:169], v[168:169], v[56:57]\n"
      "ds_write_b64 v233, v[188:189] offset:4352\n"
      "ds_write_b64 v233, v[190:191] offset:4480\n"
      "v_pk_mul_f32 v[170:171], v[170:171], v[58:59]\n"
      "v_pk_mul_f32 v[172:173], v[172:173], v[56:57]\n"
      "v_pk_mul_f32 v[174:175], v[174:175], v[58:59]\n"
      "v_pk_fma_f32 v[160:161], v[44:45], v[60:61], v[160:161]\n"
      "v_pk_fma_f32 v[162:163], v[46:47], v[62:63], v[162:163]\n"
      "v_pk_fma_f32 v[164:165], v[48:49], v[60:61], v[164:165]\n"
      "v_pk_fma_f32 v[166:167], v[50:51], v[62:63], v[166:167]\n"
      "v_pk_fma_f32 v[168:169], v[216:217], v[60:61], v[168:169]\n"
      "v_pk_fma_f32 v[170:171], v[218:219], v[62:63], v[170:171]\n"
      "v_pk_fma_f32 v[172:173], v[220:221], v[60:61], v[172:173]\n"
      "v_pk_fma_f32 v[174:175], v[222:223], v[62:63], v[174:175]\n"
      "ds_read_b64_tr_b16 v[208:209], v234 offset:4096\n"
      "ds_read_b64_tr_b16 v[210:211], v235 offset:4096\n"
      "v_cvt_pk_bf16_f32 v176, v160, v164\n"
      "s_waitcnt lgkmcnt(6)\n"
      "v_cvt_pk_bf16_f32 v177, v168, v172\n"
      "buffer_store_dwordx4 v[200:203], v250, s[72:75], s54 offen offset:128 sc0 sc1 nt\n"
      "v_cvt_pk_bf16_f32 v178, v161, v165\n"
      "v_cvt_pk_bf16_f32 v179, v169, v173\n"
      "v_cvt_pk_bf16_f32 v180, v162, v166\n"
      "buffer_store_dwordx4 v[204:207], v250, s[72:75], s54 offen offset:3200 sc0 sc1 nt\n"
      "v_cvt_pk_bf16_f32 v181, v170, v174\n"
      "v_cvt_pk_bf16_f32 v182, v163, v167\n"
      "v_cvt_pk_bf16_f32 v183, v171, v175\n"
      "ds_read_b64_tr_b16 v[212:213], v234 offset:5120\n"
      "ds_read_b64_tr_b16 v[214:215], v235 offset:5120\n"
      "v_accvgpr_read_b32 v160, a168\n"
      "v_accvgpr_read_b32 v161, a169\n"
      "v_accvgpr_read_b32 v162, a170\n"
      "v_accvgpr_read_b32 v163, a171\n"
      "v_accvgpr_read_b32 v164, a172\n"
      "v_accvgpr_read_b32 v165, a173\n"
      "v_accvgpr_read_b32 v166, a174\n"
      "ds_write_b64 v232, v[176:177] offset:0\n"
      "ds_write_b64 v232, v[178:179] offset:128\n"
      "v_accvgpr_read_b32 v167, a175\n"
      "v_and_b32 v54, 2, v0\n"
      "v_cmp_ne_u32 vcc, 0, v54\n"
      "v_mov_b32 v54, 0x7fff0000\n"
      "s_nop 0\n"
      "v_cndmask_b32 v54, v54, v250, vcc\n"
      "v_pk_mul_f32 v[160:161], v[160:161], v[56:57]\n"
      "v_pk_mul_f32 v[162:163], v[162:163], v[58:59]\n"
      "v_pk_mul_f32 v[164:165], v[164:165], v[56:57]\n"
      "ds_write_b64 v233, v[180:181] offset:256\n"
      "ds_write_b64 v233, v[182:183] offset:384\n"
      "v_pk_mul_f32 v[166:167], v[166:167], v[58:59]\n"
      "v_pk_fma_f32 v[160:161], v[36:37], v[60:61], v[160:161]\n"
      "v_pk_fma_f32 v[162:163], v[38:39], v[62:63], v[162:163]\n"
      "v_pk_fma_f32 v[164:165], v[40:41], v[60:61], v[164:165]\n"
      "v_pk_fma_f32 v[166:167], v[42:43], v[62:63], v[166:167]\n"
      "v_cvt_pk_bf16_f32 v184, v160, v164\n"
      "s_waitcnt lgkmcnt(4)\n"
      "v_cvt_pk_bf16_f32 v185, v160, v164\n"
      "ds_read_b64_tr_b16 v[192:193], v234 offset:0\n"
      "ds_read_b64_tr_b16 v[194:195], v235 offset:0\n"
      "buffer_store_dwordx4 v[208:211], v250, s[72:75], s54 offen offset:256 sc0 sc1 nt\n"
      "v_cvt_pk_bf16_f32 v186, v161, v165\n"
      "v_cvt_pk_bf16_f32 v187, v161, v165\n"
      "v_cvt_pk_bf16_f32 v188, v162, v166\n"
      "buffer_store_dwordx4 v[212:215], v250, s[72:75], s54 offen offset:3328 sc0 sc1 nt\n"
      "v_cvt_pk_bf16_f32 v189, v162, v166\n"
      "v_cvt_pk_bf16_f32 v190, v163, v167\n"
      "v_cvt_pk_bf16_f32 v191, v163, v167\n"
      "ds_read_b64_tr_b16 v[196:197], v234 offset:1024\n"
      "ds_read_b64_tr_b16 v[198:199], v235 offset:1024\n"
      "ds_write_b64 v232, v[184:185] offset:2048\n"
      "ds_write_b64 v232, v[186:187] offset:2176\n"
      "ds_write_b64 v233, v[188:189] offset:2304\n"
      "ds_write_b64 v233, v[190:191] offset:2432\n"
      "s_waitcnt lgkmcnt(4)\n"
      "buffer_store_dwordx4 v[192:195], v250, s[72:75], s55 offen offset:256 sc0 sc1 nt\n"
      "buffer_store_dwordx4 v[196:199], v250, s[72:75], s55 offen offset:3328 sc0 sc1 nt\n"
      "ds_read_b64_tr_b16 v[200:201], v234 offset:2048\n"
      "ds_read_b64_tr_b16 v[202:203], v235 offset:2048\n"
      "ds_read_b64_tr_b16 v[204:205], v234 offset:3072\n"
      "ds_read_b64_tr_b16 v[206:207], v235 offset:3072\n"
      "s_waitcnt lgkmcnt(0)\n"
      "buffer_store_dwordx4 v[200:203], v54, s[72:75], s55 offen offset:128 sc0 sc1\n"
      "buffer_store_dwordx4 v[204:207], v54, s[72:75], s55 offen offset:3200 sc0 sc1\n"
      "s_branch L_end\n"
      "L_pro_N:\n"
      "s_mov_b32 s50, 7\n"
      "ds_read_b128 a[208:211], v4 offset:0\n"
      "ds_read_b128 a[212:215], v5 offset:0\n"
      "ds_read_b128 a[216:219], v4 offset:128\n"
      "ds_read_b128 a[220:223], v5 offset:128\n"
      "ds_read_b128 a[224:227], v4 offset:256\n"
      "ds_read_b128 a[228:231], v5 offset:256\n"
      "s_add_u32 s43, s44, 49152\n"
      "s_sub_u32 s43, s43, s65\n"
      "v_add_u32 v232, s43, v6\n"
      "v_add_u32 v233, s43, v7\n"
      "s_waitcnt vmcnt(6)\n"
      "ds_read_b128 a[0:3], v232 offset:0\n"
      "ds_read_b128 a[4:7], v233 offset:0\n"
      "ds_read_b128 a[8:11], v232 offset:128\n"
      "ds_read_b128 a[12:15], v233 offset:128\n"
      "ds_read_b128 a[16:19], v232 offset:256\n"
      "ds_read_b128 a[20:23], v233 offset:256\n"
      "s_add_u32 m0, s45, 49152\n"
      "s_add_u32 s63, s54, 0\n"
      "buffer_load_dwordx4 v1, s[28:31], s63 offen offset:0 nt lds\n"
      "buffer_load_dwordx4 v2, s[28:31], s63 offen offset:1024 nt lds\n"
      "buffer_load_dwordx4 v3, s[28:31], s63 offen offset:2048 nt lds\n"
      "s_add_u32 m0, s45, 52224\n"
      "s_add_u32 s63, s54, 3072\n"
      "buffer_load_dwordx4 v1, s[28:31], s63 offen offset:0 nt lds\n"
      "buffer_load_dwordx4 v2, s[28:31], s63 offen offset:1024 nt lds\n"
      "buffer_load_dwordx4 v3, s[28:31], s63 offen offset:2048 nt lds\n"
      "s_waitcnt lgkmcnt(0)\n"
      "v_mfma_f32_16x16x32_bf16 v[36:39], a[208:211], a[0:3], 0\n"
      "ds_read_b128 a[232:235], v4 offset:6144\n"
      "ds_read_b128 a[236:239], v5 offset:6144\n"
      "v_mfma_f32_16x16x32_bf16 v[36:39], a[212:215], a[4:7], v[36:39]\n"
      "ds_read_b128 a[240:243], v4 offset:6272\n"
      "ds_read_b128 a[244:247], v5 offset:6272\n"
      "v_mfma_f32_16x16x32_bf16 v[36:39], a[216:219], a[8:11], v[36:39]\n"
      "ds_read_b128 a[248:251], v4 offset:6400\n"
      "ds_read_b128 a[252:255], v5 offset:6400\n"
      "v_mfma_f32_16x16x32_bf16 v[36:39], a[220:223], a[12:15], v[36:39]\n"
      "v_mfma_f32_16x16x32_bf16 v[36:39], a[224:227], a[16:19], v[36:39]\n"
      "v_mfma_f32_16x16x32_bf16 v[36:39], a[228:231], a[20:23], v[36:39]\n"
      "s_waitcnt lgkmcnt(4)\n"
      "v_mfma_f32_16x16x32_bf16 v[40:43], a[232:235], a[0:3], 0\n"
      "ds_read_b128 a[208:211], v4 offset:12288\n"
      "ds_read_b128 a[212:215], v5 offset:12288\n"
      "v_mfma_f32_16x16x32_bf16 v[40:43], a[236:239], a[4:7], v[40:43]\n"
      "s_waitcnt lgkmcnt(4)\n"
      "ds_read_b128 a[216:219], v4 offset:12416\n"
      "ds_read_b128 a[220:223], v5 offset:12416\n"
      "v_mfma_f32_16x16x32_bf16 v[40:43], a[240:243], a[8:11], v[40:43]\n"
      "ds_read_b128 a[224:227], v4 offset:12544\n"
      "ds_read_b128 a[228:231], v5 offset:12544\n"
      "v_mfma_f32_16x16x32_bf16 v[40:43], a[244:247], a[12:15], v[40:43]\n"
      "s_waitcnt lgkmcnt(6)\n"
      "v_max3_f32 v52, v36, v37, v38\n"
      "v_max_f32 v52, v52, v39\n"
      "v_mfma_f32_16x16x32_bf16 v[40:43], a[248:251], a[16:19], v[40:43]\n"
      "v_mfma_f32_16x16x32_bf16 v[40:43], a[252:255], a[20:23], v[40:43]\n"
      "s_waitcnt lgkmcnt(4)\n"
      "v_mfma_f32_16x16x32_bf16 v[44:47], a[208:211], a[0:3], 0\n"
      "ds_read_b128 a[232:235], v4 offset:18432\n"
      "ds_read_b128 a[236:239], v5 offset:18432\n"
      "v_mfma_f32_16x16x32_bf16 v[44:47], a[212:215], a[4:7], v[44:47]\n"
      "s_waitcnt lgkmcnt(4)\n"
      "ds_read_b128 a[240:243], v4 offset:18560\n"
      "ds_read_b128 a[244:247], v5 offset:18560\n"
      "v_mfma_f32_16x16x32_bf16 v[44:47], a[216:219], a[8:11], v[44:47]\n"
      "ds_read_b128 a[248:251], v4 offset:18688\n"
      "ds_read_b128 a[252:255], v5 offset:18688\n"
      "v_mfma_f32_16x16x32_bf16 v[44:47], a[220:223], a[12:15], v[44:47]\n"
      "s_waitcnt lgkmcnt(6)\n"
      "v_max3_f32 v52, v52, v40, v41\n"
      "v_max3_f32 v52, v52, v42, v43\n"
      "v_mfma_f32_16x16x32_bf16 v[44:47], a[224:227], a[16:19], v[44:47]\n"
      "v_mfma_f32_16x16x32_bf16 v[44:47], a[228:231], a[20:23], v[44:47]\n"
      "s_waitcnt lgkmcnt(4)\n"
      "v_mfma_f32_16x16x32_bf16 v[48:51], a[232:235], a[0:3], 0\n"
      "ds_read_b128 a[208:211], v4 offset:0\n"
      "ds_read_b128 a[212:215], v5 offset:0\n"
      "v_mfma_f32_16x16x32_bf16 v[48:51], a[236:239], a[4:7], v[48:51]\n"
      "s_waitcnt lgkmcnt(4)\n"
      "ds_read_b128 a[216:219], v4 offset:128\n"
      "ds_read_b128 a[220:223], v5 offset:128\n"
      "v_mfma_f32_16x16x32_bf16 v[48:51], a[240:243], a[8:11], v[48:51]\n"
      "ds_read_b128 a[224:227], v4 offset:256\n"
      "ds_read_b128 a[228:231], v5 offset:256\n"
      "v_mfma_f32_16x16x32_bf16 v[48:51], a[244:247], a[12:15], v[48:51]\n"
      "s_waitcnt lgkmcnt(6)\n"
      "v_max3_f32 v52, v52, v44, v45\n"
      "v_max3_f32 v52, v52, v46, v47\n"
      "v_mfma_f32_16x16x32_bf16 v[48:51], a[248:251], a[16:19], v[48:51]\n"
      "v_mfma_f32_16x16x32_bf16 v[48:51], a[252:255], a[20:23], v[48:51]\n"
      "s_nop 7\n"
      "v_max3_f32 v52, v52, v48, v49\n"
      "v_max3_f32 v52, v52, v50, v51\n"
      "v_mov_b32 v53, v52\n"
      "s_nop 1\n"
      "v_permlane32_swap_b32 v52, v53\n"
      "v_max_f32 v52, v52, v53\n"
      "v_mov_b32 v53, v52\n"
      "s_nop 1\n"
      "v_permlane16_swap_b32 v52, v53\n"
      "v_max_f32 v52, v52, v53\n"
      "v_mul_f32 v52, s62, v52\n"
      "v_add_f32 v18, 0xc1800000, v52\n"
      "v_fma_f32 v36, v36, s48, v18\n"
      "v_fma_f32 v37, v37, s48, v18\n"
      "v_fma_f32 v38, v38, s48, v18\n"
      "v_fma_f32 v39, v39, s48, v18\n"
      "v_exp_f32 v28, v36\n"
      "v_exp_f32 v29, v37\n"
      "v_exp_f32 v30, v38\n"
      "v_exp_f32 v31, v39\n"
      "v_cvt_pk_bf16_f32 v160, v28, v29\n"
      "v_cvt_pk_bf16_f32 v161, v30, v31\n"
      "v_fma_f32 v40, v40, s48, v18\n"
      "v_fma_f32 v41, v41, s48, v18\n"
      "v_fma_f32 v42, v42, s48, v18\n"
      "v_fma_f32 v43, v43, s48, v18\n"
      "v_exp_f32 v32, v40\n"
      "v_exp_f32 v33, v41\n"
      "v_exp_f32 v34, v42\n"
      "v_exp_f32 v35, v43\n"
      "v_cvt_pk_bf16_f32 v162, v32, v33\n"
      "v_cvt_pk_bf16_f32 v163, v34, v35\n"
      "v_fma_f32 v44, v44, s48, v18\n"
      "v_fma_f32 v45, v45, s48, v18\n"
      "v_fma_f32 v46, v46, s48, v18\n"
      "v_fma_f32 v47, v47, s48, v18\n"
      "v_exp_f32 v28, v44\n"
      "v_exp_f32 v29, v45\n"
      "v_exp_f32 v30, v46\n"
      "v_exp_f32 v31, v47\n"
      "v_cvt_pk_bf16_f32 v164, v28, v29\n"
      "v_cvt_pk_bf16_f32 v165, v30, v31\n"
      "v_fma_f32 v48, v48, s48, v18\n"
      "v_fma_f32 v49, v49, s48, v18\n"
      "v_fma_f32 v50, v50, s48, v18\n"
      "v_fma_f32 v51, v51, s48, v18\n"
      "v_exp_f32 v32, v48\n"
      "v_exp_f32 v33, v49\n"
      "v_exp_f32 v34, v50\n"
      "v_exp_f32 v35, v51\n"
      "v_cvt_pk_bf16_f32 v166, v32, v33\n"
      "v_cvt_pk_bf16_f32 v167, v34, v35\n"
      "s_add_u32 s43, s45, 24576\n"
      "s_sub_u32 s43, s43, s65\n"
      "v_add_u32 v232, s43, v6\n"
      "v_add_u32 v233, s43, v7\n"
      "s_waitcnt vmcnt(6)\n"
      "ds_read_b128 a[24:27], v232 offset:0\n"
      "ds_read_b128 a[28:31], v233 offset:0\n"
      "ds_read_b128 a[32:35], v232 offset:128\n"
      "ds_read_b128 a[36:39], v233 offset:128\n"
      "ds_read_b128 a[40:43], v232 offset:256\n"
      "ds_read_b128 a[44:47], v233 offset:256\n"
      "s_add_u32 m0, s44, 24576\n"
      "s_add_u32 s63, s37, 0\n"
      "buffer_load_dwordx4 v1, s[20:23], s63 offen offset:0 lds\n"
      "buffer_load_dwordx4 v2, s[20:23], s63 offen offset:1024 lds\n"
      "buffer_load_dwordx4 v3, s[20:23], s63 offen offset:2048 lds\n"
      "s_add_u32 m0, s44, 27648\n"
      "s_add_u32 s63, s37, 3072\n"
      "buffer_load_dwordx4 v1, s[20:23], s63 offen offset:0 lds\n"
      "buffer_load_dwordx4 v2, s[20:23], s63 offen offset:1024 lds\n"
      "buffer_load_dwordx4 v3, s[20:23], s63 offen offset:2048 lds\n"
      "s_waitcnt lgkmcnt(0)\n"
      "v_mfma_f32_16x16x32_bf16 v[36:39], a[208:211], a[24:27], 0\n"
      "ds_read_b128 a[232:235], v4 offset:6144\n"
      "ds_read_b128 a[236:239], v5 offset:6144\n"
      "v_mfma_f32_16x16x32_bf16 v[36:39], a[212:215], a[28:31], v[36:39]\n"
      "ds_read_b128 a[240:243], v4 offset:6272\n"
      "ds_read_b128 a[244:247], v5 offset:6272\n"
      "v_mfma_f32_16x16x32_bf16 v[36:39], a[216:219], a[32:35], v[36:39]\n"
      "ds_read_b128 a[248:251], v4 offset:6400\n"
      "ds_read_b128 a[252:255], v5 offset:6400\n"
      "v_mfma_f32_16x16x32_bf16 v[36:39], a[220:223], a[36:39], v[36:39]\n"
      "v_mfma_f32_16x16x32_bf16 v[36:39], a[224:227], a[40:43], v[36:39]\n"
      "v_mfma_f32_16x16x32_bf16 v[36:39], a[228:231], a[44:47], v[36:39]\n"
      "s_waitcnt lgkmcnt(4)\n"
      "v_mfma_f32_16x16x32_bf16 v[40:43], a[232:235], a[24:27], 0\n"
      "ds_read_b128 a[208:211], v4 offset:12288\n"
      "ds_read_b128 a[212:215], v5 offset:12288\n"
      "v_mfma_f32_16x16x32_bf16 v[40:43], a[236:239], a[28:31], v[40:43]\n"
      "s_waitcnt lgkmcnt(4)\n"
      "ds_read_b128 a[216:219], v4 offset:12416\n"
      "ds_read_b128 a[220:223], v5 offset:12416\n"
      "v_mfma_f32_16x16x32_bf16 v[40:43], a[240:243], a[32:35], v[40:43]\n"
      "ds_read_b128 a[224:227], v4 offset:12544\n"
      "ds_read_b128 a[228:231], v5 offset:12544\n"
      "v_mfma_f32_16x16x32_bf16 v[40:43], a[244:247], a[36:39], v[40:43]\n"
      "s_waitcnt lgkmcnt(6)\n"
      "v_max3_f32 v52, v36, v37, v38\n"
      "v_max_f32 v52, v52, v39\n"
      "v_mfma_f32_16x16x32_bf16 v[40:43], a[248:251], a[40:43], v[40:43]\n"
      "v_mfma_f32_16x16x32_bf16 v[40:43], a[252:255], a[44:47], v[40:43]\n"
      "s_waitcnt lgkmcnt(4)\n"
      "v_mfma_f32_16x16x32_bf16 v[44:47], a[208:211], a[24:27], 0\n"
      "ds_read_b128 a[232:235], v4 offset:18432\n"
      "ds_read_b128 a[236:239], v5 offset:18432\n"
      "v_mfma_f32_16x16x32_bf16 v[44:47], a[212:215], a[28:31], v[44:47]\n"
      "s_waitcnt lgkmcnt(4)\n"
      "ds_read_b128 a[240:243], v4 offset:18560\n"
      "ds_read_b128 a[244:247], v5 offset:18560\n"
      "v_mfma_f32_16x16x32_bf16 v[44:47], a[216:219], a[32:35], v[44:47]\n"
      "ds_read_b128 a[248:251], v4 offset:18688\n"
      "ds_read_b128 a[252:255], v5 offset:18688\n"
      "v_mfma_f32_16x16x32_bf16 v[44:47], a[220:223], a[36:39], v[44:47]\n"
      "s_waitcnt lgkmcnt(6)\n"
      "v_max3_f32 v52, v52, v40, v41\n"
      "v_max3_f32 v52, v52, v42, v43\n"
      "v_mfma_f32_16x16x32_bf16 v[44:47], a[224:227], a[40:43], v[44:47]\n"
      "v_mfma_f32_16x16x32_bf16 v[44:47], a[228:231], a[44:47], v[44:47]\n"
      "s_waitcnt lgkmcnt(4)\n"
      "v_mfma_f32_16x16x32_bf16 v[48:51], a[232:235], a[24:27], 0\n"
      "ds_read_b128 a[208:211], v4 offset:0\n"
      "ds_read_b128 a[212:215], v5 offset:0\n"
      "v_mfma_f32_16x16x32_bf16 v[48:51], a[236:239], a[28:31], v[48:51]\n"
      "s_waitcnt lgkmcnt(4)\n"
      "ds_read_b128 a[216:219], v4 offset:128\n"
      "ds_read_b128 a[220:223], v5 offset:128\n"
      "v_mfma_f32_16x16x32_bf16 v[48:51], a[240:243], a[32:35], v[48:51]\n"
      "ds_read_b128 a[224:227], v4 offset:256\n"
      "ds_read_b128 a[228:231], v5 offset:256\n"
      "v_mfma_f32_16x16x32_bf16 v[48:51], a[244:247], a[36:39], v[48:51]\n"
      "s_waitcnt lgkmcnt(6)\n"
      "v_max3_f32 v52, v52, v44, v45\n"
      "v_max3_f32 v52, v52, v46, v47\n"
      "v_mfma_f32_16x16x32_bf16 v[48:51], a[248:251], a[40:43], v[48:51]\n"
      "v_mfma_f32_16x16x32_bf16 v[48:51], a[252:255], a[44:47], v[48:51]\n"
      "s_nop 7\n"
      "v_max3_f32 v52, v52, v48, v49\n"
      "v_max3_f32 v52, v52, v50, v51\n"
      "v_mov_b32 v53, v52\n"
      "s_nop 1\n"
      "v_permlane32_swap_b32 v52, v53\n"
      "v_max_f32 v52, v52, v53\n"
      "v_mov_b32 v53, v52\n"
      "s_nop 1\n"
      "v_permlane16_swap_b32 v52, v53\n"
      "v_max_f32 v52, v52, v53\n"
      "v_mul_f32 v52, s62, v52\n"
      "v_add_f32 v19, 0xc1800000, v52\n"
      "v_fma_f32 v36, v36, s48, v19\n"
      "v_fma_f32 v37, v37, s48, v19\n"
      "v_fma_f32 v38, v38, s48, v19\n"
      "v_fma_f32 v39, v39, s48, v19\n"
      "v_exp_f32 v28, v36\n"
      "v_exp_f32 v29, v37\n"
      "v_exp_f32 v30, v38\n"
      "v_exp_f32 v31, v39\n"
      "v_cvt_pk_bf16_f32 v168, v28, v29\n"
      "v_cvt_pk_bf16_f32 v169, v30, v31\n"
      "v_fma_f32 v40, v40, s48, v19\n"
      "v_fma_f32 v41, v41, s48, v19\n"
      "v_fma_f32 v42, v42, s48, v19\n"
      "v_fma_f32 v43, v43, s48, v19\n"
      "v_exp_f32 v32, v40\n"
      "v_exp_f32 v33, v41\n"
      "v_exp_f32 v34, v42\n"
      "v_exp_f32 v35, v43\n"
      "v_cvt_pk_bf16_f32 v170, v32, v33\n"
      "v_cvt_pk_bf16_f32 v171, v34, v35\n"
      "v_fma_f32 v44, v44, s48, v19\n"
      "v_fma_f32 v45, v45, s48, v19\n"
      "v_fma_f32 v46, v46, s48, v19\n"
      "v_fma_f32 v47, v47, s48, v19\n"
      "v_exp_f32 v28, v44\n"
      "v_exp_f32 v29, v45\n"
      "v_exp_f32 v30, v46\n"
      "v_exp_f32 v31, v47\n"
      "v_cvt_pk_bf16_f32 v172, v28, v29\n"
      "v_cvt_pk_bf16_f32 v173, v30, v31\n"
      "v_fma_f32 v48, v48, s48, v19\n"
      "v_fma_f32 v49, v49, s48, v19\n"
      "v_fma_f32 v50, v50, s48, v19\n"
      "v_fma_f32 v51, v51, s48, v19\n"
      "v_exp_f32 v32, v48\n"
      "v_exp_f32 v33, v49\n"
      "v_exp_f32 v34, v50\n"
      "v_exp_f32 v35, v51\n"
      "v_cvt_pk_bf16_f32 v174, v32, v33\n"
      "v_cvt_pk_bf16_f32 v175, v34, v35\n"
      "s_add_u32 s43, s45, 49152\n"
      "s_sub_u32 s43, s43, s65\n"
      "v_add_u32 v232, s43, v6\n"
      "v_add_u32 v233, s43, v7\n"
      "s_waitcnt vmcnt(6)\n"
      "ds_read_b128 a[48:51], v232 offset:0\n"
      "ds_read_b128 a[52:55], v233 offset:0\n"
      "ds_read_b128 a[56:59], v232 offset:128\n"
      "ds_read_b128 a[60:63], v233 offset:128\n"
      "ds_read_b128 a[64:67], v232 offset:256\n"
      "ds_read_b128 a[68:71], v233 offset:256\n"
      "s_add_u32 m0, s45, 0\n"
      "s_add_u32 s63, s36, 0\n"
      "buffer_load_dwordx4 v1, s[24:27], s63 offen offset:0 lds\n"
      "buffer_load_dwordx4 v2, s[24:27], s63 offen offset:1024 lds\n"
      "buffer_load_dwordx4 v3, s[24:27], s63 offen offset:2048 lds\n"
      "s_add_u32 m0, s45, 3072\n"
      "s_add_u32 s63, s36, 3072\n"
      "buffer_load_dwordx4 v1, s[24:27], s63 offen offset:0 lds\n"
      "buffer_load_dwordx4 v2, s[24:27], s63 offen offset:1024 lds\n"
      "buffer_load_dwordx4 v3, s[24:27], s63 offen offset:2048 lds\n"
      "s_waitcnt lgkmcnt(0)\n"
      "v_mfma_f32_16x16x32_bf16 v[36:39], a[208:211], a[48:51], 0\n"
      "ds_read_b128 a[232:235], v4 offset:6144\n"
      "ds_read_b128 a[236:239], v5 offset:6144\n"
      "v_mfma_f32_16x16x32_bf16 v[36:39], a[212:215], a[52:55], v[36:39]\n"
      "ds_read_b128 a[240:243], v4 offset:6272\n"
      "ds_read_b128 a[244:247], v5 offset:6272\n"
      "v_mfma_f32_16x16x32_bf16 v[36:39], a[216:219], a[56:59], v[36:39]\n"
      "ds_read_b128 a[248:251], v4 offset:6400\n"
      "ds_read_b128 a[252:255], v5 offset:6400\n"
      "v_mfma_f32_16x16x32_bf16 v[36:39], a[220:223], a[60:63], v[36:39]\n"
      "v_mfma_f32_16x16x32_bf16 v[36:39], a[224:227], a[64:67], v[36:39]\n"
      "v_mfma_f32_16x16x32_bf16 v[36:39], a[228:231], a[68:71], v[36:39]\n"
      "s_waitcnt lgkmcnt(4)\n"
      "v_mfma_f32_16x16x32_bf16 v[40:43], a[232:235], a[48:51], 0\n"
      "ds_read_b128 a[208:211], v4 offset:12288\n"
      "ds_read_b128 a[212:215], v5 offset:12288\n"
      "v_mfma_f32_16x16x32_bf16 v[40:43], a[236:239], a[52:55], v[40:43]\n"
      "s_waitcnt lgkmcnt(4)\n"
      "ds_read_b128 a[216:219], v4 offset:12416\n"
      "ds_read_b128 a[220:223], v5 offset:12416\n"
      "v_mfma_f32_16x16x32_bf16 v[40:43], a[240:243], a[56:59], v[40:43]\n"
      "ds_read_b128 a[224:227], v4 offset:12544\n"
      "ds_read_b128 a[228:231], v5 offset:12544\n"
      "v_mfma_f32_16x16x32_bf16 v[40:43], a[244:247], a[60:63], v[40:43]\n"
      "s_waitcnt lgkmcnt(6)\n"
      "v_max3_f32 v52, v36, v37, v38\n"
      "v_max_f32 v52, v52, v39\n"
      "v_mfma_f32_16x16x32_bf16 v[40:43], a[248:251], a[64:67], v[40:43]\n"
      "v_mfma_f32_16x16x32_bf16 v[40:43], a[252:255], a[68:71], v[40:43]\n"
      "s_waitcnt lgkmcnt(4)\n"
      "v_mfma_f32_16x16x32_bf16 v[44:47], a[208:211], a[48:51], 0\n"
      "ds_read_b128 a[232:235], v4 offset:18432\n"
      "ds_read_b128 a[236:239], v5 offset:18432\n"
      "v_mfma_f32_16x16x32_bf16 v[44:47], a[212:215], a[52:55], v[44:47]\n"
      "s_waitcnt lgkmcnt(4)\n"
      "ds_read_b128 a[240:243], v4 offset:18560\n"
      "ds_read_b128 a[244:247], v5 offset:18560\n"
      "v_mfma_f32_16x16x32_bf16 v[44:47], a[216:219], a[56:59], v[44:47]\n"
      "ds_read_b128 a[248:251], v4 offset:18688\n"
      "ds_read_b128 a[252:255], v5 offset:18688\n"
      "v_mfma_f32_16x16x32_bf16 v[44:47], a[220:223], a[60:63], v[44:47]\n"
      "s_waitcnt lgkmcnt(6)\n"
      "v_max3_f32 v52, v52, v40, v41\n"
      "v_max3_f32 v52, v52, v42, v43\n"
      "v_mfma_f32_16x16x32_bf16 v[44:47], a[224:227], a[64:67], v[44:47]\n"
      "v_mfma_f32_16x16x32_bf16 v[44:47], a[228:231], a[68:71], v[44:47]\n"
      "s_waitcnt lgkmcnt(4)\n"
      "v_mfma_f32_16x16x32_bf16 v[48:51], a[232:235], a[48:51], 0\n"
      "s_nop 2\n"
      "v_mfma_f32_16x16x32_bf16 v[48:51], a[236:239], a[52:55], v[48:51]\n"
      "s_waitcnt lgkmcnt(2)\n"
      "v_mfma_f32_16x16x32_bf16 v[48:51], a[240:243], a[56:59], v[48:51]\n"
      "v_max3_f32 v52, v52, v44, v45\n"
      "v_max3_f32 v52, v52, v46, v47\n"
      "v_mfma_f32_16x16x32_bf16 v[48:51], a[244:247], a[60:63], v[48:51]\n"
      "s_waitcnt lgkmcnt(0)\n"
      "v_mfma_f32_16x16x32_bf16 v[48:51], a[248:251], a[64:67], v[48:51]\n"
      "v_mfma_f32_16x16x32_bf16 v[48:51], a[252:255], a[68:71], v[48:51]\n"
      "s_nop 7\n"
      "v_max3_f32 v52, v52, v48, v49\n"
      "v_max3_f32 v52, v52, v50, v51\n"
      "v_mov_b32 v53, v52\n"
      "s_nop 1\n"
      "v_permlane32_swap_b32 v52, v53\n"
      "v_max_f32 v52, v52, v53\n"
      "v_mov_b32 v53, v52\n"
      "s_nop 1\n"
      "v_permlane16_swap_b32 v52, v53\n"
      "v_max_f32 v52, v52, v53\n"
      "v_mul_f32 v52, s62, v52\n"
      "v_add_f32 v20, 0xc1800000, v52\n"
      "v_fma_f32 v36, v36, s48, v20\n"
      "v_fma_f32 v37, v37, s48, v20\n"
      "v_fma_f32 v38, v38, s48, v20\n"
      "v_fma_f32 v39, v39, s48, v20\n"
      "v_exp_f32 v28, v36\n"
      "v_exp_f32 v29, v37\n"
      "v_exp_f32 v30, v38\n"
      "v_exp_f32 v31, v39\n"
      "v_cvt_pk_bf16_f32 v176, v28, v29\n"
      "v_cvt_pk_bf16_f32 v177, v30, v31\n"
      "v_fma_f32 v40, v40, s48, v20\n"
      "v_fma_f32 v41, v41, s48, v20\n"
      "v_fma_f32 v42, v42, s48, v20\n"
      "v_fma_f32 v43, v43, s48, v20\n"
      "v_exp_f32 v32, v40\n"
      "v_exp_f32 v33, v41\n"
      "v_exp_f32 v34, v42\n"
      "v_exp_f32 v35, v43\n"
      "v_cvt_pk_bf16_f32 v178, v32, v33\n"
      "v_cvt_pk_bf16_f32 v179, v34, v35\n"
      "v_fma_f32 v44, v44, s48, v20\n"
      "v_fma_f32 v45, v45, s48, v20\n"
      "v_fma_f32 v46, v46, s48, v20\n"
      "v_fma_f32 v47, v47, s48, v20\n"
      "v_exp_f32 v28, v44\n"
      "v_exp_f32 v29, v45\n"
      "v_exp_f32 v30, v46\n"
      "v_exp_f32 v31, v47\n"
      "v_cvt_pk_bf16_f32 v180, v28, v29\n"
      "v_cvt_pk_bf16_f32 v181, v30, v31\n"
      "v_fma_f32 v48, v48, s48, v20\n"
      "v_fma_f32 v49, v49, s48, v20\n"
      "v_fma_f32 v50, v50, s48, v20\n"
      "v_fma_f32 v51, v51, s48, v20\n"
      "v_exp_f32 v32, v48\n"
      "v_exp_f32 v33, v49\n"
      "v_exp_f32 v34, v50\n"
      "v_exp_f32 v35, v51\n"
      "v_cvt_pk_bf16_f32 v182, v32, v33\n"
      "v_cvt_pk_bf16_f32 v183, v34, v35\n"
      "s_add_u32 m0, s44, 49152\n"
      "s_add_u32 s63, s38, 0\n"
      "buffer_load_dwordx4 v1, s[20:23], s63 offen offset:0 lds\n"
      "buffer_load_dwordx4 v2, s[20:23], s63 offen offset:1024 lds\n"
      "buffer_load_dwordx4 v3, s[20:23], s63 offen offset:2048 lds\n"
      "s_add_u32 m0, s44, 52224\n"
      "s_add_u32 s63, s38, 3072\n"
      "buffer_load_dwordx4 v1, s[20:23], s63 offen offset:0 lds\n"
      "buffer_load_dwordx4 v2, s[20:23], s63 offen offset:1024 lds\n"
      "buffer_load_dwordx4 v3, s[20:23], s63 offen offset:2048 lds\n"
      "s_add_u32 m0, s45, 24576\n"
      "s_add_u32 s63, s37, 0\n"
      "buffer_load_dwordx4 v1, s[24:27], s63 offen offset:0 lds\n"
      "buffer_load_dwordx4 v2, s[24:27], s63 offen offset:1024 lds\n"
      "buffer_load_dwordx4 v3, s[24:27], s63 offen offset:2048 lds\n"
      "s_add_u32 m0, s45, 27648\n"
      "s_add_u32 s63, s37, 3072\n"
      "buffer_load_dwordx4 v1, s[24:27], s63 offen offset:0 lds\n"
      "buffer_load_dwordx4 v2, s[24:27], s63 offen offset:1024 lds\n"
      "buffer_load_dwordx4 v3, s[24:27], s63 offen offset:2048 lds\n"
      "s_barrier\n"
      "v_add_u32 v232, 24576, v6\n"
      "v_add_u32 v233, 24576, v7\n"
      "v_mov_b32 v234, v12\n"
      "v_mov_b32 v235, v13\n"
      "v_mov_b32 v236, v14\n"
      "v_mov_b32 v237, v15\n"
      "s_add_u32 s39, s38, 3072\n"
      "s_mov_b32 s12, 3\n"
      "s_add_u32 s15, s13, 3\n"
      "s_and_b32 s15, s15, 15\n"
      "s_mul_i32 s15, s15, 24576\n"
      "s_add_u32 s36, s15, s14\n"
      "s_add_u32 s37, s36, 3072\n"
      "s_mov_b32 s40, 24576\n"
      "s_mov_b32 s41, 49152\n"
      "s_mov_b32 s42, 0\n"
      "s_waitcnt vmcnt(12)\n"
      "s_barrier\n"
      "ds_read_b128 a[208:211], v232 offset:0\n"
      "ds_read_b128 a[212:215], v233 offset:0\n"
      "ds_read_b128 a[216:219], v232 offset:128\n"
      "ds_read_b128 a[220:223], v233 offset:128\n"
      "ds_read_b128 a[224:227], v232 offset:256\n"
      "ds_read_b128 a[228:231], v233 offset:256\n"
      "s_waitcnt lgkmcnt(0)\n"
      "s_branch L_loop_N\n"
      ".p2align 6\n"
      "L_loop_N:\n"
      "v_mfma_f32_16x16x32_bf16 v[36:39], a[208:211], a[0:3], 0\n"
      "s_add_u32 s46, s44, s42\n"
      "ds_read_b64_tr_b16 a[232:233], v234 offset:0\n"
      "v_add_u32 v4, s41, v6\n"
      "v_mfma_f32_16x16x32_bf16 v[36:39], a[212:215], a[4:7], v[36:39]\n"
      "s_add_u32 s47, s45, s41\n"
      "ds_read_b64_tr_b16 a[234:235], v234 offset:6144\n"
      "v_add_u32 v5, s41, v7\n"
      "v_mfma_f32_16x16x32_bf16 v[36:39], a[216:219], a[8:11], v[36:39]\n"
      "ds_read_b64_tr_b16 a[236:237], v234 offset:12288\n"
      "v_add_u32 v8, s40, v12\n"
      "v_mfma_f32_16x16x32_bf16 v[36:39], a[220:223], a[12:15], v[36:39]\n"
      "s_mov_b32 m0, s46\n"
      "ds_read_b64_tr_b16 a[238:239], v234 offset:18432\n"
      "v_add_u32 v9, s40, v13\n"
      "v_mfma_f32_16x16x32_bf16 v[36:39], a[224:227], a[16:19], v[36:39]\n"
      "buffer_load_dwordx4 v1, s[20:23], s36 offen offset:0 lds\n"
      "v_mfma_f32_16x16x32_bf16 v[36:39], a[228:231], a[20:23], v[36:39]\n"
      "ds_read_b64_tr_b16 a[240:241], v235 offset:0\n"
      "v_add_u32 v10, s40, v14\n"
      "v_mfma_f32_16x16x32_bf16 v[40:43], a[228:231], a[44:47], 0\n"
      "ds_read_b64_tr_b16 a[242:243], v235 offset:6144\n"
      "v_add_u32 v11, s40, v15\n"
      "v_mfma_f32_16x16x32_bf16 v[40:43], a[224:227], a[40:43], v[40:43]\n"
      "s_mov_b32 s43, s40\n"
      "ds_read_b64_tr_b16 a[244:245], v235 offset:12288\n"
      "v_mfma_f32_16x16x32_bf16 v[40:43], a[220:223], a[36:39], v[40:43]\n"
      "s_mov_b32 s40, s41\n"
      "ds_read_b64_tr_b16 a[246:247], v235 offset:18432\n"
      "v_fma_f32 v36, v36, s48, v18\n"
      "v_mfma_f32_16x16x32_bf16 v[40:43], a[216:219], a[32:35], v[40:43]\n"
      "s_mov_b32 s41, s42\n"
      "ds_read_b64_tr_b16 a[248:249], v236 offset:0\n"
      "v_fma_f32 v37, v37, s48, v18\n"
      "v_mfma_f32_16x16x32_bf16 v[40:43], a[212:215], a[28:31], v[40:43]\n"
      "s_mov_b32 s42, s43\n"
      "ds_read_b64_tr_b16 a[250:251], v236 offset:6144\n"
      "v_fma_f32 v38, v38, s48, v18\n"
      "v_mfma_f32_16x16x32_bf16 v[40:43], a[208:211], a[24:27], v[40:43]\n"
      "ds_read_b64_tr_b16 a[252:253], v236 offset:12288\n"
      "v_fma_f32 v39, v39, s48, v18\n"
      "v_mfma_f32_16x16x32_bf16 v[44:47], a[208:211], a[48:51], 0\n"
      "ds_read_b64_tr_b16 a[254:255], v236 offset:18432\n"
      "v_mfma_f32_16x16x32_bf16 v[44:47], a[212:215], a[52:55], v[44:47]\n"
      "buffer_load_dwordx4 v2, s[20:23], s36 offen offset:1024 lds\n"
      "v_mfma_f32_16x16x32_bf16 v[44:47], a[216:219], a[56:59], v[44:47]\n"
      "buffer_load_dwordx4 v3, s[20:23], s36 offen offset:2048 lds\n"
      "v_mfma_f32_16x16x32_bf16 v[44:47], a[220:223], a[60:63], v[44:47]\n"
      "v_exp_f32 v28, v36\n"
      "v_mfma_f32_16x16x32_bf16 v[44:47], a[224:227], a[64:67], v[44:47]\n"
      "v_exp_f32 v29, v37\n"
      "v_mfma_f32_16x16x32_bf16 v[44:47], a[228:231], a[68:71], v[44:47]\n"
      "s_waitcnt lgkmcnt(0)\n"
      "v_exp_f32 v30, v38\n"
      "v_smfmac_f32_16x16x64_bf16 a[192:195], v[24:27], v[160:167], v16\n"
      "ds_read_b128 a[208:211], v232 offset:6144\n"
      "v_cvt_pk_bf16_f32 v188, v28, v29\n"
      "v_mfma_f32_16x16x32_bf16 v[64:67], v[164:167], a[236:239], v[64:67]\n"
      "v_exp_f32 v31, v39\n"
      "v_mfma_f32_16x16x32_bf16 v[64:67], v[160:163], a[232:235], v[64:67]\n"
      "ds_read_b128 a[212:215], v233 offset:6144\n"
      "v_cvt_pk_bf16_f32 v189, v30, v31\n"
      "v_mfma_f32_16x16x32_bf16 v[112:115], v[168:171], a[232:235], v[112:115]\n"
      "v_fma_f32 v40, v40, s48, v19\n"
      "v_fma_f32 v41, v41, s48, v19\n"
      "v_mfma_f32_16x16x32_bf16 v[112:115], v[172:175], a[236:239], v[112:115]\n"
      "ds_read_b128 a[216:219], v232 offset:6272\n"
      "v_fma_f32 v42, v42, s48, v19\n"
      "v_mfma_f32_16x16x32_bf16 a[96:99], v[180:183], a[236:239], a[96:99]\n"
      "v_exp_f32 v32, v40\n"
      "v_mfma_f32_16x16x32_bf16 a[96:99], v[176:179], a[232:235], a[96:99]\n"
      "ds_read_b128 a[220:223], v233 offset:6272\n"
      "v_fma_f32 v43, v43, s48, v19\n"
      "v_mfma_f32_16x16x32_bf16 v[68:71], v[164:167], a[244:247], v[68:71]\n"
      "ds_read_b64_tr_b16 a[232:233], v237 offset:0\n"
      "v_fma_f32 v44, v44, s48, v20\n"
      "v_mfma_f32_16x16x32_bf16 v[68:71], v[160:163], a[240:243], v[68:71]\n"
      "ds_read_b128 a[224:227], v232 offset:6400\n"
      "v_fma_f32 v45, v45, s48, v20\n"
      "v_mfma_f32_16x16x32_bf16 v[116:119], v[168:171], a[240:243], v[116:119]\n"
      "ds_read_b64_tr_b16 a[234:235], v237 offset:6144\n"
      "v_fma_f32 v46, v46, s48, v20\n"
      "v_mfma_f32_16x16x32_bf16 v[116:119], v[172:175], a[244:247], v[116:119]\n"
      "ds_read_b128 a[228:231], v233 offset:6400\n"
      "v_fma_f32 v47, v47, s48, v20\n"
      "v_mfma_f32_16x16x32_bf16 a[100:103], v[180:183], a[244:247], a[100:103]\n"
      "v_exp_f32 v33, v41\n"
      "v_mfma_f32_16x16x32_bf16 a[100:103], v[176:179], a[240:243], a[100:103]\n"
      "ds_read_b64_tr_b16 a[236:237], v237 offset:12288\n"
      "v_cvt_pk_bf16_f32 v196, v32, v33\n"
      "v_mfma_f32_16x16x32_bf16 v[72:75], v[164:167], a[252:255], v[72:75]\n"
      "ds_read_b64_tr_b16 a[238:239], v237 offset:18432\n"
      "v_or3_b32 v22, v188, v189, v196\n"
      "v_mfma_f32_16x16x32_bf16 v[72:75], v[160:163], a[248:251], v[72:75]\n"
      "v_exp_f32 v34, v42\n"
      "v_mfma_f32_16x16x32_bf16 v[120:123], v[168:171], a[248:251], v[120:123]\n"
      "v_exp_f32 v35, v43\n"
      "v_mfma_f32_16x16x32_bf16 v[120:123], v[172:175], a[252:255], v[120:123]\n"
      "ds_read_b64_tr_b16 a[240:241], v234 offset:128\n"
      "v_cvt_pk_bf16_f32 v197, v34, v35\n"
      "v_mfma_f32_16x16x32_bf16 a[104:107], v[180:183], a[252:255], a[104:107]\n"
      "v_exp_f32 v28, v44\n"
      "v_mfma_f32_16x16x32_bf16 a[104:107], v[176:179], a[248:251], a[104:107]\n"
      "s_waitcnt lgkmcnt(3)\n"
      "v_exp_f32 v29, v45\n"
      "v_mfma_f32_16x16x32_bf16 v[216:219], a[208:211], a[0:3], 0\n"
      "ds_read_b64_tr_b16 a[242:243], v234 offset:6272\n"
      "v_cvt_pk_bf16_f32 v204, v28, v29\n"
      "v_mfma_f32_16x16x32_bf16 v[216:219], a[212:215], a[4:7], v[216:219]\n"
      "ds_read_b64_tr_b16 a[244:245], v234 offset:12416\n"
      "v_or3_b32 v22, v22, v197, v204\n"
      "v_mfma_f32_16x16x32_bf16 v[216:219], a[216:219], a[8:11], v[216:219]\n"
      "v_exp_f32 v30, v46\n"
      "v_mfma_f32_16x16x32_bf16 v[216:219], a[220:223], a[12:15], v[216:219]\n"
      "s_add_u32 m0, s46, 3072\n"
      "v_exp_f32 v31, v47\n"
      "v_mfma_f32_16x16x32_bf16 v[216:219], a[224:227], a[16:19], v[216:219]\n"
      "buffer_load_dwordx4 v1, s[20:23], s37 offen offset:0 lds\n"
      "v_mfma_f32_16x16x32_bf16 v[216:219], a[228:231], a[20:23], v[216:219]\n"
      "buffer_load_dwordx4 v2, s[20:23], s37 offen offset:1024 lds\n"
      "v_mfma_f32_16x16x32_bf16 v[220:223], a[228:231], a[44:47], 0\n"
      "buffer_load_dwordx4 v3, s[20:23], s37 offen offset:2048 lds\n"
      "v_mfma_f32_16x16x32_bf16 v[220:223], a[224:227], a[40:43], v[220:223]\n"
      "ds_read_b64_tr_b16 a[246:247], v234 offset:18560\n"
      "v_cvt_pk_bf16_f32 v205, v30, v31\n"
      "v_mfma_f32_16x16x32_bf16 v[220:223], a[220:223], a[36:39], v[220:223]\n"
      "ds_read_b64_tr_b16 a[248:249], v235 offset:128\n"
      "v_fma_f32 v216, v216, s48, v18\n"
      "v_mfma_f32_16x16x32_bf16 v[220:223], a[216:219], a[32:35], v[220:223]\n"
      "ds_read_b64_tr_b16 a[250:251], v235 offset:6272\n"
      "v_fma_f32 v217, v217, s48, v18\n"
      "v_mfma_f32_16x16x32_bf16 v[220:223], a[212:215], a[28:31], v[220:223]\n"
      "ds_read_b64_tr_b16 a[252:253], v235 offset:12416\n"
      "v_fma_f32 v218, v218, s48, v18\n"
      "v_mfma_f32_16x16x32_bf16 v[220:223], a[208:211], a[24:27], v[220:223]\n"
      "ds_read_b64_tr_b16 a[254:255], v235 offset:18560\n"
      "v_fma_f32 v219, v219, s48, v18\n"
      "v_mfma_f32_16x16x32_bf16 v[224:227], a[208:211], a[48:51], 0\n"
      "v_exp_f32 v32, v216\n"
      "v_mfma_f32_16x16x32_bf16 v[224:227], a[212:215], a[52:55], v[224:227]\n"
      "v_exp_f32 v33, v217\n"
      "v_mfma_f32_16x16x32_bf16 v[224:227], a[216:219], a[56:59], v[224:227]\n"
      "v_exp_f32 v34, v218\n"
      "v_mfma_f32_16x16x32_bf16 v[224:227], a[220:223], a[60:63], v[224:227]\n"
      "v_exp_f32 v35, v219\n"
      "v_mfma_f32_16x16x32_bf16 v[224:227], a[224:227], a[64:67], v[224:227]\n"
      "v_cvt_pk_bf16_f32 v190, v32, v33\n"
      "v_or3_b32 v22, v22, v205, v190\n"
      "v_mfma_f32_16x16x32_bf16 v[224:227], a[228:231], a[68:71], v[224:227]\n"
      "s_waitcnt lgkmcnt(0)\n"
      "v_cvt_pk_bf16_f32 v191, v34, v35\n"
      "v_fma_f32 v220, v220, s48, v19\n"
      "v_smfmac_f32_16x16x64_bf16 a[196:199], v[24:27], v[168:175], v16\n"
      "ds_read_b128 a[208:211], v232 offset:12288\n"
      "v_fma_f32 v221, v221, s48, v19\n"
      "v_mfma_f32_16x16x32_bf16 v[76:79], v[164:167], a[236:239], v[76:79]\n"
      "v_exp_f32 v28, v220\n"
      "v_mfma_f32_16x16x32_bf16 v[76:79], v[160:163], a[232:235], v[76:79]\n"
      "ds_read_b128 a[212:215], v233 offset:12288\n"
      "v_fma_f32 v222, v222, s48, v19\n"
      "v_mfma_f32_16x16x32_bf16 v[124:127], v[168:171], a[232:235], v[124:127]\n"
      "v_exp_f32 v29, v221\n"
      "v_mfma_f32_16x16x32_bf16 v[124:127], v[172:175], a[236:239], v[124:127]\n"
      "ds_read_b128 a[216:219], v232 offset:12416\n"
      "v_fma_f32 v223, v223, s48, v19\n"
      "v_mfma_f32_16x16x32_bf16 a[108:111], v[180:183], a[236:239], a[108:111]\n"
      "v_exp_f32 v30, v222\n"
      "v_mfma_f32_16x16x32_bf16 a[108:111], v[176:179], a[232:235], a[108:111]\n"
      "ds_read_b128 a[220:223], v233 offset:12416\n"
      "v_cvt_pk_bf16_f32 v198, v28, v29\n"
      "v_mfma_f32_16x16x32_bf16 v[80:83], v[164:167], a[244:247], v[80:83]\n"
      "v_exp_f32 v31, v223\n"
      "v_mfma_f32_16x16x32_bf16 v[80:83], v[160:163], a[240:243], v[80:83]\n"
      "ds_read_b128 a[224:227], v232 offset:12544\n"
      "v_or3_b32 v22, v22, v191, v198\n"
      "v_mfma_f32_16x16x32_bf16 v[128:131], v[168:171], a[240:243], v[128:131]\n"
      "v_cvt_pk_bf16_f32 v199, v30, v31\n"
      "v_fma_f32 v224, v224, s48, v20\n"
      "v_mfma_f32_16x16x32_bf16 v[128:131], v[172:175], a[244:247], v[128:131]\n"
      "ds_read_b128 a[228:231], v233 offset:12544\n"
      "v_fma_f32 v225, v225, s48, v20\n"
      "v_mfma_f32_16x16x32_bf16 a[112:115], v[180:183], a[244:247], a[112:115]\n"
      "v_exp_f32 v32, v224\n"
      "v_mfma_f32_16x16x32_bf16 a[112:115], v[176:179], a[240:243], a[112:115]\n"
      "v_exp_f32 v33, v225\n"
      "v_mfma_f32_16x16x32_bf16 v[84:87], v[164:167], a[252:255], v[84:87]\n"
      "v_fma_f32 v226, v226, s48, v20\n"
      "v_fma_f32 v227, v227, s48, v20\n"
      "v_mfma_f32_16x16x32_bf16 v[84:87], v[160:163], a[248:251], v[84:87]\n"
      "v_exp_f32 v34, v226\n"
      "v_mfma_f32_16x16x32_bf16 v[132:135], v[168:171], a[248:251], v[132:135]\n"
      "v_exp_f32 v35, v227\n"
      "v_cvt_pk_bf16_f32 v206, v32, v33\n"
      "v_or3_b32 v22, v22, v199, v206\n"
      "v_cvt_pk_bf16_f32 v207, v34, v35\n"
      "v_bitop3_b32 v22, v22, v207, s49 bitop3:0xa8\n"
      "v_cmp_ne_u32 vcc, 0, v22\n"
      "v_mfma_f32_16x16x32_bf16 v[132:135], v[172:175], a[252:255], v[132:135]\n"
      "v_mfma_f32_16x16x32_bf16 a[116:119], v[180:183], a[252:255], a[116:119]\n"
      "v_mfma_f32_16x16x32_bf16 a[116:119], v[176:179], a[248:251], a[116:119]\n"
      "s_waitcnt lgkmcnt(0)\n"
      "s_cbranch_vccnz L_rare_Nf1_1\n"
      "L_rare_Nf1_1_ret:\n"
      "v_mfma_f32_16x16x32_bf16 v[36:39], a[208:211], a[0:3], 0\n"
      "ds_read_b64_tr_b16 a[232:233], v236 offset:128\n"
      "v_mfma_f32_16x16x32_bf16 v[36:39], a[212:215], a[4:7], v[36:39]\n"
      "ds_read_b64_tr_b16 a[234:235], v236 offset:6272\n"
      "v_mfma_f32_16x16x32_bf16 v[36:39], a[216:219], a[8:11], v[36:39]\n"
      "ds_read_b64_tr_b16 a[236:237], v236 offset:12416\n"
      "v_mfma_f32_16x16x32_bf16 v[36:39], a[220:223], a[12:15], v[36:39]\n"
      "s_mov_b32 m0, s47\n"
      "ds_read_b64_tr_b16 a[238:239], v236 offset:18560\n"
      "v_mfma_f32_16x16x32_bf16 v[36:39], a[224:227], a[16:19], v[36:39]\n"
      "buffer_load_dwordx4 v1, s[24:27], s38 offen offset:0 lds\n"
      "v_mfma_f32_16x16x32_bf16 v[36:39], a[228:231], a[20:23], v[36:39]\n"
      "ds_read_b64_tr_b16 a[240:241], v237 offset:128\n"
      "v_mfma_f32_16x16x32_bf16 v[40:43], a[228:231], a[44:47], 0\n"
      "ds_read_b64_tr_b16 a[242:243], v237 offset:6272\n"
      "v_mfma_f32_16x16x32_bf16 v[40:43], a[224:227], a[40:43], v[40:43]\n"
      "s_nop 0\n"
      "ds_read_b64_tr_b16 a[244:245], v237 offset:12416\n"
      "v_mfma_f32_16x16x32_bf16 v[40:43], a[220:223], a[36:39], v[40:43]\n"
      "ds_read_b64_tr_b16 a[246:247], v237 offset:18560\n"
      "v_fma_f32 v36, v36, s48, v18\n"
      "v_mfma_f32_16x16x32_bf16 v[40:43], a[216:219], a[32:35], v[40:43]\n"
      "ds_read_b64_tr_b16 a[248:249], v234 offset:256\n"
      "v_fma_f32 v37, v37, s48, v18\n"
      "v_mfma_f32_16x16x32_bf16 v[40:43], a[212:215], a[28:31], v[40:43]\n"
      "ds_read_b64_tr_b16 a[250:251], v234 offset:6400\n"
      "v_fma_f32 v38, v38, s48, v18\n"
      "v_mfma_f32_16x16x32_bf16 v[40:43], a[208:211], a[24:27], v[40:43]\n"
      "ds_read_b64_tr_b16 a[252:253], v234 offset:12544\n"
      "v_fma_f32 v39, v39, s48, v18\n"
      "v_mfma_f32_16x16x32_bf16 v[44:47], a[208:211], a[48:51], 0\n"
      "ds_read_b64_tr_b16 a[254:255], v234 offset:18688\n"
      "v_mfma_f32_16x16x32_bf16 v[44:47], a[212:215], a[52:55], v[44:47]\n"
      "buffer_load_dwordx4 v2, s[24:27], s38 offen offset:1024 lds\n"
      "v_mfma_f32_16x16x32_bf16 v[44:47], a[216:219], a[56:59], v[44:47]\n"
      "buffer_load_dwordx4 v3, s[24:27], s38 offen offset:2048 lds\n"
      "v_mfma_f32_16x16x32_bf16 v[44:47], a[220:223], a[60:63], v[44:47]\n"
      "v_exp_f32 v28, v36\n"
      "v_mfma_f32_16x16x32_bf16 v[44:47], a[224:227], a[64:67], v[44:47]\n"
      "v_exp_f32 v29, v37\n"
      "v_mfma_f32_16x16x32_bf16 v[44:47], a[228:231], a[68:71], v[44:47]\n"
      "s_waitcnt lgkmcnt(0)\n"
      "v_exp_f32 v30, v38\n"
      "v_smfmac_f32_16x16x64_bf16 a[200:203], v[24:27], v[176:183], v16\n"
      "ds_read_b128 a[208:211], v232 offset:18432\n"
      "v_cvt_pk_bf16_f32 v192, v28, v29\n"
      "v_mfma_f32_16x16x32_bf16 v[88:91], v[164:167], a[236:239], v[88:91]\n"
      "v_exp_f32 v31, v39\n"
      "v_mfma_f32_16x16x32_bf16 v[88:91], v[160:163], a[232:235], v[88:91]\n"
      "ds_read_b128 a[212:215], v233 offset:18432\n"
      "v_cvt_pk_bf16_f32 v193, v30, v31\n"
      "v_mfma_f32_16x16x32_bf16 v[136:139], v[168:171], a[232:235], v[136:139]\n"
      "v_fma_f32 v40, v40, s48, v19\n"
      "v_fma_f32 v41, v41, s48, v19\n"
      "v_mfma_f32_16x16x32_bf16 v[136:139], v[172:175], a[236:239], v[136:139]\n"
      "ds_read_b128 a[216:219], v232 offset:18560\n"
      "v_fma_f32 v42, v42, s48, v19\n"
      "v_mfma_f32_16x16x32_bf16 a[120:123], v[180:183], a[236:239], a[120:123]\n"
      "v_exp_f32 v32, v40\n"
      "v_mfma_f32_16x16x32_bf16 a[120:123], v[176:179], a[232:235], a[120:123]\n"
      "ds_read_b128 a[220:223], v233 offset:18560\n"
      "v_fma_f32 v43, v43, s48, v19\n"
      "v_mfma_f32_16x16x32_bf16 v[92:95], v[164:167], a[244:247], v[92:95]\n"
      "ds_read_b64_tr_b16 a[232:233], v235 offset:256\n"
      "v_fma_f32 v44, v44, s48, v20\n"
      "v_mfma_f32_16x16x32_bf16 v[92:95], v[160:163], a[240:243], v[92:95]\n"
      "ds_read_b128 a[224:227], v232 offset:18688\n"
      "v_fma_f32 v45, v45, s48, v20\n"
      "v_mfma_f32_16x16x32_bf16 v[140:143], v[168:171], a[240:243], v[140:143]\n"
      "ds_read_b64_tr_b16 a[234:235], v235 offset:6400\n"
      "v_fma_f32 v46, v46, s48, v20\n"
      "v_mfma_f32_16x16x32_bf16 v[140:143], v[172:175], a[244:247], v[140:143]\n"
      "ds_read_b128 a[228:231], v233 offset:18688\n"
      "v_fma_f32 v47, v47, s48, v20\n"
      "v_mfma_f32_16x16x32_bf16 a[124:127], v[180:183], a[244:247], a[124:127]\n"
      "v_exp_f32 v33, v41\n"
      "v_mfma_f32_16x16x32_bf16 a[124:127], v[176:179], a[240:243], a[124:127]\n"
      "ds_read_b64_tr_b16 a[236:237], v235 offset:12544\n"
      "v_cvt_pk_bf16_f32 v200, v32, v33\n"
      "v_mfma_f32_16x16x32_bf16 v[96:99], v[164:167], a[252:255], v[96:99]\n"
      "ds_read_b64_tr_b16 a[238:239], v235 offset:18688\n"
      "v_or3_b32 v22, v192, v193, v200\n"
      "v_mfma_f32_16x16x32_bf16 v[96:99], v[160:163], a[248:251], v[96:99]\n"
      "v_exp_f32 v34, v42\n"
      "v_mfma_f32_16x16x32_bf16 v[144:147], v[168:171], a[248:251], v[144:147]\n"
      "v_exp_f32 v35, v43\n"
      "v_mfma_f32_16x16x32_bf16 v[144:147], v[172:175], a[252:255], v[144:147]\n"
      "ds_read_b64_tr_b16 a[240:241], v236 offset:256\n"
      "v_cvt_pk_bf16_f32 v201, v34, v35\n"
      "v_mfma_f32_16x16x32_bf16 a[128:131], v[180:183], a[252:255], a[128:131]\n"
      "v_exp_f32 v28, v44\n"
      "v_mfma_f32_16x16x32_bf16 a[128:131], v[176:179], a[248:251], a[128:131]\n"
      "s_waitcnt lgkmcnt(3)\n"
      "v_exp_f32 v29, v45\n"
      "v_mfma_f32_16x16x32_bf16 v[216:219], a[208:211], a[0:3], 0\n"
      "s_add_u32 s34, s34, 6144\n"
      "ds_read_b64_tr_b16 a[242:243], v236 offset:6400\n"
      "v_cvt_pk_bf16_f32 v208, v28, v29\n"
      "v_mfma_f32_16x16x32_bf16 v[216:219], a[212:215], a[4:7], v[216:219]\n"
      "ds_read_b64_tr_b16 a[244:245], v236 offset:12544\n"
      "v_or3_b32 v22, v22, v201, v208\n"
      "v_mfma_f32_16x16x32_bf16 v[216:219], a[216:219], a[8:11], v[216:219]\n"
      "v_exp_f32 v30, v46\n"
      "v_mfma_f32_16x16x32_bf16 v[216:219], a[220:223], a[12:15], v[216:219]\n"
      "s_add_u32 m0, s47, 3072\n"
      "v_exp_f32 v31, v47\n"
      "v_mfma_f32_16x16x32_bf16 v[216:219], a[224:227], a[16:19], v[216:219]\n"
      "buffer_load_dwordx4 v1, s[24:27], s39 offen offset:0 lds\n"
      "v_mfma_f32_16x16x32_bf16 v[216:219], a[228:231], a[20:23], v[216:219]\n"
      "buffer_load_dwordx4 v2, s[24:27], s39 offen offset:1024 lds\n"
      "v_mfma_f32_16x16x32_bf16 v[220:223], a[228:231], a[44:47], 0\n"
      "buffer_load_dwordx4 v3, s[24:27], s39 offen offset:2048 lds\n"
      "v_mfma_f32_16x16x32_bf16 v[220:223], a[224:227], a[40:43], v[220:223]\n"
      "ds_read_b64_tr_b16 a[246:247], v236 offset:18688\n"
      "v_cvt_pk_bf16_f32 v209, v30, v31\n"
      "v_mfma_f32_16x16x32_bf16 v[220:223], a[220:223], a[36:39], v[220:223]\n"
      "ds_read_b64_tr_b16 a[248:249], v237 offset:256\n"
      "v_fma_f32 v216, v216, s48, v18\n"
      "v_mfma_f32_16x16x32_bf16 v[220:223], a[216:219], a[32:35], v[220:223]\n"
      "ds_read_b64_tr_b16 a[250:251], v237 offset:6400\n"
      "v_fma_f32 v217, v217, s48, v18\n"
      "v_mfma_f32_16x16x32_bf16 v[220:223], a[212:215], a[28:31], v[220:223]\n"
      "ds_read_b64_tr_b16 a[252:253], v237 offset:12544\n"
      "v_fma_f32 v218, v218, s48, v18\n"
      "v_mfma_f32_16x16x32_bf16 v[220:223], a[208:211], a[24:27], v[220:223]\n"
      "s_mov_b32 s38, s36\n"
      "ds_read_b64_tr_b16 a[254:255], v237 offset:18688\n"
      "v_fma_f32 v219, v219, s48, v18\n"
      "v_mfma_f32_16x16x32_bf16 v[224:227], a[208:211], a[48:51], 0\n"
      "s_mov_b32 s39, s37\n"
      "v_exp_f32 v32, v216\n"
      "v_mfma_f32_16x16x32_bf16 v[224:227], a[212:215], a[52:55], v[224:227]\n"
      "s_add_u32 s12, s12, 1\n"
      "v_exp_f32 v33, v217\n"
      "v_mfma_f32_16x16x32_bf16 v[224:227], a[216:219], a[56:59], v[224:227]\n"
      "s_add_u32 s15, s13, s12\n"
      "v_exp_f32 v34, v218\n"
      "v_mfma_f32_16x16x32_bf16 v[224:227], a[220:223], a[60:63], v[224:227]\n"
      "s_and_b32 s15, s15, 15\n"
      "v_exp_f32 v35, v219\n"
      "v_mfma_f32_16x16x32_bf16 v[224:227], a[224:227], a[64:67], v[224:227]\n"
      "s_waitcnt vmcnt(12) lgkmcnt(0)\n"
      "v_cvt_pk_bf16_f32 v194, v32, v33\n"
      "v_or3_b32 v22, v22, v209, v194\n"
      "v_mfma_f32_16x16x32_bf16 v[224:227], a[228:231], a[68:71], v[224:227]\n"
      "s_barrier\n"
      "v_cvt_pk_bf16_f32 v195, v34, v35\n"
      "v_fma_f32 v220, v220, s48, v19\n"
      "v_mfma_f32_16x16x32_bf16 v[100:103], v[164:167], a[236:239], v[100:103]\n"
      "s_mul_i32 s15, s15, 24576\n"
      "ds_read_b128 a[208:211], v4 offset:0\n"
      "v_fma_f32 v221, v221, s48, v19\n"
      "v_mfma_f32_16x16x32_bf16 v[100:103], v[160:163], a[232:235], v[100:103]\n"
      "s_lshr_b32 s16, s12, 4\n"
      "s_cmp_lt_u32 s34, 21504\n"
      "s_cbranch_scc0 L_not_N\n"
      "buffer_load_dword v255, v254, s[96:99], s34 offen\n"
      "L_not_N:\n"
      "v_mfma_f32_16x16x32_bf16 v[148:151], v[168:171], a[232:235], v[148:151]\n"
      "s_mul_i32 s16, s16, 393216\n"
      "ds_read_b128 a[212:215], v5 offset:0\n"
      "v_fma_f32 v222, v222, s48, v19\n"
      "v_mfma_f32_16x16x32_bf16 v[148:151], v[172:175], a[236:239], v[148:151]\n"
      "s_add_u32 s15, s15, s16\n"
      "v_exp_f32 v28, v220\n"
      "v_mfma_f32_16x16x32_bf16 a[132:135], v[180:183], a[236:239], a[132:135]\n"
      "s_add_u32 s36, s15, s14\n"
      "ds_read_b128 a[216:219], v4 offset:128\n"
      "v_fma_f32 v223, v223, s48, v19\n"
      "v_mfma_f32_16x16x32_bf16 a[132:135], v[176:179], a[232:235], a[132:135]\n"
      "s_add_u32 s37, s36, 3072\n"
      "v_exp_f32 v29, v221\n"
      "v_mfma_f32_16x16x32_bf16 v[104:107], v[164:167], a[244:247], v[104:107]\n"
      "ds_read_b128 a[220:223], v5 offset:128\n"
      "v_cvt_pk_bf16_f32 v202, v28, v29\n"
      "v_mfma_f32_16x16x32_bf16 v[104:107], v[160:163], a[240:243], v[104:107]\n"
      "v_exp_f32 v30, v222\n"
      "v_mfma_f32_16x16x32_bf16 v[152:155], v[168:171], a[240:243], v[152:155]\n"
      "ds_read_b128 a[224:227], v4 offset:256\n"
      "v_or3_b32 v22, v22, v195, v202\n"
      "v_mfma_f32_16x16x32_bf16 v[152:155], v[172:175], a[244:247], v[152:155]\n"
      "v_exp_f32 v31, v223\n"
      "v_mfma_f32_16x16x32_bf16 a[136:139], v[180:183], a[244:247], a[136:139]\n"
      "ds_read_b128 a[228:231], v5 offset:256\n"
      "v_cvt_pk_bf16_f32 v203, v30, v31\n"
      "v_mfma_f32_16x16x32_bf16 a[136:139], v[176:179], a[240:243], a[136:139]\n"
      "v_fma_f32 v224, v224, s48, v20\n"
      "v_fma_f32 v225, v225, s48, v20\n"
      "v_mfma_f32_16x16x32_bf16 v[108:111], v[164:167], a[252:255], v[108:111]\n"
      "v_exp_f32 v32, v224\n"
      "v_mfma_f32_16x16x32_bf16 v[108:111], v[160:163], a[248:251], v[108:111]\n"
      "v_fma_f32 v226, v226, s48, v20\n"
      "v_fma_f32 v227, v227, s48, v20\n"
      "v_mfma_f32_16x16x32_bf16 v[156:159], v[168:171], a[248:251], v[156:159]\n"
      "v_exp_f32 v33, v225\n"
      "v_exp_f32 v34, v226\n"
      "v_exp_f32 v35, v227\n"
      "v_cvt_pk_bf16_f32 v210, v32, v33\n"
      "v_or3_b32 v22, v22, v203, v210\n"
      "v_cvt_pk_bf16_f32 v211, v34, v35\n"
      "v_bitop3_b32 v22, v22, v211, s49 bitop3:0xa8\n"
      "v_cmp_ne_u32 vcc, 0, v22\n"
      "v_mfma_f32_16x16x32_bf16 v[156:159], v[172:175], a[252:255], v[156:159]\n"
      "s_cmp_eq_u32 s50, 0\n"
      "v_mfma_f32_16x16x32_bf16 a[140:143], v[180:183], a[252:255], a[140:143]\n"
      "v_mfma_f32_16x16x32_bf16 a[140:143], v[176:179], a[248:251], a[140:143]\n"
      "s_waitcnt lgkmcnt(0)\n"
      "s_cbranch_vccnz L_rare_Nf1_3\n"
      "L_rare_Nf1_3_ret:\n"
      "s_cbranch_scc1 L_last_N\n"
      "L_mid_N:\n"
      "v_mfma_f32_16x16x32_bf16 v[36:39], a[208:211], a[0:3], 0\n"
      "s_add_u32 s46, s44, s42\n"
      "ds_read_b64_tr_b16 a[232:233], v8 offset:0\n"
      "v_add_u32 v232, s41, v6\n"
      "v_mfma_f32_16x16x32_bf16 v[36:39], a[212:215], a[4:7], v[36:39]\n"
      "s_add_u32 s47, s45, s41\n"
      "ds_read_b64_tr_b16 a[234:235], v8 offset:6144\n"
      "v_add_u32 v233, s41, v7\n"
      "v_mfma_f32_16x16x32_bf16 v[36:39], a[216:219], a[8:11], v[36:39]\n"
      "ds_read_b64_tr_b16 a[236:237], v8 offset:12288\n"
      "v_add_u32 v234, s40, v12\n"
      "v_mfma_f32_16x16x32_bf16 v[36:39], a[220:223], a[12:15], v[36:39]\n"
      "s_mov_b32 m0, s46\n"
      "ds_read_b64_tr_b16 a[238:239], v8 offset:18432\n"
      "v_add_u32 v235, s40, v13\n"
      "v_mfma_f32_16x16x32_bf16 v[36:39], a[224:227], a[16:19], v[36:39]\n"
      "buffer_load_dwordx4 v1, s[20:23], s36 offen offset:0 lds\n"
      "v_mfma_f32_16x16x32_bf16 v[36:39], a[228:231], a[20:23], v[36:39]\n"
      "ds_read_b64_tr_b16 a[240:241], v9 offset:0\n"
      "v_add_u32 v236, s40, v14\n"
      "v_mfma_f32_16x16x32_bf16 v[40:43], a[228:231], a[44:47], 0\n"
      "ds_read_b64_tr_b16 a[242:243], v9 offset:6144\n"
      "v_add_u32 v237, s40, v15\n"
      "v_mfma_f32_16x16x32_bf16 v[40:43], a[224:227], a[40:43], v[40:43]\n"
      "s_mov_b32 s43, s40\n"
      "ds_read_b64_tr_b16 a[244:245], v9 offset:12288\n"
      "v_mfma_f32_16x16x32_bf16 v[40:43], a[220:223], a[36:39], v[40:43]\n"
      "s_mov_b32 s40, s41\n"
      "ds_read_b64_tr_b16 a[246:247], v9 offset:18432\n"
      "v_fma_f32 v36, v36, s48, v18\n"
      "v_mfma_f32_16x16x32_bf16 v[40:43], a[216:219], a[32:35], v[40:43]\n"
      "s_mov_b32 s41, s42\n"
      "ds_read_b64_tr_b16 a[248:249], v10 offset:0\n"
      "v_fma_f32 v37, v37, s48, v18\n"
      "v_mfma_f32_16x16x32_bf16 v[40:43], a[212:215], a[28:31], v[40:43]\n"
      "s_mov_b32 s42, s43\n"
      "ds_read_b64_tr_b16 a[250:251], v10 offset:6144\n"
      "v_fma_f32 v38, v38, s48, v18\n"
      "v_mfma_f32_16x16x32_bf16 v[40:43], a[208:211], a[24:27], v[40:43]\n"
      "ds_read_b64_tr_b16 a[252:253], v10 offset:12288\n"
      "v_fma_f32 v39, v39, s48, v18\n"
      "v_mfma_f32_16x16x32_bf16 v[44:47], a[208:211], a[48:51], 0\n"
      "ds_read_b64_tr_b16 a[254:255], v10 offset:18432\n"
      "v_mfma_f32_16x16x32_bf16 v[44:47], a[212:215], a[52:55], v[44:47]\n"
      "buffer_load_dwordx4 v2, s[20:23], s36 offen offset:1024 lds\n"
      "v_mfma_f32_16x16x32_bf16 v[44:47], a[216:219], a[56:59], v[44:47]\n"
      "buffer_load_dwordx4 v3, s[20:23], s36 offen offset:2048 lds\n"
      "v_mfma_f32_16x16x32_bf16 v[44:47], a[220:223], a[60:63], v[44:47]\n"
      "v_exp_f32 v28, v36\n"
      "v_mfma_f32_16x16x32_bf16 v[44:47], a[224:227], a[64:67], v[44:47]\n"
      "v_exp_f32 v29, v37\n"
      "v_mfma_f32_16x16x32_bf16 v[44:47], a[228:231], a[68:71], v[44:47]\n"
      "s_waitcnt lgkmcnt(0)\n"
      "v_exp_f32 v30, v38\n"
      "v_smfmac_f32_16x16x64_bf16 a[192:195], v[24:27], v[188:195], v16\n"
      "ds_read_b128 a[208:211], v4 offset:6144\n"
      "v_cvt_pk_bf16_f32 v160, v28, v29\n"
      "v_mfma_f32_16x16x32_bf16 v[64:67], v[192:195], a[236:239], v[64:67]\n"
      "v_exp_f32 v31, v39\n"
      "v_mfma_f32_16x16x32_bf16 v[64:67], v[188:191], a[232:235], v[64:67]\n"
      "ds_read_b128 a[212:215], v5 offset:6144\n"
      "v_cvt_pk_bf16_f32 v161, v30, v31\n"
      "v_mfma_f32_16x16x32_bf16 v[112:115], v[196:199], a[232:235], v[112:115]\n"
      "v_fma_f32 v40, v40, s48, v19\n"
      "v_fma_f32 v41, v41, s48, v19\n"
      "v_mfma_f32_16x16x32_bf16 v[112:115], v[200:203], a[236:239], v[112:115]\n"
      "ds_read_b128 a[216:219], v4 offset:6272\n"
      "v_fma_f32 v42, v42, s48, v19\n"
      "v_mfma_f32_16x16x32_bf16 a[96:99], v[208:211], a[236:239], a[96:99]\n"
      "v_exp_f32 v32, v40\n"
      "v_mfma_f32_16x16x32_bf16 a[96:99], v[204:207], a[232:235], a[96:99]\n"
      "ds_read_b128 a[220:223], v5 offset:6272\n"
      "v_fma_f32 v43, v43, s48, v19\n"
      "v_mfma_f32_16x16x32_bf16 v[68:71], v[192:195], a[244:247], v[68:71]\n"
      "ds_read_b64_tr_b16 a[232:233], v11 offset:0\n"
      "v_fma_f32 v44, v44, s48, v20\n"
      "v_mfma_f32_16x16x32_bf16 v[68:71], v[188:191], a[240:243], v[68:71]\n"
      "ds_read_b128 a[224:227], v4 offset:6400\n"
      "v_fma_f32 v45, v45, s48, v20\n"
      "v_mfma_f32_16x16x32_bf16 v[116:119], v[196:199], a[240:243], v[116:119]\n"
      "ds_read_b64_tr_b16 a[234:235], v11 offset:6144\n"
      "v_fma_f32 v46, v46, s48, v20\n"
      "v_mfma_f32_16x16x32_bf16 v[116:119], v[200:203], a[244:247], v[116:119]\n"
      "ds_read_b128 a[228:231], v5 offset:6400\n"
      "v_fma_f32 v47, v47, s48, v20\n"
      "v_mfma_f32_16x16x32_bf16 a[100:103], v[208:211], a[244:247], a[100:103]\n"
      "v_exp_f32 v33, v41\n"
      "v_mfma_f32_16x16x32_bf16 a[100:103], v[204:207], a[240:243], a[100:103]\n"
      "ds_read_b64_tr_b16 a[236:237], v11 offset:12288\n"
      "v_cvt_pk_bf16_f32 v168, v32, v33\n"
      "v_mfma_f32_16x16x32_bf16 v[72:75], v[192:195], a[252:255], v[72:75]\n"
      "ds_read_b64_tr_b16 a[238:239], v11 offset:18432\n"
      "v_or3_b32 v22, v160, v161, v168\n"
      "v_mfma_f32_16x16x32_bf16 v[72:75], v[188:191], a[248:251], v[72:75]\n"
      "v_exp_f32 v34, v42\n"
      "v_mfma_f32_16x16x32_bf16 v[120:123], v[196:199], a[248:251], v[120:123]\n"
      "v_exp_f32 v35, v43\n"
      "v_mfma_f32_16x16x32_bf16 v[120:123], v[200:203], a[252:255], v[120:123]\n"
      "ds_read_b64_tr_b16 a[240:241], v8 offset:128\n"
      "v_cvt_pk_bf16_f32 v169, v34, v35\n"
      "v_mfma_f32_16x16x32_bf16 a[104:107], v[208:211], a[252:255], a[104:107]\n"
      "v_exp_f32 v28, v44\n"
      "v_mfma_f32_16x16x32_bf16 a[104:107], v[204:207], a[248:251], a[104:107]\n"
      "s_waitcnt lgkmcnt(3)\n"
      "v_exp_f32 v29, v45\n"
      "v_mfma_f32_16x16x32_bf16 v[216:219], a[208:211], a[0:3], 0\n"
      "ds_read_b64_tr_b16 a[242:243], v8 offset:6272\n"
      "v_cvt_pk_bf16_f32 v176, v28, v29\n"
      "v_mfma_f32_16x16x32_bf16 v[216:219], a[212:215], a[4:7], v[216:219]\n"
      "ds_read_b64_tr_b16 a[244:245], v8 offset:12416\n"
      "v_or3_b32 v22, v22, v169, v176\n"
      "v_mfma_f32_16x16x32_bf16 v[216:219], a[216:219], a[8:11], v[216:219]\n"
      "v_exp_f32 v30, v46\n"
      "v_mfma_f32_16x16x32_bf16 v[216:219], a[220:223], a[12:15], v[216:219]\n"
      "s_add_u32 m0, s46, 3072\n"
      "v_exp_f32 v31, v47\n"
      "v_mfma_f32_16x16x32_bf16 v[216:219], a[224:227], a[16:19], v[216:219]\n"
      "buffer_load_dwordx4 v1, s[20:23], s37 offen offset:0 lds\n"
      "v_mfma_f32_16x16x32_bf16 v[216:219], a[228:231], a[20:23], v[216:219]\n"
      "buffer_load_dwordx4 v2, s[20:23], s37 offen offset:1024 lds\n"
      "v_mfma_f32_16x16x32_bf16 v[220:223], a[228:231], a[44:47], 0\n"
      "buffer_load_dwordx4 v3, s[20:23], s37 offen offset:2048 lds\n"
      "v_mfma_f32_16x16x32_bf16 v[220:223], a[224:227], a[40:43], v[220:223]\n"
      "ds_read_b64_tr_b16 a[246:247], v8 offset:18560\n"
      "v_cvt_pk_bf16_f32 v177, v30, v31\n"
      "v_mfma_f32_16x16x32_bf16 v[220:223], a[220:223], a[36:39], v[220:223]\n"
      "ds_read_b64_tr_b16 a[248:249], v9 offset:128\n"
      "v_fma_f32 v216, v216, s48, v18\n"
      "v_mfma_f32_16x16x32_bf16 v[220:223], a[216:219], a[32:35], v[220:223]\n"
      "ds_read_b64_tr_b16 a[250:251], v9 offset:6272\n"
      "v_fma_f32 v217, v217, s48, v18\n"
      "v_mfma_f32_16x16x32_bf16 v[220:223], a[212:215], a[28:31], v[220:223]\n"
      "ds_read_b64_tr_b16 a[252:253], v9 offset:12416\n"
      "v_fma_f32 v218, v218, s48, v18\n"
      "v_mfma_f32_16x16x32_bf16 v[220:223], a[208:211], a[24:27], v[220:223]\n"
      "ds_read_b64_tr_b16 a[254:255], v9 offset:18560\n"
      "v_fma_f32 v219, v219, s48, v18\n"
      "v_mfma_f32_16x16x32_bf16 v[224:227], a[208:211], a[48:51], 0\n"
      "v_exp_f32 v32, v216\n"
      "v_mfma_f32_16x16x32_bf16 v[224:227], a[212:215], a[52:55], v[224:227]\n"
      "v_exp_f32 v33, v217\n"
      "v_mfma_f32_16x16x32_bf16 v[224:227], a[216:219], a[56:59], v[224:227]\n"
      "v_exp_f32 v34, v218\n"
      "v_mfma_f32_16x16x32_bf16 v[224:227], a[220:223], a[60:63], v[224:227]\n"
      "v_exp_f32 v35, v219\n"
      "v_mfma_f32_16x16x32_bf16 v[224:227], a[224:227], a[64:67], v[224:227]\n"
      "v_cvt_pk_bf16_f32 v162, v32, v33\n"
      "v_or3_b32 v22, v22, v177, v162\n"
      "v_mfma_f32_16x16x32_bf16 v[224:227], a[228:231], a[68:71], v[224:227]\n"
      "s_waitcnt lgkmcnt(0)\n"
      "v_cvt_pk_bf16_f32 v163, v34, v35\n"
      "v_fma_f32 v220, v220, s48, v19\n"
      "v_smfmac_f32_16x16x64_bf16 a[196:199], v[24:27], v[196:203], v16\n"
      "ds_read_b128 a[208:211], v4 offset:12288\n"
      "v_fma_f32 v221, v221, s48, v19\n"
      "v_mfma_f32_16x16x32_bf16 v[76:79], v[192:195], a[236:239], v[76:79]\n"
      "v_exp_f32 v28, v220\n"
      "v_mfma_f32_16x16x32_bf16 v[76:79], v[188:191], a[232:235], v[76:79]\n"
      "ds_read_b128 a[212:215], v5 offset:12288\n"
      "v_fma_f32 v222, v222, s48, v19\n"
      "v_mfma_f32_16x16x32_bf16 v[124:127], v[196:199], a[232:235], v[124:127]\n"
      "v_exp_f32 v29, v221\n"
      "v_mfma_f32_16x16x32_bf16 v[124:127], v[200:203], a[236:239], v[124:127]\n"
      "ds_read_b128 a[216:219], v4 offset:12416\n"
      "v_fma_f32 v223, v223, s48, v19\n"
      "v_mfma_f32_16x16x32_bf16 a[108:111], v[208:211], a[236:239], a[108:111]\n"
      "v_exp_f32 v30, v222\n"
      "v_mfma_f32_16x16x32_bf16 a[108:111], v[204:207], a[232:235], a[108:111]\n"
      "ds_read_b128 a[220:223], v5 offset:12416\n"
      "v_cvt_pk_bf16_f32 v170, v28, v29\n"
      "v_mfma_f32_16x16x32_bf16 v[80:83], v[192:195], a[244:247], v[80:83]\n"
      "v_exp_f32 v31, v223\n"
      "v_mfma_f32_16x16x32_bf16 v[80:83], v[188:191], a[240:243], v[80:83]\n"
      "ds_read_b128 a[224:227], v4 offset:12544\n"
      "v_or3_b32 v22, v22, v163, v170\n"
      "v_mfma_f32_16x16x32_bf16 v[128:131], v[196:199], a[240:243], v[128:131]\n"
      "v_cvt_pk_bf16_f32 v171, v30, v31\n"
      "v_fma_f32 v224, v224, s48, v20\n"
      "v_mfma_f32_16x16x32_bf16 v[128:131], v[200:203], a[244:247], v[128:131]\n"
      "ds_read_b128 a[228:231], v5 offset:12544\n"
      "v_fma_f32 v225, v225, s48, v20\n"
      "v_mfma_f32_16x16x32_bf16 a[112:115], v[208:211], a[244:247], a[112:115]\n"
      "v_exp_f32 v32, v224\n"
      "v_mfma_f32_16x16x32_bf16 a[112:115], v[204:207], a[240:243], a[112:115]\n"
      "v_exp_f32 v33, v225\n"
      "v_mfma_f32_16x16x32_bf16 v[84:87], v[192:195], a[252:255], v[84:87]\n"
      "v_fma_f32 v226, v226, s48, v20\n"
      "v_fma_f32 v227, v227, s48, v20\n"
      "v_mfma_f32_16x16x32_bf16 v[84:87], v[188:191], a[248:251], v[84:87]\n"
      "v_exp_f32 v34, v226\n"
      "v_mfma_f32_16x16x32_bf16 v[132:135], v[196:199], a[248:251], v[132:135]\n"
      "v_exp_f32 v35, v227\n"
      "v_cvt_pk_bf16_f32 v178, v32, v33\n"
      "v_or3_b32 v22, v22, v171, v178\n"
      "v_cvt_pk_bf16_f32 v179, v34, v35\n"
      "v_bitop3_b32 v22, v22, v179, s49 bitop3:0xa8\n"
      "v_cmp_ne_u32 vcc, 0, v22\n"
      "v_mfma_f32_16x16x32_bf16 v[132:135], v[200:203], a[252:255], v[132:135]\n"
      "v_mfma_f32_16x16x32_bf16 a[116:119], v[208:211], a[252:255], a[116:119]\n"
      "v_mfma_f32_16x16x32_bf16 a[116:119], v[204:207], a[248:251], a[116:119]\n"
      "s_waitcnt lgkmcnt(0)\n"
      "s_cbranch_vccnz L_rare_Nf0_1\n"
      "L_rare_Nf0_1_ret:\n"
      "v_mfma_f32_16x16x32_bf16 v[36:39], a[208:211], a[0:3], 0\n"
      "ds_read_b64_tr_b16 a[232:233], v10 offset:128\n"
      "v_mfma_f32_16x16x32_bf16 v[36:39], a[212:215], a[4:7], v[36:39]\n"
      "ds_read_b64_tr_b16 a[234:235], v10 offset:6272\n"
      "v_mfma_f32_16x16x32_bf16 v[36:39], a[216:219], a[8:11], v[36:39]\n"
      "ds_read_b64_tr_b16 a[236:237], v10 offset:12416\n"
      "v_mfma_f32_16x16x32_bf16 v[36:39], a[220:223], a[12:15], v[36:39]\n"
      "s_mov_b32 m0, s47\n"
      "ds_read_b64_tr_b16 a[238:239], v10 offset:18560\n"
      "v_mfma_f32_16x16x32_bf16 v[36:39], a[224:227], a[16:19], v[36:39]\n"
      "buffer_load_dwordx4 v1, s[24:27], s38 offen offset:0 lds\n"
      "v_mfma_f32_16x16x32_bf16 v[36:39], a[228:231], a[20:23], v[36:39]\n"
      "ds_read_b64_tr_b16 a[240:241], v11 offset:128\n"
      "v_mfma_f32_16x16x32_bf16 v[40:43], a[228:231], a[44:47], 0\n"
      "ds_read_b64_tr_b16 a[242:243], v11 offset:6272\n"
      "v_mfma_f32_16x16x32_bf16 v[40:43], a[224:227], a[40:43], v[40:43]\n"
      "s_nop 0\n"
      "ds_read_b64_tr_b16 a[244:245], v11 offset:12416\n"
      "v_mfma_f32_16x16x32_bf16 v[40:43], a[220:223], a[36:39], v[40:43]\n"
      "ds_read_b64_tr_b16 a[246:247], v11 offset:18560\n"
      "v_fma_f32 v36, v36, s48, v18\n"
      "v_mfma_f32_16x16x32_bf16 v[40:43], a[216:219], a[32:35], v[40:43]\n"
      "ds_read_b64_tr_b16 a[248:249], v8 offset:256\n"
      "v_fma_f32 v37, v37, s48, v18\n"
      "v_mfma_f32_16x16x32_bf16 v[40:43], a[212:215], a[28:31], v[40:43]\n"
      "ds_read_b64_tr_b16 a[250:251], v8 offset:6400\n"
      "v_fma_f32 v38, v38, s48, v18\n"
      "v_mfma_f32_16x16x32_bf16 v[40:43], a[208:211], a[24:27], v[40:43]\n"
      "ds_read_b64_tr_b16 a[252:253], v8 offset:12544\n"
      "v_fma_f32 v39, v39, s48, v18\n"
      "v_mfma_f32_16x16x32_bf16 v[44:47], a[208:211], a[48:51], 0\n"
      "ds_read_b64_tr_b16 a[254:255], v8 offset:18688\n"
      "v_mfma_f32_16x16x32_bf16 v[44:47], a[212:215], a[52:55], v[44:47]\n"
      "buffer_load_dwordx4 v2, s[24:27], s38 offen offset:1024 lds\n"
      "v_mfma_f32_16x16x32_bf16 v[44:47], a[216:219], a[56:59], v[44:47]\n"
      "buffer_load_dwordx4 v3, s[24:27], s38 offen offset:2048 lds\n"
      "v_mfma_f32_16x16x32_bf16 v[44:47], a[220:223], a[60:63], v[44:47]\n"
      "v_exp_f32 v28, v36\n"
      "v_mfma_f32_16x16x32_bf16 v[44:47], a[224:227], a[64:67], v[44:47]\n"
      "v_exp_f32 v29, v37\n"
      "v_mfma_f32_16x16x32_bf16 v[44:47], a[228:231], a[68:71], v[44:47]\n"
      "s_waitcnt lgkmcnt(0)\n"
      "v_exp_f32 v30, v38\n"
      "v_smfmac_f32_16x16x64_bf16 a[200:203], v[24:27], v[204:211], v16\n"
      "ds_read_b128 a[208:211], v4 offset:18432\n"
      "v_cvt_pk_bf16_f32 v164, v28, v29\n"
      "v_mfma_f32_16x16x32_bf16 v[88:91], v[192:195], a[236:239], v[88:91]\n"
      "v_exp_f32 v31, v39\n"
      "v_mfma_f32_16x16x32_bf16 v[88:91], v[188:191], a[232:235], v[88:91]\n"
      "ds_read_b128 a[212:215], v5 offset:18432\n"
      "v_cvt_pk_bf16_f32 v165, v30, v31\n"
      "v_mfma_f32_16x16x32_bf16 v[136:139], v[196:199], a[232:235], v[136:139]\n"
      "v_fma_f32 v40, v40, s48, v19\n"
      "v_fma_f32 v41, v41, s48, v19\n"
      "v_mfma_f32_16x16x32_bf16 v[136:139], v[200:203], a[236:239], v[136:139]\n"
      "ds_read_b128 a[216:219], v4 offset:18560\n"
      "v_fma_f32 v42, v42, s48, v19\n"
      "v_mfma_f32_16x16x32_bf16 a[120:123], v[208:211], a[236:239], a[120:123]\n"
      "v_exp_f32 v32, v40\n"
      "v_mfma_f32_16x16x32_bf16 a[120:123], v[204:207], a[232:235], a[120:123]\n"
      "ds_read_b128 a[220:223], v5 offset:18560\n"
      "v_fma_f32 v43, v43, s48, v19\n"
      "v_mfma_f32_16x16x32_bf16 v[92:95], v[192:195], a[244:247], v[92:95]\n"
      "ds_read_b64_tr_b16 a[232:233], v9 offset:256\n"
      "v_fma_f32 v44, v44, s48, v20\n"
      "v_mfma_f32_16x16x32_bf16 v[92:95], v[188:191], a[240:243], v[92:95]\n"
      "ds_read_b128 a[224:227], v4 offset:18688\n"
      "v_fma_f32 v45, v45, s48, v20\n"
      "v_mfma_f32_16x16x32_bf16 v[140:143], v[196:199], a[240:243], v[140:143]\n"
      "ds_read_b64_tr_b16 a[234:235], v9 offset:6400\n"
      "v_fma_f32 v46, v46, s48, v20\n"
      "v_mfma_f32_16x16x32_bf16 v[140:143], v[200:203], a[244:247], v[140:143]\n"
      "ds_read_b128 a[228:231], v5 offset:18688\n"
      "v_fma_f32 v47, v47, s48, v20\n"
      "v_mfma_f32_16x16x32_bf16 a[124:127], v[208:211], a[244:247], a[124:127]\n"
      "v_exp_f32 v33, v41\n"
      "v_mfma_f32_16x16x32_bf16 a[124:127], v[204:207], a[240:243], a[124:127]\n"
      "ds_read_b64_tr_b16 a[236:237], v9 offset:12544\n"
      "v_cvt_pk_bf16_f32 v172, v32, v33\n"
      "v_mfma_f32_16x16x32_bf16 v[96:99], v[192:195], a[252:255], v[96:99]\n"
      "ds_read_b64_tr_b16 a[238:239], v9 offset:18688\n"
      "v_or3_b32 v22, v164, v165, v172\n"
      "v_mfma_f32_16x16x32_bf16 v[96:99], v[188:191], a[248:251], v[96:99]\n"
      "v_exp_f32 v34, v42\n"
      "v_mfma_f32_16x16x32_bf16 v[144:147], v[196:199], a[248:251], v[144:147]\n"
      "v_exp_f32 v35, v43\n"
      "v_mfma_f32_16x16x32_bf16 v[144:147], v[200:203], a[252:255], v[144:147]\n"
      "ds_read_b64_tr_b16 a[240:241], v10 offset:256\n"
      "v_cvt_pk_bf16_f32 v173, v34, v35\n"
      "v_mfma_f32_16x16x32_bf16 a[128:131], v[208:211], a[252:255], a[128:131]\n"
      "v_exp_f32 v28, v44\n"
      "v_mfma_f32_16x16x32_bf16 a[128:131], v[204:207], a[248:251], a[128:131]\n"
      "s_waitcnt lgkmcnt(3)\n"
      "v_exp_f32 v29, v45\n"
      "v_mfma_f32_16x16x32_bf16 v[216:219], a[208:211], a[0:3], 0\n"
      "ds_read_b64_tr_b16 a[242:243], v10 offset:6400\n"
      "v_cvt_pk_bf16_f32 v180, v28, v29\n"
      "v_mfma_f32_16x16x32_bf16 v[216:219], a[212:215], a[4:7], v[216:219]\n"
      "ds_read_b64_tr_b16 a[244:245], v10 offset:12544\n"
      "v_or3_b32 v22, v22, v173, v180\n"
      "v_mfma_f32_16x16x32_bf16 v[216:219], a[216:219], a[8:11], v[216:219]\n"
      "v_exp_f32 v30, v46\n"
      "v_mfma_f32_16x16x32_bf16 v[216:219], a[220:223], a[12:15], v[216:219]\n"
      "s_add_u32 m0, s47, 3072\n"
      "v_exp_f32 v31, v47\n"
      "v_mfma_f32_16x16x32_bf16 v[216:219], a[224:227], a[16:19], v[216:219]\n"
      "buffer_load_dwordx4 v1, s[24:27], s39 offen offset:0 lds\n"
      "v_mfma_f32_16x16x32_bf16 v[216:219], a[228:231], a[20:23], v[216:219]\n"
      "buffer_load_dwordx4 v2, s[24:27], s39 offen offset:1024 lds\n"
      "v_mfma_f32_16x16x32_bf16 v[220:223], a[228:231], a[44:47], 0\n"
      "buffer_load_dwordx4 v3, s[24:27], s39 offen offset:2048 lds\n"
      "v_mfma_f32_16x16x32_bf16 v[220:223], a[224:227], a[40:43], v[220:223]\n"
      "ds_read_b64_tr_b16 a[246:247], v10 offset:18688\n"
      "v_cvt_pk_bf16_f32 v181, v30, v31\n"
      "v_mfma_f32_16x16x32_bf16 v[220:223], a[220:223], a[36:39], v[220:223]\n"
      "ds_read_b64_tr_b16 a[248:249], v11 offset:256\n"
      "v_fma_f32 v216, v216, s48, v18\n"
      "v_mfma_f32_16x16x32_bf16 v[220:223], a[216:219], a[32:35], v[220:223]\n"
      "ds_read_b64_tr_b16 a[250:251], v11 offset:6400\n"
      "v_fma_f32 v217, v217, s48, v18\n"
      "v_mfma_f32_16x16x32_bf16 v[220:223], a[212:215], a[28:31], v[220:223]\n"
      "ds_read_b64_tr_b16 a[252:253], v11 offset:12544\n"
      "v_fma_f32 v218, v218, s48, v18\n"
      "v_mfma_f32_16x16x32_bf16 v[220:223], a[208:211], a[24:27], v[220:223]\n"
      "s_mov_b32 s38, s36\n"
      "ds_read_b64_tr_b16 a[254:255], v11 offset:18688\n"
      "v_fma_f32 v219, v219, s48, v18\n"
      "v_mfma_f32_16x16x32_bf16 v[224:227], a[208:211], a[48:51], 0\n"
      "s_mov_b32 s39, s37\n"
      "v_exp_f32 v32, v216\n"
      "v_mfma_f32_16x16x32_bf16 v[224:227], a[212:215], a[52:55], v[224:227]\n"
      "s_add_u32 s12, s12, 1\n"
      "v_exp_f32 v33, v217\n"
      "v_mfma_f32_16x16x32_bf16 v[224:227], a[216:219], a[56:59], v[224:227]\n"
      "s_add_u32 s15, s13, s12\n"
      "v_exp_f32 v34, v218\n"
      "v_mfma_f32_16x16x32_bf16 v[224:227], a[220:223], a[60:63], v[224:227]\n"
      "s_and_b32 s15, s15, 15\n"
      "v_exp_f32 v35, v219\n"
      "v_mfma_f32_16x16x32_bf16 v[224:227], a[224:227], a[64:67], v[224:227]\n"
      "s_waitcnt vmcnt(12) lgkmcnt(0)\n"
      "v_cvt_pk_bf16_f32 v166, v32, v33\n"
      "v_or3_b32 v22, v22, v181, v166\n"
      "v_mfma_f32_16x16x32_bf16 v[224:227], a[228:231], a[68:71], v[224:227]\n"
      "s_barrier\n"
      "v_cvt_pk_bf16_f32 v167, v34, v35\n"
      "v_fma_f32 v220, v220, s48, v19\n"
      "v_mfma_f32_16x16x32_bf16 v[100:103], v[192:195], a[236:239], v[100:103]\n"
      "s_mul_i32 s15, s15, 24576\n"
      "ds_read_b128 a[208:211], v232 offset:0\n"
      "v_fma_f32 v221, v221, s48, v19\n"
      "v_mfma_f32_16x16x32_bf16 v[100:103], v[188:191], a[232:235], v[100:103]\n"
      "s_lshr_b32 s16, s12, 4\n"
      "v_exp_f32 v28, v220\n"
      "v_mfma_f32_16x16x32_bf16 v[148:151], v[196:199], a[232:235], v[148:151]\n"
      "s_mul_i32 s16, s16, 393216\n"
      "ds_read_b128 a[212:215], v233 offset:0\n"
      "v_fma_f32 v222, v222, s48, v19\n"
      "v_mfma_f32_16x16x32_bf16 v[148:151], v[200:203], a[236:239], v[148:151]\n"
      "s_add_u32 s15, s15, s16\n"
      "v_exp_f32 v29, v221\n"
      "v_mfma_f32_16x16x32_bf16 a[132:135], v[208:211], a[236:239], a[132:135]\n"
      "s_add_u32 s36, s15, s14\n"
      "ds_read_b128 a[216:219], v232 offset:128\n"
      "v_fma_f32 v223, v223, s48, v19\n"
      "v_mfma_f32_16x16x32_bf16 a[132:135], v[204:207], a[232:235], a[132:135]\n"
      "s_add_u32 s37, s36, 3072\n"
      "v_exp_f32 v30, v222\n"
      "v_mfma_f32_16x16x32_bf16 v[104:107], v[192:195], a[244:247], v[104:107]\n"
      "ds_read_b128 a[220:223], v233 offset:128\n"
      "v_cvt_pk_bf16_f32 v174, v28, v29\n"
      "v_mfma_f32_16x16x32_bf16 v[104:107], v[188:191], a[240:243], v[104:107]\n"
      "v_exp_f32 v31, v223\n"
      "v_mfma_f32_16x16x32_bf16 v[152:155], v[196:199], a[240:243], v[152:155]\n"
      "ds_read_b128 a[224:227], v232 offset:256\n"
      "v_or3_b32 v22, v22, v167, v174\n"
      "v_mfma_f32_16x16x32_bf16 v[152:155], v[200:203], a[244:247], v[152:155]\n"
      "v_cvt_pk_bf16_f32 v175, v30, v31\n"
      "v_fma_f32 v224, v224, s48, v20\n"
      "v_mfma_f32_16x16x32_bf16 a[136:139], v[208:211], a[244:247], a[136:139]\n"
      "ds_read_b128 a[228:231], v233 offset:256\n"
      "v_fma_f32 v225, v225, s48, v20\n"
      "v_mfma_f32_16x16x32_bf16 a[136:139], v[204:207], a[240:243], a[136:139]\n"
      "v_exp_f32 v32, v224\n"
      "v_mfma_f32_16x16x32_bf16 v[108:111], v[192:195], a[252:255], v[108:111]\n"
      "v_exp_f32 v33, v225\n"
      "v_mfma_f32_16x16x32_bf16 v[108:111], v[188:191], a[248:251], v[108:111]\n"
      "s_cmp_eq_u32 s50, s86\n"
      "s_cbranch_scc1 L_hook_N\n"
      "L_hook_N_ret:\n"
      "v_fma_f32 v226, v226, s48, v20\n"
      "v_mfma_f32_16x16x32_bf16 v[156:159], v[196:199], a[248:251], v[156:159]\n"
      "v_fma_f32 v227, v227, s48, v20\n"
      "v_exp_f32 v34, v226\n"
      "v_exp_f32 v35, v227\n"
      "v_cvt_pk_bf16_f32 v182, v32, v33\n"
      "v_or3_b32 v22, v22, v175, v182\n"
      "v_cvt_pk_bf16_f32 v183, v34, v35\n"
      "v_bitop3_b32 v22, v22, v183, s49 bitop3:0xa8\n"
      "v_cmp_ne_u32 vcc, 0, v22\n"
      "v_mfma_f32_16x16x32_bf16 v[156:159], v[200:203], a[252:255], v[156:159]\n"
      "s_sub_u32 s50, s50, 1\n"
      "v_mfma_f32_16x16x32_bf16 a[140:143], v[208:211], a[252:255], a[140:143]\n"
      "v_mfma_f32_16x16x32_bf16 a[140:143], v[204:207], a[248:251], a[140:143]\n"
      "s_waitcnt lgkmcnt(0)\n"
      "s_cbranch_vccnz L_rare_Nf0_3\n"
      "L_rare_Nf0_3_ret:\n"
      "s_branch L_loop_N\n"
      "L_last_N:\n"
      "s_cmp_lg_u32 s51, 0\n"
      "s_cbranch_scc1 L_fin_N\n"
      "ds_read_b64_tr_b16 a[232:233], v8 offset:0\n"
      "ds_read_b64_tr_b16 a[234:235], v8 offset:6144\n"
      "ds_read_b64_tr_b16 a[236:237], v8 offset:12288\n"
      "ds_read_b64_tr_b16 a[238:239], v8 offset:18432\n"
      "ds_read_b64_tr_b16 a[240:241], v9 offset:0\n"
      "ds_read_b64_tr_b16 a[242:243], v9 offset:6144\n"
      "ds_read_b64_tr_b16 a[244:245], v9 offset:12288\n"
      "ds_read_b64_tr_b16 a[246:247], v9 offset:18432\n"
      "ds_read_b64_tr_b16 a[248:249], v10 offset:0\n"
      "ds_read_b64_tr_b16 a[250:251], v10 offset:6144\n"
      "ds_read_b64_tr_b16 a[252:253], v10 offset:12288\n"
      "ds_read_b64_tr_b16 a[254:255], v10 offset:18432\n"
      "s_mov_b32 s20, s90\n"
      "s_mov_b32 s24, s93\n"
      "s_mov_b32 s21, s91\n"
      "s_mov_b32 s25, s94\n"
      "s_mov_b32 s22, s92\n"
      "s_mov_b32 s26, s95\n"
      "s_mov_b32 s36, s84\n"
      "s_add_u32 s37, s84, 3072\n"
      "s_mov_b32 s38, s85\n"
      "s_add_u32 s39, s85, 3072\n"
      "s_add_u32 s46, s44, s41\n"
      "s_add_u32 s47, s45, s40\n"
      "s_sub_u32 s63, s46, s65\n"
      "s_sub_u32 s64, s47, s65\n"
      "s_add_u32 s82, s45, s41\n"
      "s_sub_u32 s83, s82, s65\n"
      "v_smfmac_f32_16x16x64_bf16 a[192:195], v[24:27], v[188:195], v16\n"
      "s_add_u32 m0, s82, 0\n"
      "v_add_u32 v216, s63, v6\n"
      "v_add_u32 v217, s63, v7\n"
      "v_smfmac_f32_16x16x64_bf16 a[196:199], v[24:27], v[196:203], v16\n"
      "s_add_u32 s43, s66, 0\n"
      "v_add_u32 v218, s64, v6\n"
      "v_add_u32 v219, s64, v7\n"
      "v_smfmac_f32_16x16x64_bf16 a[200:203], v[24:27], v[204:211], v16\n"
      "s_waitcnt lgkmcnt(8)\n"
      "buffer_load_dwordx4 v1, s[28:31], s43 offen offset:0 nt lds\n"
      "v_mfma_f32_16x16x32_bf16 v[64:67], v[192:195], a[236:239], v[64:67]\n"
      "buffer_load_dwordx4 v2, s[28:31], s43 offen offset:1024 nt lds\n"
      "v_mfma_f32_16x16x32_bf16 v[64:67], v[188:191], a[232:235], v[64:67]\n"
      "buffer_load_dwordx4 v3, s[28:31], s43 offen offset:2048 nt lds\n"
      "v_mfma_f32_16x16x32_bf16 v[112:115], v[196:199], a[232:235], v[112:115]\n"
      "s_add_u32 m0, s82, 3072\n"
      "v_add_u32 v220, s89, v6\n"
      "v_add_u32 v221, s89, v7\n"
      "v_mfma_f32_16x16x32_bf16 v[112:115], v[200:203], a[236:239], v[112:115]\n"
      "s_add_u32 s43, s66, 3072\n"
      "v_add_u32 v222, s83, v6\n"
      "v_add_u32 v223, s83, v7\n"
      "v_mfma_f32_16x16x32_bf16 a[96:99], v[208:211], a[236:239], a[96:99]\n"
      "buffer_load_dwordx4 v1, s[28:31], s43 offen offset:0 nt lds\n"
      "v_mfma_f32_16x16x32_bf16 a[96:99], v[204:207], a[232:235], a[96:99]\n"
      "s_waitcnt lgkmcnt(4)\n"
      "buffer_load_dwordx4 v2, s[28:31], s43 offen offset:1024 nt lds\n"
      "v_mfma_f32_16x16x32_bf16 v[68:71], v[192:195], a[244:247], v[68:71]\n"
      "ds_read_b64_tr_b16 a[232:233], v11 offset:0\n"
      "ds_read_b64_tr_b16 a[234:235], v11 offset:6144\n"
      "v_mfma_f32_16x16x32_bf16 v[68:71], v[188:191], a[240:243], v[68:71]\n"
      "ds_read_b64_tr_b16 a[236:237], v11 offset:12288\n"
      "ds_read_b64_tr_b16 a[238:239], v11 offset:18432\n"
      "v_mfma_f32_16x16x32_bf16 v[116:119], v[196:199], a[240:243], v[116:119]\n"
      "buffer_load_dwordx4 v3, s[28:31], s43 offen offset:2048 nt lds\n"
      "v_mfma_f32_16x16x32_bf16 v[116:119], v[200:203], a[244:247], v[116:119]\n"
      "v_accvgpr_read_b32 v238, a192\n"
      "v_accvgpr_read_b32 v242, a196\n"
      "v_mfma_f32_16x16x32_bf16 a[100:103], v[208:211], a[244:247], a[100:103]\n"
      "v_accvgpr_read_b32 v246, a200\n"
      "v_accvgpr_read_b32 v239, a193\n"
      "v_mfma_f32_16x16x32_bf16 a[100:103], v[204:207], a[240:243], a[100:103]\n"
      "s_waitcnt lgkmcnt(4)\n"
      "ds_read_b64_tr_b16 a[240:241], v8 offset:128\n"
      "v_accvgpr_read_b32 v243, a197\n"
      "v_mfma_f32_16x16x32_bf16 v[72:75], v[192:195], a[252:255], v[72:75]\n"
      "ds_read_b64_tr_b16 a[242:243], v8 offset:6272\n"
      "ds_read_b64_tr_b16 a[244:245], v8 offset:12416\n"
      "v_mfma_f32_16x16x32_bf16 v[72:75], v[188:191], a[248:251], v[72:75]\n"
      "ds_read_b64_tr_b16 a[246:247], v8 offset:18560\n"
      "v_accvgpr_read_b32 v247, a201\n"
      "v_mfma_f32_16x16x32_bf16 v[120:123], v[196:199], a[248:251], v[120:123]\n"
      "s_lshl_b32 s63, s61, 9\n"
      "v_mfma_f32_16x16x32_bf16 v[120:123], v[200:203], a[252:255], v[120:123]\n"
      "s_add_u32 s63, s63, s65\n"
      "v_mfma_f32_16x16x32_bf16 a[104:107], v[208:211], a[252:255], a[104:107]\n"
      "s_add_u32 s63, s63, 161792\n"
      "v_and_b32 v236, 15, v0\n"
      "v_lshl_add_u32 v236, v236, 2, s63\n"
      "v_mfma_f32_16x16x32_bf16 a[104:107], v[204:207], a[248:251], a[104:107]\n"
      "s_waitcnt lgkmcnt(4)\n"
      "s_waitcnt vmcnt(6)\n"
      "v_and_b32 v237, 48, v0\n"
      "v_mfma_f32_16x16x32_bf16 v[76:79], v[192:195], a[236:239], v[76:79]\n"
      "ds_read_b64_tr_b16 a[248:249], v9 offset:128\n"
      "ds_read_b64_tr_b16 a[250:251], v9 offset:6272\n"
      "v_mfma_f32_16x16x32_bf16 v[76:79], v[188:191], a[232:235], v[76:79]\n"
      "ds_read_b64_tr_b16 a[252:253], v9 offset:12416\n"
      "ds_read_b64_tr_b16 a[254:255], v9 offset:18560\n"
      "v_mfma_f32_16x16x32_bf16 v[124:127], v[196:199], a[232:235], v[124:127]\n"
      "ds_read_b128 a[24:27], v216 offset:0\n"
      "v_add_u32 v237, s63, v237\n"
      "v_mfma_f32_16x16x32_bf16 v[124:127], v[200:203], a[236:239], v[124:127]\n"
      "ds_read_b128 a[28:31], v217 offset:0\n"
      "v_add_f32 v238, v238, v239\n"
      "v_mfma_f32_16x16x32_bf16 a[108:111], v[208:211], a[236:239], a[108:111]\n"
      "ds_read_b128 a[32:35], v216 offset:128\n"
      "v_add_f32 v242, v242, v243\n"
      "v_mfma_f32_16x16x32_bf16 a[108:111], v[204:207], a[232:235], a[108:111]\n"
      "s_waitcnt lgkmcnt(7)\n"
      "ds_read_b64_tr_b16 a[232:233], v10 offset:128\n"
      "v_add_f32 v246, v246, v247\n"
      "v_mfma_f32_16x16x32_bf16 v[80:83], v[192:195], a[244:247], v[80:83]\n"
      "s_add_u32 s82, s45, s41\n"
      "ds_read_b64_tr_b16 a[234:235], v10 offset:6272\n"
      "ds_read_b64_tr_b16 a[236:237], v10 offset:12416\n"
      "v_mfma_f32_16x16x32_bf16 v[80:83], v[188:191], a[240:243], v[80:83]\n"
      "ds_read_b64_tr_b16 a[238:239], v10 offset:18560\n"
      "ds_read_b128 a[36:39], v217 offset:128\n"
      "v_mfma_f32_16x16x32_bf16 v[128:131], v[196:199], a[240:243], v[128:131]\n"
      "v_rcp_f32 v238, v238\n"
      "v_mfma_f32_16x16x32_bf16 v[128:131], v[200:203], a[244:247], v[128:131]\n"
      "v_rcp_f32 v242, v242\n"
      "v_mfma_f32_16x16x32_bf16 a[112:115], v[208:211], a[244:247], a[112:115]\n"
      "v_rcp_f32 v246, v246\n"
      "v_mfma_f32_16x16x32_bf16 a[112:115], v[204:207], a[240:243], a[112:115]\n"
      "s_waitcnt lgkmcnt(8)\n"
      "ds_read_b64_tr_b16 a[240:241], v11 offset:128\n"
      "v_add_u32 v232, s82, v251\n"
      "v_mfma_f32_16x16x32_bf16 v[84:87], v[192:195], a[252:255], v[84:87]\n"
      "ds_read_b64_tr_b16 a[242:243], v11 offset:6272\n"
      "ds_read_b64_tr_b16 a[244:245], v11 offset:12416\n"
      "v_mfma_f32_16x16x32_bf16 v[84:87], v[188:191], a[248:251], v[84:87]\n"
      "ds_read_b64_tr_b16 a[246:247], v11 offset:18560\n"
      "ds_read_b128 a[40:43], v216 offset:256\n"
      "v_mfma_f32_16x16x32_bf16 v[132:135], v[196:199], a[248:251], v[132:135]\n"
      "ds_read_b128 a[44:47], v217 offset:256\n"
      "ds_write_b32 v236, v238 offset:0\n"
      "v_mfma_f32_16x16x32_bf16 v[132:135], v[200:203], a[252:255], v[132:135]\n"
      "ds_read_b128 a[48:51], v218 offset:0\n"
      "ds_write_b32 v236, v242 offset:64\n"
      "v_mfma_f32_16x16x32_bf16 a[116:119], v[208:211], a[252:255], a[116:119]\n"
      "ds_read_b128 a[52:55], v219 offset:0\n"
      "ds_write_b32 v236, v246 offset:128\n"
      "v_mfma_f32_16x16x32_bf16 a[116:119], v[204:207], a[248:251], a[116:119]\n"
      "s_waitcnt lgkmcnt(12)\n"
      "ds_read_b128 a[56:59], v218 offset:128\n"
      "ds_read_b64_tr_b16 a[248:249], v8 offset:256\n"
      "v_mfma_f32_16x16x32_bf16 v[88:91], v[192:195], a[236:239], v[88:91]\n"
      "ds_read_b64_tr_b16 a[250:251], v8 offset:6400\n"
      "ds_read_b64_tr_b16 a[252:253], v8 offset:12544\n"
      "v_mfma_f32_16x16x32_bf16 v[88:91], v[188:191], a[232:235], v[88:91]\n"
      "ds_read_b64_tr_b16 a[254:255], v8 offset:18688\n"
      "ds_read_b128 a[60:63], v219 offset:128\n"
      "v_mfma_f32_16x16x32_bf16 v[136:139], v[196:199], a[232:235], v[136:139]\n"
      "ds_read_b128 a[64:67], v218 offset:256\n"
      "ds_read_b128 v[238:241], v237 offset:0\n"
      "v_mfma_f32_16x16x32_bf16 v[136:139], v[200:203], a[236:239], v[136:139]\n"
      "ds_read_b128 a[68:71], v219 offset:256\n"
      "ds_read_b128 v[242:245], v237 offset:64\n"
      "v_mfma_f32_16x16x32_bf16 a[120:123], v[208:211], a[236:239], a[120:123]\n"
      "ds_read_b128 v[246:249], v237 offset:128\n"
      "v_add_u32 v234, s82, v17\n"
      "v_mfma_f32_16x16x32_bf16 a[120:123], v[204:207], a[232:235], a[120:123]\n"
      "s_waitcnt lgkmcnt(14)\n"
      "ds_read_b64_tr_b16 a[232:233], v9 offset:256\n"
      "v_xor_b32 v233, 32, v232\n"
      "v_mfma_f32_16x16x32_bf16 v[92:95], v[192:195], a[244:247], v[92:95]\n"
      "ds_read_b64_tr_b16 a[234:235], v9 offset:6400\n"
      "ds_read_b64_tr_b16 a[236:237], v9 offset:12544\n"
      "v_mfma_f32_16x16x32_bf16 v[92:95], v[188:191], a[240:243], v[92:95]\n"
      "s_waitcnt lgkmcnt(5)\n"
      "s_mov_b32 m0, s46\n"
      "ds_read_b64_tr_b16 a[238:239], v9 offset:18688\n"
      "v_mfma_f32_16x16x32_bf16 v[140:143], v[196:199], a[240:243], v[140:143]\n"
      "buffer_load_dwordx4 v1, s[20:23], s36 offen offset:0 lds\n"
      "v_mfma_f32_16x16x32_bf16 v[140:143], v[200:203], a[244:247], v[140:143]\n"
      "v_xor_b32 v235, 32, v234\n"
      "v_lshlrev_b32 v23, 4, v0\n"
      "v_mfma_f32_16x16x32_bf16 a[124:127], v[208:211], a[244:247], a[124:127]\n"
      "buffer_load_dwordx4 v2, s[20:23], s36 offen offset:1024 lds\n"
      "v_mfma_f32_16x16x32_bf16 a[124:127], v[204:207], a[240:243], a[124:127]\n"
      "s_waitcnt lgkmcnt(6)\n"
      "ds_read_b64_tr_b16 a[240:241], v10 offset:256\n"
      "v_mov_b32 v28, 0\n"
      "v_mfma_f32_16x16x32_bf16 v[96:99], v[192:195], a[252:255], v[96:99]\n"
      "buffer_load_dwordx4 v3, s[20:23], s36 offen offset:2048 lds\n"
      "s_add_u32 m0, s46, 3072\n"
      "v_mfma_f32_16x16x32_bf16 v[96:99], v[188:191], a[248:251], v[96:99]\n"
      "ds_read_b64_tr_b16 a[242:243], v10 offset:6400\n"
      "ds_read_b64_tr_b16 a[244:245], v10 offset:12544\n"
      "ds_read_b64_tr_b16 a[246:247], v10 offset:18688\n"
      "buffer_load_dwordx4 v1, s[20:23], s37 offen offset:0 lds\n"
      "v_mfma_f32_16x16x32_bf16 v[144:147], v[196:199], a[248:251], v[144:147]\n"
      "v_mov_b32 v29, 0\n"
      "v_mov_b32 v30, 0\n"
      "v_mfma_f32_16x16x32_bf16 v[144:147], v[200:203], a[252:255], v[144:147]\n"
      "buffer_load_dwordx4 v2, s[20:23], s37 offen offset:1024 lds\n"
      "v_mfma_f32_16x16x32_bf16 a[128:131], v[208:211], a[252:255], a[128:131]\n"
      "v_mov_b32 v31, 0\n"
      "v_mfma_f32_16x16x32_bf16 a[128:131], v[204:207], a[248:251], a[128:131]\n"
      "s_waitcnt lgkmcnt(4)\n"
      "buffer_load_dwordx4 v3, s[20:23], s37 offen offset:2048 lds\n"
      "s_mov_b32 m0, s47\n"
      "v_mfma_f32_16x16x32_bf16 v[100:103], v[192:195], a[236:239], v[100:103]\n"
      "s_waitcnt lgkmcnt(4)\n"
      "buffer_load_dwordx4 v1, s[24:27], s38 offen offset:0 lds\n"
      "v_mfma_f32_16x16x32_bf16 v[100:103], v[188:191], a[232:235], v[100:103]\n"
      "ds_read_b64_tr_b16 a[248:249], v11 offset:256\n"
      "ds_read_b64_tr_b16 a[250:251], v11 offset:6400\n"
      "ds_read_b64_tr_b16 a[252:253], v11 offset:12544\n"
      "ds_read_b64_tr_b16 a[254:255], v11 offset:18688\n"
      "v_mfma_f32_16x16x32_bf16 v[148:151], v[196:199], a[232:235], v[148:151]\n"
      "buffer_load_dwordx4 v2, s[24:27], s38 offen offset:1024 lds\n"
      "v_mfma_f32_16x16x32_bf16 v[148:151], v[200:203], a[236:239], v[148:151]\n"
      "v_mfma_f32_16x16x32_bf16 a[132:135], v[208:211], a[236:239], a[132:135]\n"
      "buffer_load_dwordx4 v3, s[24:27], s38 offen offset:2048 lds\n"
      "s_add_u32 m0, s47, 3072\n"
      "v_mfma_f32_16x16x32_bf16 a[132:135], v[204:207], a[232:235], a[132:135]\n"
      "s_waitcnt lgkmcnt(4)\n"
      "buffer_load_dwordx4 v1, s[24:27], s39 offen offset:0 lds\n"
      "v_mfma_f32_16x16x32_bf16 v[104:107], v[192:195], a[244:247], v[104:107]\n"
      "v_mfma_f32_16x16x32_bf16 v[104:107], v[188:191], a[240:243], v[104:107]\n"
      "buffer_load_dwordx4 v2, s[24:27], s39 offen offset:1024 lds\n"
      "v_mfma_f32_16x16x32_bf16 v[152:155], v[196:199], a[240:243], v[152:155]\n"
      "v_mfma_f32_16x16x32_bf16 v[152:155], v[200:203], a[244:247], v[152:155]\n"
      "buffer_load_dwordx4 v3, s[24:27], s39 offen offset:2048 lds\n"
      "v_mfma_f32_16x16x32_bf16 a[136:139], v[208:211], a[244:247], a[136:139]\n"
      "s_waitcnt vmcnt(12)\n"
      "v_mfma_f32_16x16x32_bf16 a[136:139], v[204:207], a[240:243], a[136:139]\n"
      "s_waitcnt lgkmcnt(0)\n"
      "ds_read_b128 a[0:3], v222 offset:0\n"
      "v_mfma_f32_16x16x32_bf16 v[108:111], v[192:195], a[252:255], v[108:111]\n"
      "s_mov_b32 s38, s36\n"
      "ds_read_b128 a[4:7], v223 offset:0\n"
      "v_mfma_f32_16x16x32_bf16 v[108:111], v[188:191], a[248:251], v[108:111]\n"
      "s_mov_b32 s39, s37\n"
      "ds_read_b128 a[8:11], v222 offset:128\n"
      "v_mfma_f32_16x16x32_bf16 v[156:159], v[196:199], a[248:251], v[156:159]\n"
      "s_add_u32 s15, s13, s12\n"
      "ds_read_b128 a[12:15], v223 offset:128\n"
      "v_mfma_f32_16x16x32_bf16 v[156:159], v[200:203], a[252:255], v[156:159]\n"
      "s_and_b32 s15, s15, 15\n"
      "s_mul_i32 s15, s15, 24576\n"
      "ds_read_b128 a[16:19], v222 offset:256\n"
      "v_mfma_f32_16x16x32_bf16 a[140:143], v[208:211], a[252:255], a[140:143]\n"
      "s_lshr_b32 s16, s12, 4\n"
      "s_mul_i32 s16, s16, 393216\n"
      "ds_read_b128 a[20:23], v223 offset:256\n"
      "v_mfma_f32_16x16x32_bf16 a[140:143], v[204:207], a[248:251], a[140:143]\n"
      "s_add_u32 s15, s15, s16\n"
      "s_add_u32 s36, s15, s14\n"
      "s_add_u32 s37, s36, 3072\n"
      "v_pk_mul_f32 v[64:65], v[64:65], v[238:239]\n"
      "v_pk_mul_f32 v[66:67], v[66:67], v[240:241]\n"
      "v_pk_mul_f32 v[68:69], v[68:69], v[238:239]\n"
      "v_pk_mul_f32 v[70:71], v[70:71], v[240:241]\n"
      "v_pk_mul_f32 v[72:73], v[72:73], v[238:239]\n"
      "v_pk_mul_f32 v[74:75], v[74:75], v[240:241]\n"
      "v_pk_mul_f32 v[76:77], v[76:77], v[238:239]\n"
      "v_pk_mul_f32 v[78:79], v[78:79], v[240:241]\n"
      "v_cvt_pk_bf16_f32 v176, v64, v68\n"
      "v_cvt_pk_bf16_f32 v177, v72, v76\n"
      "v_cvt_pk_bf16_f32 v178, v65, v69\n"
      "v_cvt_pk_bf16_f32 v179, v73, v77\n"
      "v_cvt_pk_bf16_f32 v180, v66, v70\n"
      "v_cvt_pk_bf16_f32 v181, v74, v78\n"
      "v_cvt_pk_bf16_f32 v182, v67, v71\n"
      "v_cvt_pk_bf16_f32 v183, v75, v79\n"
      "v_pk_mul_f32 v[80:81], v[80:81], v[238:239]\n"
      "v_pk_mul_f32 v[82:83], v[82:83], v[240:241]\n"
      "v_pk_mul_f32 v[84:85], v[84:85], v[238:239]\n"
      "v_pk_mul_f32 v[86:87], v[86:87], v[240:241]\n"
      "ds_write_b64 v232, v[176:177] offset:0\n"
      "ds_write_b64 v232, v[178:179] offset:128\n"
      "v_pk_mul_f32 v[88:89], v[88:89], v[238:239]\n"
      "v_pk_mul_f32 v[90:91], v[90:91], v[240:241]\n"
      "v_pk_mul_f32 v[92:93], v[92:93], v[238:239]\n"
      "v_pk_mul_f32 v[94:95], v[94:95], v[240:241]\n"
      "ds_write_b64 v233, v[180:181] offset:256\n"
      "ds_write_b64 v233, v[182:183] offset:384\n"
      "v_mfma_f32_32x32x16_bf16 v[64:79], v[28:31], v[28:31], 0\n"
      "v_cvt_pk_bf16_f32 v184, v80, v84\n"
      "v_cvt_pk_bf16_f32 v185, v88, v92\n"
      "v_cvt_pk_bf16_f32 v186, v81, v85\n"
      "ds_read_b64_tr_b16 v[192:193], v234 offset:0\n"
      "ds_read_b64_tr_b16 v[194:195], v235 offset:0\n"
      "v_cvt_pk_bf16_f32 v187, v89, v93\n"
      "v_cvt_pk_bf16_f32 v188, v82, v86\n"
      "v_cvt_pk_bf16_f32 v189, v90, v94\n"
      "v_cvt_pk_bf16_f32 v190, v83, v87\n"
      "v_cvt_pk_bf16_f32 v191, v91, v95\n"
      "ds_read_b64_tr_b16 v[196:197], v234 offset:1024\n"
      "ds_read_b64_tr_b16 v[198:199], v235 offset:1024\n"
      "v_pk_mul_f32 v[96:97], v[96:97], v[238:239]\n"
      "v_pk_mul_f32 v[98:99], v[98:99], v[240:241]\n"
      "v_pk_mul_f32 v[100:101], v[100:101], v[238:239]\n"
      "v_pk_mul_f32 v[102:103], v[102:103], v[240:241]\n"
      "v_pk_mul_f32 v[104:105], v[104:105], v[238:239]\n"
      "ds_write_b64 v232, v[184:185] offset:2048\n"
      "ds_write_b64 v232, v[186:187] offset:2176\n"
      "v_pk_mul_f32 v[106:107], v[106:107], v[240:241]\n"
      "v_pk_mul_f32 v[108:109], v[108:109], v[238:239]\n"
      "v_pk_mul_f32 v[110:111], v[110:111], v[240:241]\n"
      "v_mfma_f32_32x32x16_bf16 v[80:95], v[28:31], v[28:31], 0\n"
      "v_cvt_pk_bf16_f32 v176, v96, v100\n"
      "ds_write_b64 v233, v[188:189] offset:2304\n"
      "ds_write_b64 v233, v[190:191] offset:2432\n"
      "s_waitcnt lgkmcnt(4)\n"
      "v_cvt_pk_bf16_f32 v177, v104, v108\n"
      "buffer_store_dwordx4 v[192:195], v250, s[72:75], s52 offen offset:0\n"
      "v_cvt_pk_bf16_f32 v178, v97, v101\n"
      "v_cvt_pk_bf16_f32 v179, v105, v109\n"
      "ds_read_b64_tr_b16 v[200:201], v234 offset:2048\n"
      "ds_read_b64_tr_b16 v[202:203], v235 offset:2048\n"
      "v_cvt_pk_bf16_f32 v180, v98, v102\n"
      "buffer_store_dwordx4 v[196:199], v250, s[72:75], s52 offen offset:3072\n"
      "v_cvt_pk_bf16_f32 v181, v106, v110\n"
      "v_cvt_pk_bf16_f32 v182, v99, v103\n"
      "v_cvt_pk_bf16_f32 v183, v107, v111\n"
      "ds_read_b64_tr_b16 v[204:205], v234 offset:3072\n"
      "ds_read_b64_tr_b16 v[206:207], v235 offset:3072\n"
      "v_pk_mul_f32 v[112:113], v[112:113], v[242:243]\n"
      "v_pk_mul_f32 v[114:115], v[114:115], v[244:245]\n"
      "v_pk_mul_f32 v[116:117], v[116:117], v[242:243]\n"
      "v_pk_mul_f32 v[118:119], v[118:119], v[244:245]\n"
      "v_pk_mul_f32 v[120:121], v[120:121], v[242:243]\n"
      "ds_write_b64 v232, v[176:177] offset:4096\n"
      "ds_write_b64 v232, v[178:179] offset:4224\n"
      "v_pk_mul_f32 v[122:123], v[122:123], v[244:245]\n"
      "v_pk_mul_f32 v[124:125], v[124:125], v[242:243]\n"
      "v_pk_mul_f32 v[126:127], v[126:127], v[244:245]\n"
      "v_mfma_f32_32x32x16_bf16 v[96:111], v[28:31], v[28:31], 0\n"
      "v_cvt_pk_bf16_f32 v184, v112, v116\n"
      "ds_write_b64 v233, v[180:181] offset:4352\n"
      "ds_write_b64 v233, v[182:183] offset:4480\n"
      "s_waitcnt lgkmcnt(4)\n"
      "v_cvt_pk_bf16_f32 v185, v120, v124\n"
      "buffer_store_dwordx4 v[200:203], v250, s[72:75], s52 offen offset:128\n"
      "v_cvt_pk_bf16_f32 v186, v113, v117\n"
      "v_cvt_pk_bf16_f32 v187, v121, v125\n"
      "ds_read_b64_tr_b16 v[208:209], v234 offset:4096\n"
      "ds_read_b64_tr_b16 v[210:211], v235 offset:4096\n"
      "v_cvt_pk_bf16_f32 v188, v114, v118\n"
      "buffer_store_dwordx4 v[204:207], v250, s[72:75], s52 offen offset:3200\n"
      "v_cvt_pk_bf16_f32 v189, v122, v126\n"
      "v_cvt_pk_bf16_f32 v190, v115, v119\n"
      "v_cvt_pk_bf16_f32 v191, v123, v127\n"
      "ds_read_b64_tr_b16 v[212:213], v234 offset:5120\n"
      "ds_read_b64_tr_b16 v[214:215], v235 offset:5120\n"
      "v_pk_mul_f32 v[128:129], v[128:129], v[242:243]\n"
      "v_pk_mul_f32 v[130:131], v[130:131], v[244:245]\n"
      "v_pk_mul_f32 v[132:133], v[132:133], v[242:243]\n"
      "v_pk_mul_f32 v[134:135], v[134:135], v[244:245]\n"
      "v_pk_mul_f32 v[136:137], v[136:137], v[242:243]\n"
      "ds_write_b64 v232, v[184:185] offset:0\n"
      "ds_write_b64 v232, v[186:187] offset:128\n"
      "v_pk_mul_f32 v[138:139], v[138:139], v[244:245]\n"
      "v_pk_mul_f32 v[140:141], v[140:141], v[242:243]\n"
      "v_pk_mul_f32 v[142:143], v[142:143], v[244:245]\n"
      "v_mfma_f32_32x32x16_bf16 v[112:127], v[28:31], v[28:31], 0\n"
      "v_cvt_pk_bf16_f32 v176, v128, v132\n"
      "ds_write_b64 v233, v[188:189] offset:256\n"
      "ds_write_b64 v233, v[190:191] offset:384\n"
      "s_waitcnt lgkmcnt(4)\n"
      "v_cvt_pk_bf16_f32 v177, v136, v140\n"
      "buffer_store_dwordx4 v[208:211], v250, s[72:75], s52 offen offset:256\n"
      "v_cvt_pk_bf16_f32 v178, v129, v133\n"
      "v_cvt_pk_bf16_f32 v179, v137, v141\n"
      "ds_read_b64_tr_b16 v[192:193], v234 offset:0\n"
      "ds_read_b64_tr_b16 v[194:195], v235 offset:0\n"
      "v_cvt_pk_bf16_f32 v180, v130, v134\n"
      "buffer_store_dwordx4 v[212:215], v250, s[72:75], s52 offen offset:3328\n"
      "v_cvt_pk_bf16_f32 v181, v138, v142\n"
      "v_cvt_pk_bf16_f32 v182, v131, v135\n"
      "v_cvt_pk_bf16_f32 v183, v139, v143\n"
      "ds_read_b64_tr_b16 v[196:197], v234 offset:1024\n"
      "ds_read_b64_tr_b16 v[198:199], v235 offset:1024\n"
      "s_waitcnt lgkmcnt(8)\n"
      "s_barrier\n"
      "v_pk_mul_f32 v[144:145], v[144:145], v[242:243]\n"
      "v_pk_mul_f32 v[146:147], v[146:147], v[244:245]\n"
      "v_pk_mul_f32 v[148:149], v[148:149], v[242:243]\n"
      "v_pk_mul_f32 v[150:151], v[150:151], v[244:245]\n"
      "v_pk_mul_f32 v[152:153], v[152:153], v[242:243]\n"
      "ds_write_b64 v232, v[176:177] offset:2048\n"
      "ds_write_b64 v232, v[178:179] offset:2176\n"
      "v_pk_mul_f32 v[154:155], v[154:155], v[244:245]\n"
      "v_pk_mul_f32 v[156:157], v[156:157], v[242:243]\n"
      "v_pk_mul_f32 v[158:159], v[158:159], v[244:245]\n"
      "v_mfma_f32_32x32x16_bf16 v[128:143], v[28:31], v[28:31], 0\n"
      "v_cvt_pk_bf16_f32 v184, v144, v148\n"
      "ds_write_b64 v233, v[180:181] offset:2304\n"
      "ds_write_b64 v233, v[182:183] offset:2432\n"
      "s_waitcnt lgkmcnt(4)\n"
      "v_cvt_pk_bf16_f32 v185, v152, v156\n"
      "buffer_store_dwordx4 v[192:195], v250, s[72:75], s53 offen offset:0\n"
      "v_cvt_pk_bf16_f32 v186, v145, v149\n"
      "v_cvt_pk_bf16_f32 v187, v153, v157\n"
      "ds_read_b64_tr_b16 v[200:201], v234 offset:2048\n"
      "ds_read_b64_tr_b16 v[202:203], v235 offset:2048\n"
      "v_cvt_pk_bf16_f32 v188, v146, v150\n"
      "buffer_store_dwordx4 v[196:199], v250, s[72:75], s53 offen offset:3072\n"
      "v_cvt_pk_bf16_f32 v189, v154, v158\n"
      "v_cvt_pk_bf16_f32 v190, v147, v151\n"
      "v_cvt_pk_bf16_f32 v191, v155, v159\n"
      "ds_read_b64_tr_b16 v[204:205], v234 offset:3072\n"
      "ds_read_b64_tr_b16 v[206:207], v235 offset:3072\n"
      "v_mfma_f32_32x32x16_bf16 v[144:159], v[28:31], v[28:31], 0\n"
      "v_accvgpr_read_b32 v160, a96\n"
      "v_accvgpr_read_b32 v161, a97\n"
      "v_accvgpr_read_b32 v162, a98\n"
      "v_accvgpr_read_b32 v163, a99\n"
      "v_accvgpr_read_b32 v164, a100\n"
      "v_accvgpr_read_b32 v165, a101\n"
      "v_accvgpr_read_b32 v166, a102\n"
      "v_accvgpr_read_b32 v167, a103\n"
      "ds_write_b64 v232, v[184:185] offset:4096\n"
      "ds_write_b64 v232, v[186:187] offset:4224\n"
      "v_accvgpr_read_b32 v168, a104\n"
      "v_accvgpr_read_b32 v169, a105\n"
      "v_accvgpr_read_b32 v170, a106\n"
      "v_accvgpr_read_b32 v171, a107\n"
      "v_accvgpr_read_b32 v172, a108\n"
      "v_accvgpr_read_b32 v173, a109\n"
      "v_accvgpr_read_b32 v174, a110\n"
      "v_accvgpr_read_b32 v175, a111\n"
      "v_pk_mul_f32 v[160:161], v[160:161], v[246:247]\n"
      "ds_write_b64 v233, v[188:189] offset:4352\n"
      "ds_write_b64 v233, v[190:191] offset:4480\n"
      "v_pk_mul_f32 v[162:163], v[162:163], v[248:249]\n"
      "v_pk_mul_f32 v[164:165], v[164:165], v[246:247]\n"
      "v_pk_mul_f32 v[166:167], v[166:167], v[248:249]\n"
      "v_pk_mul_f32 v[168:169], v[168:169], v[246:247]\n"
      "v_pk_mul_f32 v[170:171], v[170:171], v[248:249]\n"
      "v_pk_mul_f32 v[172:173], v[172:173], v[246:247]\n"
      "v_pk_mul_f32 v[174:175], v[174:175], v[248:249]\n"
      "v_cvt_pk_bf16_f32 v176, v160, v164\n"
      "s_waitcnt lgkmcnt(4)\n"
      "ds_read_b64_tr_b16 v[208:209], v234 offset:4096\n"
      "ds_read_b64_tr_b16 v[210:211], v235 offset:4096\n"
      "v_cvt_pk_bf16_f32 v177, v168, v172\n"
      "buffer_store_dwordx4 v[200:203], v250, s[72:75], s53 offen offset:128\n"
      "v_cvt_pk_bf16_f32 v178, v161, v165\n"
      "v_cvt_pk_bf16_f32 v179, v169, v173\n"
      "v_cvt_pk_bf16_f32 v180, v162, v166\n"
      "buffer_store_dwordx4 v[204:207], v250, s[72:75], s53 offen offset:3200\n"
      "v_cvt_pk_bf16_f32 v181, v170, v174\n"
      "v_cvt_pk_bf16_f32 v182, v163, v167\n"
      "v_cvt_pk_bf16_f32 v183, v171, v175\n"
      "ds_read_b64_tr_b16 v[212:213], v234 offset:5120\n"
      "ds_read_b64_tr_b16 v[214:215], v235 offset:5120\n"
      "v_mfma_f32_32x32x16_bf16 a[96:111], v[28:31], v[28:31], 0\n"
      "v_accvgpr_read_b32 v160, a112\n"
      "v_accvgpr_read_b32 v161, a113\n"
      "v_accvgpr_read_b32 v162, a114\n"
      "v_accvgpr_read_b32 v163, a115\n"
      "v_accvgpr_read_b32 v164, a116\n"
      "v_accvgpr_read_b32 v165, a117\n"
      "v_accvgpr_read_b32 v166, a118\n"
      "v_accvgpr_read_b32 v167, a119\n"
      "ds_write_b64 v232, v[176:177] offset:0\n"
      "ds_write_b64 v232, v[178:179] offset:128\n"
      "v_accvgpr_read_b32 v168, a120\n"
      "v_accvgpr_read_b32 v169, a121\n"
      "v_accvgpr_read_b32 v170, a122\n"
      "v_accvgpr_read_b32 v171, a123\n"
      "v_accvgpr_read_b32 v172, a124\n"
      "v_accvgpr_read_b32 v173, a125\n"
      "v_accvgpr_read_b32 v174, a126\n"
      "v_accvgpr_read_b32 v175, a127\n"
      "v_pk_mul_f32 v[160:161], v[160:161], v[246:247]\n"
      "ds_write_b64 v233, v[180:181] offset:256\n"
      "ds_write_b64 v233, v[182:183] offset:384\n"
      "v_pk_mul_f32 v[162:163], v[162:163], v[248:249]\n"
      "v_pk_mul_f32 v[164:165], v[164:165], v[246:247]\n"
      "v_pk_mul_f32 v[166:167], v[166:167], v[248:249]\n"
      "v_pk_mul_f32 v[168:169], v[168:169], v[246:247]\n"
      "v_pk_mul_f32 v[170:171], v[170:171], v[248:249]\n"
      "v_pk_mul_f32 v[172:173], v[172:173], v[246:247]\n"
      "v_pk_mul_f32 v[174:175], v[174:175], v[248:249]\n"
      "v_cvt_pk_bf16_f32 v184, v160, v164\n"
      "s_waitcnt lgkmcnt(4)\n"
      "ds_read_b64_tr_b16 v[192:193], v234 offset:0\n"
      "ds_read_b64_tr_b16 v[194:195], v235 offset:0\n"
      "v_cvt_pk_bf16_f32 v185, v168, v172\n"
      "buffer_store_dwordx4 v[208:211], v250, s[72:75], s53 offen offset:256\n"
      "v_cvt_pk_bf16_f32 v186, v161, v165\n"
      "v_cvt_pk_bf16_f32 v187, v169, v173\n"
      "v_cvt_pk_bf16_f32 v188, v162, v166\n"
      "buffer_store_dwordx4 v[212:215], v250, s[72:75], s53 offen offset:3328\n"
      "v_cvt_pk_bf16_f32 v189, v170, v174\n"
      "v_cvt_pk_bf16_f32 v190, v163, v167\n"
      "v_cvt_pk_bf16_f32 v191, v171, v175\n"
      "ds_read_b64_tr_b16 v[196:197], v234 offset:1024\n"
      "ds_read_b64_tr_b16 v[198:199], v235 offset:1024\n"
      "v_mfma_f32_32x32x16_bf16 a[112:127], v[28:31], v[28:31], 0\n"
      "v_accvgpr_read_b32 v160, a128\n"
      "v_accvgpr_read_b32 v161, a129\n"
      "v_accvgpr_read_b32 v162, a130\n"
      "v_accvgpr_read_b32 v163, a131\n"
      "v_accvgpr_read_b32 v164, a132\n"
      "v_accvgpr_read_b32 v165, a133\n"
      "v_accvgpr_read_b32 v166, a134\n"
      "v_accvgpr_read_b32 v167, a135\n"
      "ds_write_b64 v232, v[184:185] offset:2048\n"
      "ds_write_b64 v232, v[186:187] offset:2176\n"
      "v_accvgpr_read_b32 v168, a136\n"
      "v_accvgpr_read_b32 v169, a137\n"
      "v_accvgpr_read_b32 v170, a138\n"
      "v_accvgpr_read_b32 v171, a139\n"
      "v_accvgpr_read_b32 v172, a140\n"
      "v_accvgpr_read_b32 v173, a141\n"
      "v_accvgpr_read_b32 v174, a142\n"
      "v_accvgpr_read_b32 v175, a143\n"
      "v_pk_mul_f32 v[160:161], v[160:161], v[246:247]\n"
      "ds_write_b64 v233, v[188:189] offset:2304\n"
      "ds_write_b64 v233, v[190:191] offset:2432\n"
      "v_pk_mul_f32 v[162:163], v[162:163], v[248:249]\n"
      "v_pk_mul_f32 v[164:165], v[164:165], v[246:247]\n"
      "v_pk_mul_f32 v[166:167], v[166:167], v[248:249]\n"
      "v_pk_mul_f32 v[168:169], v[168:169], v[246:247]\n"
      "v_pk_mul_f32 v[170:171], v[170:171], v[248:249]\n"
      "v_pk_mul_f32 v[172:173], v[172:173], v[246:247]\n"
      "v_pk_mul_f32 v[174:175], v[174:175], v[248:249]\n"
      "v_cvt_pk_bf16_f32 v176, v160, v164\n"
      "s_waitcnt lgkmcnt(4)\n"
      "ds_read_b64_tr_b16 v[200:201], v234 offset:2048\n"
      "ds_read_b64_tr_b16 v[202:203], v235 offset:2048\n"
      "v_cvt_pk_bf16_f32 v177, v168, v172\n"
      "buffer_store_dwordx4 v[192:195], v250, s[72:75], s54 offen offset:0\n"
      "v_cvt_pk_bf16_f32 v178, v161, v165\n"
      "v_cvt_pk_bf16_f32 v179, v169, v173\n"
      "v_cvt_pk_bf16_f32 v180, v162, v166\n"
      "buffer_store_dwordx4 v[196:199], v250, s[72:75], s54 offen offset:3072\n"
      "v_cvt_pk_bf16_f32 v181, v170, v174\n"
      "v_cvt_pk_bf16_f32 v182, v163, v167\n"
      "v_cvt_pk_bf16_f32 v183, v171, v175\n"
      "ds_read_b64_tr_b16 v[204:205], v234 offset:3072\n"
      "ds_read_b64_tr_b16 v[206:207], v235 offset:3072\n"
      "ds_write_b64 v232, v[176:177] offset:4096\n"
      "ds_write_b64 v232, v[178:179] offset:4224\n"
      "ds_write_b64 v233, v[180:181] offset:4352\n"
      "ds_write_b64 v233, v[182:183] offset:4480\n"
      "s_waitcnt lgkmcnt(4)\n"
      "buffer_store_dwordx4 v[200:203], v250, s[72:75], s54 offen offset:128\n"
      "buffer_store_dwordx4 v[204:207], v250, s[72:75], s54 offen offset:3200\n"
      "v_mfma_f32_32x32x16_bf16 a[128:143], v[28:31], v[28:31], 0\n"
      "v_mfma_f32_32x32x16_bf16 a[192:207], v[28:31], v[28:31], 0\n"
      "ds_read_b64_tr_b16 v[208:209], v234 offset:4096\n"
      "ds_read_b64_tr_b16 v[210:211], v235 offset:4096\n"
      "ds_read_b64_tr_b16 v[212:213], v234 offset:5120\n"
      "ds_read_b64_tr_b16 v[214:215], v235 offset:5120\n"
      "s_waitcnt lgkmcnt(0)\n"
      "buffer_store_dwordx4 v[208:211], v250, s[72:75], s54 offen offset:256\n"
      "buffer_store_dwordx4 v[212:215], v250, s[72:75], s54 offen offset:3328\n"
      "s_mov_b32 s51, 1\n"
      "s_mov_b32 s43, s52\n"
      "s_mov_b32 s52, s66\n"
      "s_mov_b32 s66, s43\n"
      "s_mov_b32 s43, s53\n"
      "s_mov_b32 s53, s67\n"
      "s_mov_b32 s67, s43\n"
      "s_mov_b32 s43, s54\n"
      "s_mov_b32 s54, s68\n"
      "s_mov_b32 s68, s43\n"
      "s_mov_b32 s43, s55\n"
      "s_mov_b32 s55, s69\n"
      "s_mov_b32 s69, s43\n"
      "s_mov_b32 s86, -1\n"
      "s_mov_b32 s50, 7\n"
      "s_mov_b32 s35, 0\n"
      "v_mfma_f32_16x16x32_bf16 v[36:39], a[208:211], a[0:3], 0\n"
      "s_add_u32 s46, s44, s42\n"
      "ds_read_b128 a[232:235], v4 offset:6144\n"
      "v_add_u32 v232, s41, v6\n"
      "v_mfma_f32_16x16x32_bf16 v[36:39], a[212:215], a[4:7], v[36:39]\n"
      "s_add_u32 s47, s45, s41\n"
      "ds_read_b128 a[236:239], v5 offset:6144\n"
      "v_add_u32 v233, s41, v7\n"
      "v_mfma_f32_16x16x32_bf16 v[36:39], a[216:219], a[8:11], v[36:39]\n"
      "ds_read_b128 a[240:243], v4 offset:6272\n"
      "ds_read_b128 a[244:247], v5 offset:6272\n"
      "v_mfma_f32_16x16x32_bf16 v[36:39], a[220:223], a[12:15], v[36:39]\n"
      "s_mov_b32 m0, s46\n"
      "ds_read_b128 a[248:251], v4 offset:6400\n"
      "ds_read_b128 a[252:255], v5 offset:6400\n"
      "v_mfma_f32_16x16x32_bf16 v[36:39], a[224:227], a[16:19], v[36:39]\n"
      "buffer_load_dwordx4 v1, s[20:23], s36 offen offset:0 lds\n"
      "v_mfma_f32_16x16x32_bf16 v[36:39], a[228:231], a[20:23], v[36:39]\n"
      "buffer_load_dwordx4 v2, s[20:23], s36 offen offset:1024 lds\n"
      "v_mfma_f32_16x16x32_bf16 v[40:43], a[228:231], a[44:47], 0\n"
      "buffer_load_dwordx4 v3, s[20:23], s36 offen offset:2048 lds\n"
      "v_mfma_f32_16x16x32_bf16 v[40:43], a[224:227], a[40:43], v[40:43]\n"
      "s_nop 0\n"
      "v_add_u32 v234, s40, v12\n"
      "v_add_u32 v235, s40, v13\n"
      "v_mfma_f32_16x16x32_bf16 v[40:43], a[220:223], a[36:39], v[40:43]\n"
      "v_max3_f32 v52, v36, v37, v38\n"
      "v_max_f32 v52, v52, v39\n"
      "v_mfma_f32_16x16x32_bf16 v[40:43], a[216:219], a[32:35], v[40:43]\n"
      "v_mov_b32 v53, v52\n"
      "s_nop 1\n"
      "v_permlane32_swap_b32 v52, v53\n"
      "v_mfma_f32_16x16x32_bf16 v[40:43], a[212:215], a[28:31], v[40:43]\n"
      "v_max_f32 v52, v52, v53\n"
      "v_mov_b32 v53, v52\n"
      "v_mfma_f32_16x16x32_bf16 v[40:43], a[208:211], a[24:27], v[40:43]\n"
      "s_nop 0\n"
      "v_permlane16_swap_b32 v52, v53\n"
      "v_max_f32 v52, v52, v53\n"
      "v_mfma_f32_16x16x32_bf16 v[44:47], a[208:211], a[48:51], 0\n"
      "v_mul_f32 v52, s62, v52\n"
      "v_add_f32 v18, 0xc1800000, v52\n"
      "v_mfma_f32_16x16x32_bf16 v[44:47], a[212:215], a[52:55], v[44:47]\n"
      "v_fma_f32 v36, v36, s48, v18\n"
      "v_fma_f32 v37, v37, s48, v18\n"
      "v_mfma_f32_16x16x32_bf16 v[44:47], a[216:219], a[56:59], v[44:47]\n"
      "v_exp_f32 v28, v36\n"
      "v_mfma_f32_16x16x32_bf16 v[44:47], a[220:223], a[60:63], v[44:47]\n"
      "v_exp_f32 v29, v37\n"
      "v_mfma_f32_16x16x32_bf16 v[44:47], a[224:227], a[64:67], v[44:47]\n"
      "v_fma_f32 v38, v38, s48, v18\n"
      "v_fma_f32 v39, v39, s48, v18\n"
      "v_mfma_f32_16x16x32_bf16 v[44:47], a[228:231], a[68:71], v[44:47]\n"
      "s_waitcnt lgkmcnt(0)\n"
      "v_exp_f32 v30, v38\n"
      "v_mfma_f32_16x16x32_bf16 v[216:219], a[232:235], a[0:3], 0\n"
      "ds_read_b128 a[208:211], v4 offset:12288\n"
      "v_cvt_pk_bf16_f32 v160, v28, v29\n"
      "v_mfma_f32_16x16x32_bf16 v[216:219], a[236:239], a[4:7], v[216:219]\n"
      "ds_read_b128 a[212:215], v5 offset:12288\n"
      "v_max3_f32 v54, v40, v41, v42\n"
      "v_mfma_f32_16x16x32_bf16 v[216:219], a[240:243], a[8:11], v[216:219]\n"
      "ds_read_b128 a[216:219], v4 offset:12416\n"
      "ds_read_b128 a[220:223], v5 offset:12416\n"
      "v_mfma_f32_16x16x32_bf16 v[216:219], a[244:247], a[12:15], v[216:219]\n"
      "s_add_u32 m0, s46, 3072\n"
      "ds_read_b128 a[224:227], v4 offset:12544\n"
      "ds_read_b128 a[228:231], v5 offset:12544\n"
      "v_mfma_f32_16x16x32_bf16 v[216:219], a[248:251], a[16:19], v[216:219]\n"
      "buffer_load_dwordx4 v1, s[20:23], s37 offen offset:0 lds\n"
      "v_mfma_f32_16x16x32_bf16 v[216:219], a[252:255], a[20:23], v[216:219]\n"
      "buffer_load_dwordx4 v2, s[20:23], s37 offen offset:1024 lds\n"
      "v_mfma_f32_16x16x32_bf16 v[220:223], a[252:255], a[44:47], 0\n"
      "buffer_load_dwordx4 v3, s[20:23], s37 offen offset:2048 lds\n"
      "v_mfma_f32_16x16x32_bf16 v[220:223], a[248:251], a[40:43], v[220:223]\n"
      "v_exp_f32 v31, v39\n"
      "v_mfma_f32_16x16x32_bf16 v[220:223], a[244:247], a[36:39], v[220:223]\n"
      "v_cvt_pk_bf16_f32 v161, v30, v31\n"
      "v_max_f32 v54, v54, v43\n"
      "v_mfma_f32_16x16x32_bf16 v[220:223], a[240:243], a[32:35], v[220:223]\n"
      "v_mov_b32 v55, v54\n"
      "s_nop 1\n"
      "v_permlane32_swap_b32 v54, v55\n"
      "v_mfma_f32_16x16x32_bf16 v[220:223], a[236:239], a[28:31], v[220:223]\n"
      "v_max_f32 v54, v54, v55\n"
      "v_mov_b32 v55, v54\n"
      "v_mfma_f32_16x16x32_bf16 v[220:223], a[232:235], a[24:27], v[220:223]\n"
      "s_nop 0\n"
      "v_permlane16_swap_b32 v54, v55\n"
      "v_max_f32 v54, v54, v55\n"
      "v_mfma_f32_16x16x32_bf16 v[224:227], a[232:235], a[48:51], 0\n"
      "v_mul_f32 v54, s62, v54\n"
      "v_add_f32 v19, 0xc1800000, v54\n"
      "v_mfma_f32_16x16x32_bf16 v[224:227], a[236:239], a[52:55], v[224:227]\n"
      "v_fma_f32 v40, v40, s48, v19\n"
      "v_fma_f32 v41, v41, s48, v19\n"
      "v_mfma_f32_16x16x32_bf16 v[224:227], a[240:243], a[56:59], v[224:227]\n"
      "v_fma_f32 v42, v42, s48, v19\n"
      "v_fma_f32 v43, v43, s48, v19\n"
      "v_exp_f32 v32, v40\n"
      "v_exp_f32 v33, v41\n"
      "v_exp_f32 v34, v42\n"
      "v_exp_f32 v35, v43\n"
      "v_cvt_pk_bf16_f32 v168, v32, v33\n"
      "v_or3_b32 v22, v160, v161, v168\n"
      "v_cvt_pk_bf16_f32 v169, v34, v35\n"
      "v_max3_f32 v56, v44, v45, v46\n"
      "v_max_f32 v56, v56, v47\n"
      "v_mov_b32 v57, v56\n"
      "s_nop 1\n"
      "v_permlane32_swap_b32 v56, v57\n"
      "v_max_f32 v56, v56, v57\n"
      "v_mov_b32 v57, v56\n"
      "s_nop 1\n"
      "v_permlane16_swap_b32 v56, v57\n"
      "v_max_f32 v56, v56, v57\n"
      "v_mul_f32 v56, s62, v56\n"
      "v_add_f32 v20, 0xc1800000, v56\n"
      "v_fma_f32 v44, v44, s48, v20\n"
      "v_fma_f32 v45, v45, s48, v20\n"
      "v_fma_f32 v46, v46, s48, v20\n"
      "v_fma_f32 v47, v47, s48, v20\n"
      "v_exp_f32 v28, v44\n"
      "v_exp_f32 v29, v45\n"
      "v_exp_f32 v30, v46\n"
      "v_exp_f32 v31, v47\n"
      "v_cvt_pk_bf16_f32 v176, v28, v29\n"
      "v_or3_b32 v22, v22, v169, v176\n"
      "v_cvt_pk_bf16_f32 v177, v30, v31\n"
      "v_bitop3_b32 v22, v22, v177, s49 bitop3:0xa8\n"
      "v_cmp_ne_u32 vcc, 0, v22\n"
      "v_mfma_f32_16x16x32_bf16 v[224:227], a[244:247], a[60:63], v[224:227]\n"
      "v_add_u32 v236, s40, v14\n"
      "v_add_u32 v237, s40, v15\n"
      "v_mfma_f32_16x16x32_bf16 v[224:227], a[248:251], a[64:67], v[224:227]\n"
      "s_mov_b32 s43, s40\n"
      "v_mfma_f32_16x16x32_bf16 v[224:227], a[252:255], a[68:71], v[224:227]\n"
      "s_waitcnt lgkmcnt(0)\n"
      "s_cbranch_vccnz L_rare_Na_0\n"
      "L_rare_Na_0_ret:\n"
      "v_fma_f32 v216, v216, s48, v18\n"
      "v_mfma_f32_16x16x32_bf16 v[36:39], a[208:211], a[0:3], 0\n"
      "s_mov_b32 s40, s41\n"
      "ds_read_b128 a[232:235], v4 offset:18432\n"
      "v_fma_f32 v217, v217, s48, v18\n"
      "v_mfma_f32_16x16x32_bf16 v[36:39], a[212:215], a[4:7], v[36:39]\n"
      "s_mov_b32 s41, s42\n"
      "ds_read_b128 a[236:239], v5 offset:18432\n"
      "v_fma_f32 v218, v218, s48, v18\n"
      "v_mfma_f32_16x16x32_bf16 v[36:39], a[216:219], a[8:11], v[36:39]\n"
      "s_mov_b32 s42, s43\n"
      "ds_read_b128 a[240:243], v4 offset:18560\n"
      "ds_read_b128 a[244:247], v5 offset:18560\n"
      "v_mfma_f32_16x16x32_bf16 v[36:39], a[220:223], a[12:15], v[36:39]\n"
      "s_mov_b32 m0, s47\n"
      "ds_read_b128 a[248:251], v4 offset:18688\n"
      "ds_read_b128 a[252:255], v5 offset:18688\n"
      "v_mfma_f32_16x16x32_bf16 v[36:39], a[224:227], a[16:19], v[36:39]\n"
      "buffer_load_dwordx4 v1, s[24:27], s38 offen offset:0 lds\n"
      "v_mfma_f32_16x16x32_bf16 v[36:39], a[228:231], a[20:23], v[36:39]\n"
      "buffer_load_dwordx4 v2, s[24:27], s38 offen offset:1024 lds\n"
      "v_mfma_f32_16x16x32_bf16 v[40:43], a[228:231], a[44:47], 0\n"
      "buffer_load_dwordx4 v3, s[24:27], s38 offen offset:2048 lds\n"
      "v_mfma_f32_16x16x32_bf16 v[40:43], a[224:227], a[40:43], v[40:43]\n"
      "v_exp_f32 v32, v216\n"
      "v_mfma_f32_16x16x32_bf16 v[40:43], a[220:223], a[36:39], v[40:43]\n"
      "v_exp_f32 v33, v217\n"
      "v_mfma_f32_16x16x32_bf16 v[40:43], a[216:219], a[32:35], v[40:43]\n"
      "v_exp_f32 v34, v218\n"
      "v_mfma_f32_16x16x32_bf16 v[40:43], a[212:215], a[28:31], v[40:43]\n"
      "v_fma_f32 v219, v219, s48, v18\n"
      "v_cvt_pk_bf16_f32 v162, v32, v33\n"
      "v_mfma_f32_16x16x32_bf16 v[40:43], a[208:211], a[24:27], v[40:43]\n"
      "v_exp_f32 v35, v219\n"
      "v_mfma_f32_16x16x32_bf16 v[44:47], a[208:211], a[48:51], 0\n"
      "v_cvt_pk_bf16_f32 v163, v34, v35\n"
      "v_fma_f32 v220, v220, s48, v19\n"
      "v_mfma_f32_16x16x32_bf16 v[44:47], a[212:215], a[52:55], v[44:47]\n"
      "v_fma_f32 v221, v221, s48, v19\n"
      "v_fma_f32 v222, v222, s48, v19\n"
      "v_mfma_f32_16x16x32_bf16 v[44:47], a[216:219], a[56:59], v[44:47]\n"
      "v_fma_f32 v223, v223, s48, v19\n"
      "v_exp_f32 v28, v220\n"
      "v_exp_f32 v29, v221\n"
      "v_exp_f32 v30, v222\n"
      "v_exp_f32 v31, v223\n"
      "v_cvt_pk_bf16_f32 v170, v28, v29\n"
      "v_or3_b32 v22, v162, v163, v170\n"
      "v_cvt_pk_bf16_f32 v171, v30, v31\n"
      "v_fma_f32 v224, v224, s48, v20\n"
      "v_fma_f32 v225, v225, s48, v20\n"
      "v_fma_f32 v226, v226, s48, v20\n"
      "v_fma_f32 v227, v227, s48, v20\n"
      "v_exp_f32 v32, v224\n"
      "v_exp_f32 v33, v225\n"
      "v_exp_f32 v34, v226\n"
      "v_exp_f32 v35, v227\n"
      "v_cvt_pk_bf16_f32 v178, v32, v33\n"
      "v_or3_b32 v22, v22, v171, v178\n"
      "v_cvt_pk_bf16_f32 v179, v34, v35\n"
      "v_bitop3_b32 v22, v22, v179, s49 bitop3:0xa8\n"
      "v_cmp_ne_u32 vcc, 0, v22\n"
      "v_mfma_f32_16x16x32_bf16 v[44:47], a[220:223], a[60:63], v[44:47]\n"
      "v_mfma_f32_16x16x32_bf16 v[44:47], a[224:227], a[64:67], v[44:47]\n"
      "v_mfma_f32_16x16x32_bf16 v[44:47], a[228:231], a[68:71], v[44:47]\n"
      "s_waitcnt lgkmcnt(0)\n"
      "s_cbranch_vccnz L_rare_Na_1\n"
      "L_rare_Na_1_ret:\n"
      "v_fma_f32 v36, v36, s48, v18\n"
      "v_mfma_f32_16x16x32_bf16 v[216:219], a[232:235], a[0:3], 0\n"
      "v_exp_f32 v28, v36\n"
      "v_mfma_f32_16x16x32_bf16 v[216:219], a[236:239], a[4:7], v[216:219]\n"
      "v_fma_f32 v37, v37, s48, v18\n"
      "v_fma_f32 v38, v38, s48, v18\n"
      "v_mfma_f32_16x16x32_bf16 v[216:219], a[240:243], a[8:11], v[216:219]\n"
      "v_exp_f32 v29, v37\n"
      "v_mfma_f32_16x16x32_bf16 v[216:219], a[244:247], a[12:15], v[216:219]\n"
      "s_add_u32 m0, s47, 3072\n"
      "v_exp_f32 v30, v38\n"
      "v_mfma_f32_16x16x32_bf16 v[216:219], a[248:251], a[16:19], v[216:219]\n"
      "buffer_load_dwordx4 v1, s[24:27], s39 offen offset:0 lds\n"
      "v_mfma_f32_16x16x32_bf16 v[216:219], a[252:255], a[20:23], v[216:219]\n"
      "buffer_load_dwordx4 v2, s[24:27], s39 offen offset:1024 lds\n"
      "v_mfma_f32_16x16x32_bf16 v[220:223], a[252:255], a[44:47], 0\n"
      "buffer_load_dwordx4 v3, s[24:27], s39 offen offset:2048 lds\n"
      "v_mfma_f32_16x16x32_bf16 v[220:223], a[248:251], a[40:43], v[220:223]\n"
      "s_mov_b32 s38, s36\n"
      "v_fma_f32 v39, v39, s48, v18\n"
      "v_cvt_pk_bf16_f32 v164, v28, v29\n"
      "v_mfma_f32_16x16x32_bf16 v[220:223], a[244:247], a[36:39], v[220:223]\n"
      "s_mov_b32 s39, s37\n"
      "v_exp_f32 v31, v39\n"
      "v_mfma_f32_16x16x32_bf16 v[220:223], a[240:243], a[32:35], v[220:223]\n"
      "s_add_u32 s12, s12, 1\n"
      "v_cvt_pk_bf16_f32 v165, v30, v31\n"
      "v_fma_f32 v40, v40, s48, v19\n"
      "v_mfma_f32_16x16x32_bf16 v[220:223], a[236:239], a[28:31], v[220:223]\n"
      "s_waitcnt vmcnt(30)\n"
      "s_add_u32 s15, s13, s12\n"
      "s_and_b32 s15, s15, 15\n"
      "s_mul_i32 s15, s15, 24576\n"
      "s_lshr_b32 s16, s12, 4\n"
      "s_mul_i32 s16, s16, 393216\n"
      "s_add_u32 s15, s15, s16\n"
      "s_add_u32 s36, s15, s14\n"
      "s_add_u32 s37, s36, 3072\n"
      "v_mfma_f32_16x16x32_bf16 v[220:223], a[232:235], a[24:27], v[220:223]\n"
      "s_barrier\n"
      "ds_read_b128 a[208:211], v232 offset:0\n"
      "v_fma_f32 v41, v41, s48, v19\n"
      "v_mfma_f32_16x16x32_bf16 v[224:227], a[232:235], a[48:51], 0\n"
      "ds_read_b128 a[212:215], v233 offset:0\n"
      "v_fma_f32 v42, v42, s48, v19\n"
      "v_mfma_f32_16x16x32_bf16 v[224:227], a[236:239], a[52:55], v[224:227]\n"
      "ds_read_b128 a[216:219], v232 offset:128\n"
      "ds_read_b128 a[220:223], v233 offset:128\n"
      "v_mfma_f32_16x16x32_bf16 v[224:227], a[240:243], a[56:59], v[224:227]\n"
      "ds_read_b128 a[224:227], v232 offset:256\n"
      "ds_read_b128 a[228:231], v233 offset:256\n"
      "v_fma_f32 v43, v43, s48, v19\n"
      "v_exp_f32 v32, v40\n"
      "v_exp_f32 v33, v41\n"
      "v_exp_f32 v34, v42\n"
      "v_exp_f32 v35, v43\n"
      "v_cvt_pk_bf16_f32 v172, v32, v33\n"
      "v_or3_b32 v22, v164, v165, v172\n"
      "v_cvt_pk_bf16_f32 v173, v34, v35\n"
      "v_fma_f32 v44, v44, s48, v20\n"
      "v_fma_f32 v45, v45, s48, v20\n"
      "v_fma_f32 v46, v46, s48, v20\n"
      "v_fma_f32 v47, v47, s48, v20\n"
      "v_exp_f32 v28, v44\n"
      "v_exp_f32 v29, v45\n"
      "v_exp_f32 v30, v46\n"
      "v_exp_f32 v31, v47\n"
      "v_cvt_pk_bf16_f32 v180, v28, v29\n"
      "v_or3_b32 v22, v22, v173, v180\n"
      "v_cvt_pk_bf16_f32 v181, v30, v31\n"
      "v_bitop3_b32 v22, v22, v181, s49 bitop3:0xa8\n"
      "v_cmp_ne_u32 vcc, 0, v22\n"
      "v_mfma_f32_16x16x32_bf16 v[224:227], a[244:247], a[60:63], v[224:227]\n"
      "v_mfma_f32_16x16x32_bf16 v[224:227], a[248:251], a[64:67], v[224:227]\n"
      "v_mfma_f32_16x16x32_bf16 v[224:227], a[252:255], a[68:71], v[224:227]\n"
      "s_waitcnt lgkmcnt(0)\n"
      "s_cbranch_vccnz L_rare_Na_2\n"
      "L_rare_Na_2_ret:\n"
      "v_fma_f32 v216, v216, s48, v18\n"
      "v_fma_f32 v217, v217, s48, v18\n"
      "v_fma_f32 v218, v218, s48, v18\n"
      "v_fma_f32 v219, v219, s48, v18\n"
      "v_exp_f32 v32, v216\n"
      "v_exp_f32 v33, v217\n"
      "v_exp_f32 v34, v218\n"
      "v_exp_f32 v35, v219\n"
      "v_cvt_pk_bf16_f32 v166, v32, v33\n"
      "v_cvt_pk_bf16_f32 v167, v34, v35\n"
      "v_fma_f32 v220, v220, s48, v19\n"
      "v_fma_f32 v221, v221, s48, v19\n"
      "v_fma_f32 v222, v222, s48, v19\n"
      "v_fma_f32 v223, v223, s48, v19\n"
      "v_exp_f32 v28, v220\n"
      "v_exp_f32 v29, v221\n"
      "v_exp_f32 v30, v222\n"
      "v_exp_f32 v31, v223\n"
      "v_cvt_pk_bf16_f32 v174, v28, v29\n"
      "v_or3_b32 v22, v166, v167, v174\n"
      "v_cvt_pk_bf16_f32 v175, v30, v31\n"
      "v_fma_f32 v224, v224, s48, v20\n"
      "v_fma_f32 v225, v225, s48, v20\n"
      "v_fma_f32 v226, v226, s48, v20\n"
      "v_fma_f32 v227, v227, s48, v20\n"
      "v_exp_f32 v32, v224\n"
      "v_exp_f32 v33, v225\n"
      "v_exp_f32 v34, v226\n"
      "v_exp_f32 v35, v227\n"
      "v_cvt_pk_bf16_f32 v182, v32, v33\n"
      "v_or3_b32 v22, v22, v175, v182\n"
      "v_cvt_pk_bf16_f32 v183, v34, v35\n"
      "v_bitop3_b32 v22, v22, v183, s49 bitop3:0xa8\n"
      "v_cmp_ne_u32 vcc, 0, v22\n"
      "s_nop 1\n"
      "s_cbranch_vccnz L_rare_Na_3\n"
      "L_rare_Na_3_ret:\n"
      "s_branch L_loop_N\n"
      "L_fin_N:\n"
      "ds_read_b64_tr_b16 a[232:233], v8 offset:0\n"
      "ds_read_b64_tr_b16 a[234:235], v8 offset:6144\n"
      "ds_read_b64_tr_b16 a[236:237], v8 offset:12288\n"
      "ds_read_b64_tr_b16 a[238:239], v8 offset:18432\n"
      "ds_read_b64_tr_b16 a[240:241], v9 offset:0\n"
      "ds_read_b64_tr_b16 a[242:243], v9 offset:6144\n"
      "ds_read_b64_tr_b16 a[244:245], v9 offset:12288\n"
      "ds_read_b64_tr_b16 a[246:247], v9 offset:18432\n"
      "ds_read_b64_tr_b16 a[248:249], v10 offset:0\n"
      "ds_read_b64_tr_b16 a[250:251], v10 offset:6144\n"
      "ds_read_b64_tr_b16 a[252:253], v10 offset:12288\n"
      "ds_read_b64_tr_b16 a[254:255], v10 offset:18432\n"
      "v_smfmac_f32_16x16x64_bf16 a[192:195], v[24:27], v[188:195], v16\n"
      "v_smfmac_f32_16x16x64_bf16 a[196:199], v[24:27], v[196:203], v16\n"
      "v_smfmac_f32_16x16x64_bf16 a[200:203], v[24:27], v[204:211], v16\n"
      "s_waitcnt lgkmcnt(8)\n"
      "v_mfma_f32_16x16x32_bf16 v[64:67], v[192:195], a[236:239], v[64:67]\n"
      "v_mfma_f32_16x16x32_bf16 v[64:67], v[188:191], a[232:235], v[64:67]\n"
      "v_mfma_f32_16x16x32_bf16 v[112:115], v[196:199], a[232:235], v[112:115]\n"
      "s_nop 0\n"
      "v_mfma_f32_16x16x32_bf16 v[112:115], v[200:203], a[236:239], v[112:115]\n"
      "v_accvgpr_read_b32 v238, a192\n"
      "v_accvgpr_read_b32 v242, a196\n"
      "v_mfma_f32_16x16x32_bf16 a[96:99], v[208:211], a[236:239], a[96:99]\n"
      "v_accvgpr_read_b32 v246, a200\n"
      "v_accvgpr_read_b32 v239, a193\n"
      "v_mfma_f32_16x16x32_bf16 a[96:99], v[204:207], a[232:235], a[96:99]\n"
      "s_waitcnt lgkmcnt(4)\n"
      "ds_read_b64_tr_b16 a[232:233], v11 offset:0\n"
      "v_accvgpr_read_b32 v243, a197\n"
      "v_mfma_f32_16x16x32_bf16 v[68:71], v[192:195], a[244:247], v[68:71]\n"
      "ds_read_b64_tr_b16 a[234:235], v11 offset:6144\n"
      "ds_read_b64_tr_b16 a[236:237], v11 offset:12288\n"
      "v_mfma_f32_16x16x32_bf16 v[68:71], v[188:191], a[240:243], v[68:71]\n"
      "ds_read_b64_tr_b16 a[238:239], v11 offset:18432\n"
      "v_accvgpr_read_b32 v247, a201\n"
      "v_mfma_f32_16x16x32_bf16 v[116:119], v[196:199], a[240:243], v[116:119]\n"
      "s_lshl_b32 s63, s61, 9\n"
      "v_mfma_f32_16x16x32_bf16 v[116:119], v[200:203], a[244:247], v[116:119]\n"
      "s_add_u32 s63, s63, s65\n"
      "v_mfma_f32_16x16x32_bf16 a[100:103], v[208:211], a[244:247], a[100:103]\n"
      "s_add_u32 s63, s63, 161792\n"
      "v_and_b32 v236, 15, v0\n"
      "v_lshl_add_u32 v236, v236, 2, s63\n"
      "v_mfma_f32_16x16x32_bf16 a[100:103], v[204:207], a[240:243], a[100:103]\n"
      "s_waitcnt lgkmcnt(4)\n"
      "ds_read_b64_tr_b16 a[240:241], v8 offset:128\n"
      "v_and_b32 v237, 48, v0\n"
      "v_mfma_f32_16x16x32_bf16 v[72:75], v[192:195], a[252:255], v[72:75]\n"
      "ds_read_b64_tr_b16 a[242:243], v8 offset:6272\n"
      "ds_read_b64_tr_b16 a[244:245], v8 offset:12416\n"
      "v_mfma_f32_16x16x32_bf16 v[72:75], v[188:191], a[248:251], v[72:75]\n"
      "ds_read_b64_tr_b16 a[246:247], v8 offset:18560\n"
      "v_add_u32 v237, s63, v237\n"
      "v_mfma_f32_16x16x32_bf16 v[120:123], v[196:199], a[248:251], v[120:123]\n"
      "v_add_f32 v238, v238, v239\n"
      "v_add_f32 v242, v242, v243\n"
      "v_mfma_f32_16x16x32_bf16 v[120:123], v[200:203], a[252:255], v[120:123]\n"
      "v_add_f32 v246, v246, v247\n"
      "v_mfma_f32_16x16x32_bf16 a[104:107], v[208:211], a[252:255], a[104:107]\n"
      "s_add_u32 s82, s45, s41\n"
      "v_rcp_f32 v238, v238\n"
      "v_mfma_f32_16x16x32_bf16 a[104:107], v[204:207], a[248:251], a[104:107]\n"
      "s_waitcnt lgkmcnt(4)\n"
      "v_rcp_f32 v242, v242\n"
      "v_mfma_f32_16x16x32_bf16 v[76:79], v[192:195], a[236:239], v[76:79]\n"
      "ds_read_b64_tr_b16 a[248:249], v9 offset:128\n"
      "ds_read_b64_tr_b16 a[250:251], v9 offset:6272\n"
      "v_mfma_f32_16x16x32_bf16 v[76:79], v[188:191], a[232:235], v[76:79]\n"
      "ds_read_b64_tr_b16 a[252:253], v9 offset:12416\n"
      "ds_read_b64_tr_b16 a[254:255], v9 offset:18560\n"
      "v_mfma_f32_16x16x32_bf16 v[124:127], v[196:199], a[232:235], v[124:127]\n"
      "v_rcp_f32 v246, v246\n"
      "v_mfma_f32_16x16x32_bf16 v[124:127], v[200:203], a[236:239], v[124:127]\n"
      "v_add_u32 v232, s82, v251\n"
      "v_mfma_f32_16x16x32_bf16 a[108:111], v[208:211], a[236:239], a[108:111]\n"
      "ds_write_b32 v236, v238 offset:0\n"
      "ds_write_b32 v236, v242 offset:64\n"
      "v_mfma_f32_16x16x32_bf16 a[108:111], v[204:207], a[232:235], a[108:111]\n"
      "s_waitcnt lgkmcnt(6)\n"
      "ds_read_b64_tr_b16 a[232:233], v10 offset:128\n"
      "ds_read_b64_tr_b16 a[234:235], v10 offset:6272\n"
      "v_mfma_f32_16x16x32_bf16 v[80:83], v[192:195], a[244:247], v[80:83]\n"
      "ds_read_b64_tr_b16 a[236:237], v10 offset:12416\n"
      "ds_read_b64_tr_b16 a[238:239], v10 offset:18560\n"
      "v_mfma_f32_16x16x32_bf16 v[80:83], v[188:191], a[240:243], v[80:83]\n"
      "ds_write_b32 v236, v246 offset:128\n"
      "ds_read_b128 v[238:241], v237 offset:0\n"
      "v_mfma_f32_16x16x32_bf16 v[128:131], v[196:199], a[240:243], v[128:131]\n"
      "ds_read_b128 v[242:245], v237 offset:64\n"
      "ds_read_b128 v[246:249], v237 offset:128\n"
      "v_mfma_f32_16x16x32_bf16 v[128:131], v[200:203], a[244:247], v[128:131]\n"
      "v_add_u32 v234, s82, v17\n"
      "v_xor_b32 v233, 32, v232\n"
      "v_mfma_f32_16x16x32_bf16 a[112:115], v[208:211], a[244:247], a[112:115]\n"
      "v_xor_b32 v235, 32, v234\n"
      "v_lshlrev_b32 v23, 4, v0\n"
      "v_mfma_f32_16x16x32_bf16 a[112:115], v[204:207], a[240:243], a[112:115]\n"
      "s_waitcnt lgkmcnt(10)\n"
      "ds_read_b64_tr_b16 a[240:241], v11 offset:128\n"
      "ds_read_b64_tr_b16 a[242:243], v11 offset:6272\n"
      "v_mfma_f32_16x16x32_bf16 v[84:87], v[192:195], a[252:255], v[84:87]\n"
      "s_waitcnt lgkmcnt(2)\n"
      "ds_read_b64_tr_b16 a[244:245], v11 offset:12416\n"
      "ds_read_b64_tr_b16 a[246:247], v11 offset:18560\n"
      "v_mfma_f32_16x16x32_bf16 v[84:87], v[188:191], a[248:251], v[84:87]\n"
      "v_mul_f32 v64, v64, v238\n"
      "v_mul_f32 v65, v65, v239\n"
      "v_mfma_f32_16x16x32_bf16 v[132:135], v[196:199], a[248:251], v[132:135]\n"
      "v_mul_f32 v66, v66, v240\n"
      "v_mul_f32 v67, v67, v241\n"
      "v_mfma_f32_16x16x32_bf16 v[132:135], v[200:203], a[252:255], v[132:135]\n"
      "v_mul_f32 v68, v68, v238\n"
      "v_mul_f32 v69, v69, v239\n"
      "v_mfma_f32_16x16x32_bf16 a[116:119], v[208:211], a[252:255], a[116:119]\n"
      "v_mul_f32 v70, v70, v240\n"
      "v_mul_f32 v71, v71, v241\n"
      "v_mfma_f32_16x16x32_bf16 a[116:119], v[204:207], a[248:251], a[116:119]\n"
      "s_waitcnt lgkmcnt(4)\n"
      "ds_read_b64_tr_b16 a[248:249], v8 offset:256\n"
      "v_mul_f32 v72, v72, v238\n"
      "v_mfma_f32_16x16x32_bf16 v[88:91], v[192:195], a[236:239], v[88:91]\n"
      "ds_read_b64_tr_b16 a[250:251], v8 offset:6400\n"
      "ds_read_b64_tr_b16 a[252:253], v8 offset:12544\n"
      "v_mfma_f32_16x16x32_bf16 v[88:91], v[188:191], a[232:235], v[88:91]\n"
      "ds_read_b64_tr_b16 a[254:255], v8 offset:18688\n"
      "v_mul_f32 v73, v73, v239\n"
      "v_mfma_f32_16x16x32_bf16 v[136:139], v[196:199], a[232:235], v[136:139]\n"
      "v_mul_f32 v74, v74, v240\n"
      "v_mul_f32 v75, v75, v241\n"
      "v_mfma_f32_16x16x32_bf16 v[136:139], v[200:203], a[236:239], v[136:139]\n"
      "v_mul_f32 v76, v76, v238\n"
      "v_mul_f32 v77, v77, v239\n"
      "v_mfma_f32_16x16x32_bf16 a[120:123], v[208:211], a[236:239], a[120:123]\n"
      "v_mul_f32 v78, v78, v240\n"
      "v_mul_f32 v79, v79, v241\n"
      "v_mfma_f32_16x16x32_bf16 a[120:123], v[204:207], a[232:235], a[120:123]\n"
      "s_waitcnt lgkmcnt(4)\n"
      "ds_read_b64_tr_b16 a[232:233], v9 offset:256\n"
      "v_cvt_pk_bf16_f32 v176, v64, v68\n"
      "v_mfma_f32_16x16x32_bf16 v[92:95], v[192:195], a[244:247], v[92:95]\n"
      "ds_read_b64_tr_b16 a[234:235], v9 offset:6400\n"
      "ds_read_b64_tr_b16 a[236:237], v9 offset:12544\n"
      "v_mfma_f32_16x16x32_bf16 v[92:95], v[188:191], a[240:243], v[92:95]\n"
      "ds_read_b64_tr_b16 a[238:239], v9 offset:18688\n"
      "v_cvt_pk_bf16_f32 v177, v72, v76\n"
      "v_mfma_f32_16x16x32_bf16 v[140:143], v[196:199], a[240:243], v[140:143]\n"
      "v_cvt_pk_bf16_f32 v178, v65, v69\n"
      "v_cvt_pk_bf16_f32 v179, v73, v77\n"
      "v_mfma_f32_16x16x32_bf16 v[140:143], v[200:203], a[244:247], v[140:143]\n"
      "v_cvt_pk_bf16_f32 v180, v66, v70\n"
      "v_cvt_pk_bf16_f32 v181, v74, v78\n"
      "v_mfma_f32_16x16x32_bf16 a[124:127], v[208:211], a[244:247], a[124:127]\n"
      "v_cvt_pk_bf16_f32 v182, v67, v71\n"
      "v_cvt_pk_bf16_f32 v183, v75, v79\n"
      "v_mfma_f32_16x16x32_bf16 a[124:127], v[204:207], a[240:243], a[124:127]\n"
      "s_waitcnt lgkmcnt(4)\n"
      "ds_read_b64_tr_b16 a[240:241], v10 offset:256\n"
      "ds_read_b64_tr_b16 a[242:243], v10 offset:6400\n"
      "v_mfma_f32_16x16x32_bf16 v[96:99], v[192:195], a[252:255], v[96:99]\n"
      "ds_read_b64_tr_b16 a[244:245], v10 offset:12544\n"
      "ds_read_b64_tr_b16 a[246:247], v10 offset:18688\n"
      "v_mfma_f32_16x16x32_bf16 v[96:99], v[188:191], a[248:251], v[96:99]\n"
      "ds_write_b64 v232, v[176:177] offset:0\n"
      "ds_write_b64 v232, v[178:179] offset:128\n"
      "v_mfma_f32_16x16x32_bf16 v[144:147], v[196:199], a[248:251], v[144:147]\n"
      "ds_write_b64 v233, v[180:181] offset:256\n"
      "ds_write_b64 v233, v[182:183] offset:384\n"
      "v_mfma_f32_16x16x32_bf16 v[144:147], v[200:203], a[252:255], v[144:147]\n"
      "ds_read_b64_tr_b16 v[216:217], v234 offset:0\n"
      "ds_read_b64_tr_b16 v[218:219], v235 offset:0\n"
      "v_mfma_f32_16x16x32_bf16 a[128:131], v[208:211], a[252:255], a[128:131]\n"
      "ds_read_b64_tr_b16 v[220:221], v234 offset:1024\n"
      "ds_read_b64_tr_b16 v[222:223], v235 offset:1024\n"
      "v_mfma_f32_16x16x32_bf16 a[128:131], v[204:207], a[248:251], a[128:131]\n"
      "s_waitcnt lgkmcnt(12)\n"
      "ds_read_b64_tr_b16 a[248:249], v11 offset:256\n"
      "v_mul_f32 v112, v112, v242\n"
      "v_mfma_f32_16x16x32_bf16 v[100:103], v[192:195], a[236:239], v[100:103]\n"
      "ds_read_b64_tr_b16 a[250:251], v11 offset:6400\n"
      "ds_read_b64_tr_b16 a[252:253], v11 offset:12544\n"
      "v_mfma_f32_16x16x32_bf16 v[100:103], v[188:191], a[232:235], v[100:103]\n"
      "ds_read_b64_tr_b16 a[254:255], v11 offset:18688\n"
      "v_mul_f32 v113, v113, v243\n"
      "v_mfma_f32_16x16x32_bf16 v[148:151], v[196:199], a[232:235], v[148:151]\n"
      "v_mul_f32 v114, v114, v244\n"
      "v_mul_f32 v115, v115, v245\n"
      "v_mfma_f32_16x16x32_bf16 v[148:151], v[200:203], a[236:239], v[148:151]\n"
      "v_mul_f32 v116, v116, v242\n"
      "v_mul_f32 v117, v117, v243\n"
      "v_mfma_f32_16x16x32_bf16 a[132:135], v[208:211], a[236:239], a[132:135]\n"
      "v_mul_f32 v118, v118, v244\n"
      "v_mul_f32 v119, v119, v245\n"
      "v_mfma_f32_16x16x32_bf16 a[132:135], v[204:207], a[232:235], a[132:135]\n"
      "s_waitcnt lgkmcnt(12)\n"
      "v_mul_f32 v120, v120, v242\n"
      "v_mul_f32 v121, v121, v243\n"
      "v_mfma_f32_16x16x32_bf16 v[104:107], v[192:195], a[244:247], v[104:107]\n"
      "s_waitcnt lgkmcnt(4)\n"
      "v_mul_f32 v122, v122, v244\n"
      "v_mul_f32 v123, v123, v245\n"
      "v_mfma_f32_16x16x32_bf16 v[104:107], v[188:191], a[240:243], v[104:107]\n"
      "buffer_store_dwordx4 v[216:219], v250, s[72:75], s52 offen offset:0 sc0 sc1 nt\n"
      "v_mfma_f32_16x16x32_bf16 v[152:155], v[196:199], a[240:243], v[152:155]\n"
      "buffer_store_dwordx4 v[220:223], v250, s[72:75], s52 offen offset:3072 sc0 sc1 nt\n"
      "v_mfma_f32_16x16x32_bf16 v[152:155], v[200:203], a[244:247], v[152:155]\n"
      "v_mul_f32 v124, v124, v242\n"
      "v_mul_f32 v125, v125, v243\n"
      "v_mfma_f32_16x16x32_bf16 a[136:139], v[208:211], a[244:247], a[136:139]\n"
      "v_mul_f32 v126, v126, v244\n"
      "v_mul_f32 v127, v127, v245\n"
      "v_mfma_f32_16x16x32_bf16 a[136:139], v[204:207], a[240:243], a[136:139]\n"
      "s_waitcnt lgkmcnt(0)\n"
      "v_cvt_pk_bf16_f32 v36, v112, v116\n"
      "v_cvt_pk_bf16_f32 v37, v120, v124\n"
      "v_mfma_f32_16x16x32_bf16 v[108:111], v[192:195], a[252:255], v[108:111]\n"
      "v_cvt_pk_bf16_f32 v38, v113, v117\n"
      "v_cvt_pk_bf16_f32 v39, v121, v125\n"
      "v_mfma_f32_16x16x32_bf16 v[108:111], v[188:191], a[248:251], v[108:111]\n"
      "v_cvt_pk_bf16_f32 v40, v114, v118\n"
      "v_cvt_pk_bf16_f32 v41, v122, v126\n"
      "v_mfma_f32_16x16x32_bf16 v[156:159], v[196:199], a[248:251], v[156:159]\n"
      "v_cvt_pk_bf16_f32 v42, v115, v119\n"
      "v_cvt_pk_bf16_f32 v43, v123, v127\n"
      "v_mfma_f32_16x16x32_bf16 v[156:159], v[200:203], a[252:255], v[156:159]\n"
      "ds_write_b64 v232, v[36:37] offset:2048\n"
      "ds_write_b64 v232, v[38:39] offset:2176\n"
      "v_mfma_f32_16x16x32_bf16 a[140:143], v[208:211], a[252:255], a[140:143]\n"
      "ds_write_b64 v233, v[40:41] offset:2304\n"
      "ds_write_b64 v233, v[42:43] offset:2432\n"
      "v_mfma_f32_16x16x32_bf16 a[140:143], v[204:207], a[248:251], a[140:143]\n"
      "ds_read_b64_tr_b16 v[224:225], v234 offset:2048\n"
      "ds_read_b64_tr_b16 v[226:227], v235 offset:2048\n"
      "ds_read_b64_tr_b16 v[228:229], v234 offset:3072\n"
      "ds_read_b64_tr_b16 v[230:231], v235 offset:3072\n"
      "s_waitcnt lgkmcnt(0)\n"
      "buffer_store_dwordx4 v[224:227], v250, s[72:75], s53 offen offset:0 sc0 sc1 nt\n"
      "buffer_store_dwordx4 v[228:231], v250, s[72:75], s53 offen offset:3072 sc0 sc1 nt\n"
      "v_accvgpr_read_b32 v160, a96\n"
      "v_accvgpr_read_b32 v161, a97\n"
      "v_accvgpr_read_b32 v162, a98\n"
      "v_accvgpr_read_b32 v163, a99\n"
      "v_accvgpr_read_b32 v164, a100\n"
      "v_accvgpr_read_b32 v165, a101\n"
      "v_accvgpr_read_b32 v166, a102\n"
      "v_accvgpr_read_b32 v167, a103\n"
      "v_accvgpr_read_b32 v168, a104\n"
      "v_accvgpr_read_b32 v169, a105\n"
      "v_accvgpr_read_b32 v170, a106\n"
      "v_accvgpr_read_b32 v171, a107\n"
      "v_accvgpr_read_b32 v172, a108\n"
      "v_accvgpr_read_b32 v173, a109\n"
      "v_accvgpr_read_b32 v174, a110\n"
      "v_accvgpr_read_b32 v175, a111\n"
      "v_mul_f32 v160, v160, v246\n"
      "v_mul_f32 v161, v161, v247\n"
      "v_mul_f32 v162, v162, v248\n"
      "v_mul_f32 v163, v163, v249\n"
      "v_mul_f32 v164, v164, v246\n"
      "v_mul_f32 v165, v165, v247\n"
      "v_mul_f32 v166, v166, v248\n"
      "v_mul_f32 v167, v167, v249\n"
      "v_mul_f32 v168, v168, v246\n"
      "v_mul_f32 v169, v169, v247\n"
      "v_mul_f32 v170, v170, v248\n"
      "v_mul_f32 v171, v171, v249\n"
      "v_mul_f32 v172, v172, v246\n"
      "v_mul_f32 v173, v173, v247\n"
      "v_mul_f32 v174, v174, v248\n"
      "v_mul_f32 v175, v175, v249\n"
      "v_cvt_pk_bf16_f32 v176, v160, v164\n"
      "v_cvt_pk_bf16_f32 v177, v168, v172\n"
      "v_cvt_pk_bf16_f32 v178, v161, v165\n"
      "v_cvt_pk_bf16_f32 v179, v169, v173\n"
      "v_cvt_pk_bf16_f32 v180, v162, v166\n"
      "v_cvt_pk_bf16_f32 v181, v170, v174\n"
      "v_cvt_pk_bf16_f32 v182, v163, v167\n"
      "v_cvt_pk_bf16_f32 v183, v171, v175\n"
      "ds_write_b64 v232, v[176:177] offset:4096\n"
      "ds_write_b64 v232, v[178:179] offset:4224\n"
      "ds_write_b64 v233, v[180:181] offset:4352\n"
      "ds_write_b64 v233, v[182:183] offset:4480\n"
      "ds_read_b64_tr_b16 v[44:45], v234 offset:4096\n"
      "ds_read_b64_tr_b16 v[46:47], v235 offset:4096\n"
      "ds_read_b64_tr_b16 v[48:49], v234 offset:5120\n"
      "ds_read_b64_tr_b16 v[50:51], v235 offset:5120\n"
      "v_pk_mul_f32 v[80:81], v[80:81], v[238:239]\n"
      "v_pk_mul_f32 v[82:83], v[82:83], v[240:241]\n"
      "v_pk_mul_f32 v[84:85], v[84:85], v[238:239]\n"
      "v_pk_mul_f32 v[86:87], v[86:87], v[240:241]\n"
      "v_pk_mul_f32 v[88:89], v[88:89], v[238:239]\n"
      "v_pk_mul_f32 v[90:91], v[90:91], v[240:241]\n"
      "v_pk_mul_f32 v[92:93], v[92:93], v[238:239]\n"
      "v_pk_mul_f32 v[94:95], v[94:95], v[240:241]\n"
      "s_waitcnt lgkmcnt(0)\n"
      "buffer_store_dwordx4 v[44:47], v250, s[72:75], s54 offen offset:0 sc0 sc1 nt\n"
      "buffer_store_dwordx4 v[48:51], v250, s[72:75], s54 offen offset:3072 sc0 sc1 nt\n"
      "v_cvt_pk_bf16_f32 v176, v80, v84\n"
      "v_cvt_pk_bf16_f32 v177, v88, v92\n"
      "v_cvt_pk_bf16_f32 v178, v81, v85\n"
      "v_cvt_pk_bf16_f32 v179, v89, v93\n"
      "v_cvt_pk_bf16_f32 v180, v82, v86\n"
      "v_cvt_pk_bf16_f32 v181, v90, v94\n"
      "v_cvt_pk_bf16_f32 v182, v83, v87\n"
      "v_cvt_pk_bf16_f32 v183, v91, v95\n"
      "v_pk_mul_f32 v[96:97], v[96:97], v[238:239]\n"
      "v_pk_mul_f32 v[98:99], v[98:99], v[240:241]\n"
      "v_pk_mul_f32 v[100:101], v[100:101], v[238:239]\n"
      "v_pk_mul_f32 v[102:103], v[102:103], v[240:241]\n"
      "ds_write_b64 v232, v[176:177] offset:0\n"
      "ds_write_b64 v232, v[178:179] offset:128\n"
      "v_pk_mul_f32 v[104:105], v[104:105], v[238:239]\n"
      "v_pk_mul_f32 v[106:107], v[106:107], v[240:241]\n"
      "v_pk_mul_f32 v[108:109], v[108:109], v[238:239]\n"
      "v_pk_mul_f32 v[110:111], v[110:111], v[240:241]\n"
      "ds_write_b64 v233, v[180:181] offset:256\n"
      "ds_write_b64 v233, v[182:183] offset:384\n"
      "v_cvt_pk_bf16_f32 v184, v96, v100\n"
      "v_cvt_pk_bf16_f32 v185, v104, v108\n"
      "v_cvt_pk_bf16_f32 v186, v97, v101\n"
      "v_cvt_pk_bf16_f32 v187, v105, v109\n"
      "ds_read_b64_tr_b16 v[192:193], v234 offset:0\n"
      "ds_read_b64_tr_b16 v[194:195], v235 offset:0\n"
      "v_cvt_pk_bf16_f32 v188, v98, v102\n"
      "v_cvt_pk_bf16_f32 v189, v106, v110\n"
      "v_cvt_pk_bf16_f32 v190, v99, v103\n"
      "v_cvt_pk_bf16_f32 v191, v107, v111\n"
      "ds_read_b64_tr_b16 v[196:197], v234 offset:1024\n"
      "ds_read_b64_tr_b16 v[198:199], v235 offset:1024\n"
      "v_pk_mul_f32 v[128:129], v[128:129], v[242:243]\n"
      "v_pk_mul_f32 v[130:131], v[130:131], v[244:245]\n"
      "v_pk_mul_f32 v[132:133], v[132:133], v[242:243]\n"
      "v_pk_mul_f32 v[134:135], v[134:135], v[244:245]\n"
      "ds_write_b64 v232, v[184:185] offset:2048\n"
      "ds_write_b64 v232, v[186:187] offset:2176\n"
      "v_pk_mul_f32 v[136:137], v[136:137], v[242:243]\n"
      "v_pk_mul_f32 v[138:139], v[138:139], v[244:245]\n"
      "v_pk_mul_f32 v[140:141], v[140:141], v[242:243]\n"
      "v_pk_mul_f32 v[142:143], v[142:143], v[244:245]\n"
      "v_cvt_pk_bf16_f32 v176, v128, v132\n"
      "ds_write_b64 v233, v[188:189] offset:2304\n"
      "ds_write_b64 v233, v[190:191] offset:2432\n"
      "s_waitcnt lgkmcnt(4)\n"
      "v_cvt_pk_bf16_f32 v177, v136, v140\n"
      "buffer_store_dwordx4 v[192:195], v250, s[72:75], s52 offen offset:128 sc0 sc1 nt\n"
      "v_cvt_pk_bf16_f32 v178, v129, v133\n"
      "v_cvt_pk_bf16_f32 v179, v137, v141\n"
      "ds_read_b64_tr_b16 v[200:201], v234 offset:2048\n"
      "ds_read_b64_tr_b16 v[202:203], v235 offset:2048\n"
      "v_cvt_pk_bf16_f32 v180, v130, v134\n"
      "buffer_store_dwordx4 v[196:199], v250, s[72:75], s52 offen offset:3200 sc0 sc1 nt\n"
      "v_cvt_pk_bf16_f32 v181, v138, v142\n"
      "v_cvt_pk_bf16_f32 v182, v131, v135\n"
      "v_cvt_pk_bf16_f32 v183, v139, v143\n"
      "ds_read_b64_tr_b16 v[204:205], v234 offset:3072\n"
      "ds_read_b64_tr_b16 v[206:207], v235 offset:3072\n"
      "v_pk_mul_f32 v[144:145], v[144:145], v[242:243]\n"
      "v_pk_mul_f32 v[146:147], v[146:147], v[244:245]\n"
      "v_pk_mul_f32 v[148:149], v[148:149], v[242:243]\n"
      "v_pk_mul_f32 v[150:151], v[150:151], v[244:245]\n"
      "ds_write_b64 v232, v[176:177] offset:4096\n"
      "ds_write_b64 v232, v[178:179] offset:4224\n"
      "v_pk_mul_f32 v[152:153], v[152:153], v[242:243]\n"
      "v_pk_mul_f32 v[154:155], v[154:155], v[244:245]\n"
      "v_pk_mul_f32 v[156:157], v[156:157], v[242:243]\n"
      "v_pk_mul_f32 v[158:159], v[158:159], v[244:245]\n"
      "v_cvt_pk_bf16_f32 v184, v144, v148\n"
      "ds_write_b64 v233, v[180:181] offset:4352\n"
      "ds_write_b64 v233, v[182:183] offset:4480\n"
      "s_waitcnt lgkmcnt(4)\n"
      "v_cvt_pk_bf16_f32 v185, v152, v156\n"
      "buffer_store_dwordx4 v[200:203], v250, s[72:75], s52 offen offset:256 sc0 sc1 nt\n"
      "v_cvt_pk_bf16_f32 v186, v145, v149\n"
      "v_cvt_pk_bf16_f32 v187, v153, v157\n"
      "ds_read_b64_tr_b16 v[208:209], v234 offset:4096\n"
      "ds_read_b64_tr_b16 v[210:211], v235 offset:4096\n"
      "v_cvt_pk_bf16_f32 v188, v146, v150\n"
      "buffer_store_dwordx4 v[204:207], v250, s[72:75], s52 offen offset:3328 sc0 sc1 nt\n"
      "v_cvt_pk_bf16_f32 v189, v154, v158\n"
      "v_cvt_pk_bf16_f32 v190, v147, v151\n"
      "v_cvt_pk_bf16_f32 v191, v155, v159\n"
      "ds_read_b64_tr_b16 v[212:213], v234 offset:5120\n"
      "ds_read_b64_tr_b16 v[214:215], v235 offset:5120\n"
      "s_waitcnt lgkmcnt(8)\n"
      "s_barrier\n"
      "v_accvgpr_read_b32 v160, a112\n"
      "v_accvgpr_read_b32 v161, a113\n"
      "v_accvgpr_read_b32 v162, a114\n"
      "v_accvgpr_read_b32 v163, a115\n"
      "v_accvgpr_read_b32 v164, a116\n"
      "v_accvgpr_read_b32 v165, a117\n"
      "v_accvgpr_read_b32 v166, a118\n"
      "v_accvgpr_read_b32 v167, a119\n"
      "ds_write_b64 v232, v[184:185] offset:0\n"
      "ds_write_b64 v232, v[186:187] offset:128\n"
      "v_accvgpr_read_b32 v168, a120\n"
      "v_accvgpr_read_b32 v169, a121\n"
      "v_accvgpr_read_b32 v170, a122\n"
      "v_accvgpr_read_b32 v171, a123\n"
      "v_accvgpr_read_b32 v172, a124\n"
      "v_accvgpr_read_b32 v173, a125\n"
      "v_accvgpr_read_b32 v174, a126\n"
      "v_accvgpr_read_b32 v175, a127\n"
      "v_pk_mul_f32 v[160:161], v[160:161], v[246:247]\n"
      "ds_write_b64 v233, v[188:189] offset:256\n"
      "ds_write_b64 v233, v[190:191] offset:384\n"
      "v_pk_mul_f32 v[162:163], v[162:163], v[248:249]\n"
      "v_pk_mul_f32 v[164:165], v[164:165], v[246:247]\n"
      "v_pk_mul_f32 v[166:167], v[166:167], v[248:249]\n"
      "v_pk_mul_f32 v[168:169], v[168:169], v[246:247]\n"
      "v_pk_mul_f32 v[170:171], v[170:171], v[248:249]\n"
      "v_pk_mul_f32 v[172:173], v[172:173], v[246:247]\n"
      "v_pk_mul_f32 v[174:175], v[174:175], v[248:249]\n"
      "v_cvt_pk_bf16_f32 v176, v160, v164\n"
      "s_waitcnt lgkmcnt(4)\n"
      "ds_read_b64_tr_b16 v[192:193], v234 offset:0\n"
      "ds_read_b64_tr_b16 v[194:195], v235 offset:0\n"
      "v_cvt_pk_bf16_f32 v177, v168, v172\n"
      "buffer_store_dwordx4 v[208:211], v250, s[72:75], s53 offen offset:128 sc0 sc1 nt\n"
      "v_cvt_pk_bf16_f32 v178, v161, v165\n"
      "v_cvt_pk_bf16_f32 v179, v169, v173\n"
      "v_cvt_pk_bf16_f32 v180, v162, v166\n"
      "buffer_store_dwordx4 v[212:215], v250, s[72:75], s53 offen offset:3200 sc0 sc1 nt\n"
      "v_cvt_pk_bf16_f32 v181, v170, v174\n"
      "v_cvt_pk_bf16_f32 v182, v163, v167\n"
      "v_cvt_pk_bf16_f32 v183, v171, v175\n"
      "ds_read_b64_tr_b16 v[196:197], v234 offset:1024\n"
      "ds_read_b64_tr_b16 v[198:199], v235 offset:1024\n"
      "v_accvgpr_read_b32 v160, a128\n"
      "v_accvgpr_read_b32 v161, a129\n"
      "v_accvgpr_read_b32 v162, a130\n"
      "v_accvgpr_read_b32 v163, a131\n"
      "v_accvgpr_read_b32 v164, a132\n"
      "v_accvgpr_read_b32 v165, a133\n"
      "v_accvgpr_read_b32 v166, a134\n"
      "v_accvgpr_read_b32 v167, a135\n"
      "ds_write_b64 v232, v[176:177] offset:2048\n"
      "ds_write_b64 v232, v[178:179] offset:2176\n"
      "v_accvgpr_read_b32 v168, a136\n"
      "v_accvgpr_read_b32 v169, a137\n"
      "v_accvgpr_read_b32 v170, a138\n"
      "v_accvgpr_read_b32 v171, a139\n"
      "v_accvgpr_read_b32 v172, a140\n"
      "v_accvgpr_read_b32 v173, a141\n"
      "v_accvgpr_read_b32 v174, a142\n"
      "v_accvgpr_read_b32 v175, a143\n"
      "v_pk_mul_f32 v[160:161], v[160:161], v[246:247]\n"
      "ds_write_b64 v233, v[180:181] offset:2304\n"
      "ds_write_b64 v233, v[182:183] offset:2432\n"
      "v_pk_mul_f32 v[162:163], v[162:163], v[248:249]\n"
      "v_pk_mul_f32 v[164:165], v[164:165], v[246:247]\n"
      "v_pk_mul_f32 v[166:167], v[166:167], v[248:249]\n"
      "v_pk_mul_f32 v[168:169], v[168:169], v[246:247]\n"
      "v_pk_mul_f32 v[170:171], v[170:171], v[248:249]\n"
      "v_pk_mul_f32 v[172:173], v[172:173], v[246:247]\n"
      "v_pk_mul_f32 v[174:175], v[174:175], v[248:249]\n"
      "v_cvt_pk_bf16_f32 v184, v160, v164\n"
      "s_waitcnt lgkmcnt(4)\n"
      "ds_read_b64_tr_b16 v[200:201], v234 offset:2048\n"
      "ds_read_b64_tr_b16 v[202:203], v235 offset:2048\n"
      "v_cvt_pk_bf16_f32 v185, v168, v172\n"
      "buffer_store_dwordx4 v[192:195], v250, s[72:75], s53 offen offset:256 sc0 sc1 nt\n"
      "v_cvt_pk_bf16_f32 v186, v161, v165\n"
      "v_cvt_pk_bf16_f32 v187, v169, v173\n"
      "v_cvt_pk_bf16_f32 v188, v162, v166\n"
      "buffer_store_dwordx4 v[196:199], v250, s[72:75], s53 offen offset:3328 sc0 sc1 nt\n"
      "v_cvt_pk_bf16_f32 v189, v170, v174\n"
      "v_cvt_pk_bf16_f32 v190, v163, v167\n"
      "v_cvt_pk_bf16_f32 v191, v171, v175\n"
      "ds_read_b64_tr_b16 v[204:205], v234 offset:3072\n"
      "ds_read_b64_tr_b16 v[206:207], v235 offset:3072\n"
      "ds_write_b64 v232, v[184:185] offset:4096\n"
      "ds_write_b64 v232, v[186:187] offset:4224\n"
      "ds_write_b64 v233, v[188:189] offset:4352\n"
      "ds_write_b64 v233, v[190:191] offset:4480\n"
      "s_waitcnt lgkmcnt(4)\n"
      "buffer_store_dwordx4 v[200:203], v250, s[72:75], s54 offen offset:128 sc0 sc1 nt\n"
      "buffer_store_dwordx4 v[204:207], v250, s[72:75], s54 offen offset:3200 sc0 sc1 nt\n"
      "ds_read_b64_tr_b16 v[208:209], v234 offset:4096\n"
      "ds_read_b64_tr_b16 v[210:211], v235 offset:4096\n"
      "ds_read_b64_tr_b16 v[212:213], v234 offset:5120\n"
      "ds_read_b64_tr_b16 v[214:215], v235 offset:5120\n"
      "s_waitcnt lgkmcnt(0)\n"
      "buffer_store_dwordx4 v[208:211], v250, s[72:75], s54 offen offset:256 sc0 sc1 nt\n"
      "buffer_store_dwordx4 v[212:215], v250, s[72:75], s54 offen offset:3328 sc0 sc1 nt\n"
      "s_branch L_end\n"
      "L_rare_first:\n"
      "L_rare_Ea_0:\n"
      "s_cselect_b32 s70, 1, 0\n"
      "s_mov_b32 s60, 31\n"
      "s_nop 7\n"
      "s_nop 7\n"
      "s_swappc_b64 s[56:57], s[58:59]\n"
      "v_exp_f32 v56, v36\n"
      "v_exp_f32 v57, v37\n"
      "v_exp_f32 v58, v38\n"
      "v_exp_f32 v59, v39\n"
      "v_cvt_pk_bf16_f32 v160, v56, v57\n"
      "v_cvt_pk_bf16_f32 v161, v58, v59\n"
      "v_exp_f32 v56, v40\n"
      "v_exp_f32 v57, v41\n"
      "v_exp_f32 v58, v42\n"
      "v_exp_f32 v59, v43\n"
      "v_cvt_pk_bf16_f32 v168, v56, v57\n"
      "v_cvt_pk_bf16_f32 v169, v58, v59\n"
      "v_exp_f32 v56, v44\n"
      "v_exp_f32 v57, v45\n"
      "v_exp_f32 v58, v46\n"
      "v_exp_f32 v59, v47\n"
      "v_cvt_pk_bf16_f32 v176, v56, v57\n"
      "v_cvt_pk_bf16_f32 v177, v58, v59\n"
      "v_exp_f32 v56, v48\n"
      "v_exp_f32 v57, v49\n"
      "v_exp_f32 v58, v50\n"
      "v_exp_f32 v59, v51\n"
      "v_cvt_pk_bf16_f32 v184, v56, v57\n"
      "v_cvt_pk_bf16_f32 v185, v58, v59\n"
      "s_nop 7\n"
      "s_cmp_lg_u32 s70, 0\n"
      "s_branch L_rare_Ea_0_ret\n"
      "L_rare_Ea_1:\n"
      "s_cselect_b32 s70, 1, 0\n"
      "s_mov_b32 s60, 31\n"
      "s_nop 7\n"
      "s_nop 7\n"
      "v_swap_b32 v36, v216\n"
      "v_swap_b32 v37, v217\n"
      "v_swap_b32 v38, v218\n"
      "v_swap_b32 v39, v219\n"
      "v_swap_b32 v40, v220\n"
      "v_swap_b32 v41, v221\n"
      "v_swap_b32 v42, v222\n"
      "v_swap_b32 v43, v223\n"
      "v_swap_b32 v44, v224\n"
      "v_swap_b32 v45, v225\n"
      "v_swap_b32 v46, v226\n"
      "v_swap_b32 v47, v227\n"
      "v_swap_b32 v48, v228\n"
      "v_swap_b32 v49, v229\n"
      "v_swap_b32 v50, v230\n"
      "v_swap_b32 v51, v231\n"
      "s_swappc_b64 s[56:57], s[58:59]\n"
      "v_exp_f32 v56, v36\n"
      "v_exp_f32 v57, v37\n"
      "v_exp_f32 v58, v38\n"
      "v_exp_f32 v59, v39\n"
      "v_cvt_pk_bf16_f32 v162, v56, v57\n"
      "v_cvt_pk_bf16_f32 v163, v58, v59\n"
      "v_exp_f32 v56, v40\n"
      "v_exp_f32 v57, v41\n"
      "v_exp_f32 v58, v42\n"
      "v_exp_f32 v59, v43\n"
      "v_cvt_pk_bf16_f32 v170, v56, v57\n"
      "v_cvt_pk_bf16_f32 v171, v58, v59\n"
      "v_exp_f32 v56, v44\n"
      "v_exp_f32 v57, v45\n"
      "v_exp_f32 v58, v46\n"
      "v_exp_f32 v59, v47\n"
      "v_cvt_pk_bf16_f32 v178, v56, v57\n"
      "v_cvt_pk_bf16_f32 v179, v58, v59\n"
      "v_exp_f32 v56, v48\n"
      "v_exp_f32 v57, v49\n"
      "v_exp_f32 v58, v50\n"
      "v_exp_f32 v59, v51\n"
      "v_cvt_pk_bf16_f32 v186, v56, v57\n"
      "v_cvt_pk_bf16_f32 v187, v58, v59\n"
      "v_swap_b32 v36, v216\n"
      "v_swap_b32 v37, v217\n"
      "v_swap_b32 v38, v218\n"
      "v_swap_b32 v39, v219\n"
      "v_swap_b32 v40, v220\n"
      "v_swap_b32 v41, v221\n"
      "v_swap_b32 v42, v222\n"
      "v_swap_b32 v43, v223\n"
      "v_swap_b32 v44, v224\n"
      "v_swap_b32 v45, v225\n"
      "v_swap_b32 v46, v226\n"
      "v_swap_b32 v47, v227\n"
      "v_swap_b32 v48, v228\n"
      "v_swap_b32 v49, v229\n"
      "v_swap_b32 v50, v230\n"
      "v_swap_b32 v51, v231\n"
      "s_nop 7\n"
      "s_cmp_lg_u32 s70, 0\n"
      "s_branch L_rare_Ea_1_ret\n"
      "L_rare_Ea_2:\n"
      "s_cselect_b32 s70, 1, 0\n"
      "s_mov_b32 s60, 23\n"
      "s_nop 7\n"
      "s_nop 7\n"
      "s_swappc_b64 s[56:57], s[58:59]\n"
      "v_exp_f32 v56, v36\n"
      "v_exp_f32 v57, v37\n"
      "v_exp_f32 v58, v38\n"
      "v_exp_f32 v59, v39\n"
      "v_cvt_pk_bf16_f32 v164, v56, v57\n"
      "v_cvt_pk_bf16_f32 v165, v58, v59\n"
      "v_exp_f32 v56, v40\n"
      "v_exp_f32 v57, v41\n"
      "v_exp_f32 v58, v42\n"
      "v_exp_f32 v59, v43\n"
      "v_cvt_pk_bf16_f32 v172, v56, v57\n"
      "v_cvt_pk_bf16_f32 v173, v58, v59\n"
      "v_exp_f32 v56, v44\n"
      "v_exp_f32 v57, v45\n"
      "v_exp_f32 v58, v46\n"
      "v_exp_f32 v59, v47\n"
      "v_cvt_pk_bf16_f32 v180, v56, v57\n"
      "v_cvt_pk_bf16_f32 v181, v58, v59\n"
      "s_nop 7\n"
      "s_cmp_lg_u32 s70, 0\n"
      "s_branch L_rare_Ea_2_ret\n"
      "L_rare_Ea_3:\n"
      "s_cselect_b32 s70, 1, 0\n"
      "s_mov_b32 s60, 23\n"
      "s_nop 7\n"
      "s_nop 7\n"
      "v_swap_b32 v36, v216\n"
      "v_swap_b32 v37, v217\n"
      "v_swap_b32 v38, v218\n"
      "v_swap_b32 v39, v219\n"
      "v_swap_b32 v40, v220\n"
      "v_swap_b32 v41, v221\n"
      "v_swap_b32 v42, v222\n"
      "v_swap_b32 v43, v223\n"
      "v_swap_b32 v44, v224\n"
      "v_swap_b32 v45, v225\n"
      "v_swap_b32 v46, v226\n"
      "v_swap_b32 v47, v227\n"
      "v_swap_b32 v48, v228\n"
      "v_swap_b32 v49, v229\n"
      "v_swap_b32 v50, v230\n"
      "v_swap_b32 v51, v231\n"
      "s_swappc_b64 s[56:57], s[58:59]\n"
      "v_exp_f32 v56, v36\n"
      "v_exp_f32 v57, v37\n"
      "v_exp_f32 v58, v38\n"
      "v_exp_f32 v59, v39\n"
      "v_cvt_pk_bf16_f32 v166, v56, v57\n"
      "v_cvt_pk_bf16_f32 v167, v58, v59\n"
      "v_exp_f32 v56, v40\n"
      "v_exp_f32 v57, v41\n"
      "v_exp_f32 v58, v42\n"
      "v_exp_f32 v59, v43\n"
      "v_cvt_pk_bf16_f32 v174, v56, v57\n"
      "v_cvt_pk_bf16_f32 v175, v58, v59\n"
      "v_exp_f32 v56, v44\n"
      "v_exp_f32 v57, v45\n"
      "v_exp_f32 v58, v46\n"
      "v_exp_f32 v59, v47\n"
      "v_cvt_pk_bf16_f32 v182, v56, v57\n"
      "v_cvt_pk_bf16_f32 v183, v58, v59\n"
      "v_swap_b32 v36, v216\n"
      "v_swap_b32 v37, v217\n"
      "v_swap_b32 v38, v218\n"
      "v_swap_b32 v39, v219\n"
      "v_swap_b32 v40, v220\n"
      "v_swap_b32 v41, v221\n"
      "v_swap_b32 v42, v222\n"
      "v_swap_b32 v43, v223\n"
      "v_swap_b32 v44, v224\n"
      "v_swap_b32 v45, v225\n"
      "v_swap_b32 v46, v226\n"
      "v_swap_b32 v47, v227\n"
      "v_swap_b32 v48, v228\n"
      "v_swap_b32 v49, v229\n"
      "v_swap_b32 v50, v230\n"
      "v_swap_b32 v51, v231\n"
      "s_nop 7\n"
      "s_cmp_lg_u32 s70, 0\n"
      "s_branch L_rare_Ea_3_ret\n"
      "L_rare_Ef1_1:\n"
      "s_cselect_b32 s70, 1, 0\n"
      "s_mov_b32 s60, 47\n"
      "s_nop 7\n"
      "s_nop 7\n"
      "s_swappc_b64 s[56:57], s[58:59]\n"
      "v_exp_f32 v56, v36\n"
      "v_exp_f32 v57, v37\n"
      "v_exp_f32 v58, v38\n"
      "v_exp_f32 v59, v39\n"
      "v_cvt_pk_bf16_f32 v188, v56, v57\n"
      "v_cvt_pk_bf16_f32 v189, v58, v59\n"
      "v_exp_f32 v56, v40\n"
      "v_exp_f32 v57, v41\n"
      "v_exp_f32 v58, v42\n"
      "v_exp_f32 v59, v43\n"
      "v_cvt_pk_bf16_f32 v196, v56, v57\n"
      "v_cvt_pk_bf16_f32 v197, v58, v59\n"
      "v_exp_f32 v56, v44\n"
      "v_exp_f32 v57, v45\n"
      "v_exp_f32 v58, v46\n"
      "v_exp_f32 v59, v47\n"
      "v_cvt_pk_bf16_f32 v204, v56, v57\n"
      "v_cvt_pk_bf16_f32 v205, v58, v59\n"
      "v_exp_f32 v56, v48\n"
      "v_exp_f32 v57, v49\n"
      "v_exp_f32 v58, v50\n"
      "v_exp_f32 v59, v51\n"
      "v_cvt_pk_bf16_f32 v212, v56, v57\n"
      "v_cvt_pk_bf16_f32 v213, v58, v59\n"
      "v_exp_f32 v56, v216\n"
      "v_exp_f32 v57, v217\n"
      "v_exp_f32 v58, v218\n"
      "v_exp_f32 v59, v219\n"
      "v_cvt_pk_bf16_f32 v190, v56, v57\n"
      "v_cvt_pk_bf16_f32 v191, v58, v59\n"
      "v_exp_f32 v56, v220\n"
      "v_exp_f32 v57, v221\n"
      "v_exp_f32 v58, v222\n"
      "v_exp_f32 v59, v223\n"
      "v_cvt_pk_bf16_f32 v198, v56, v57\n"
      "v_cvt_pk_bf16_f32 v199, v58, v59\n"
      "v_exp_f32 v56, v224\n"
      "v_exp_f32 v57, v225\n"
      "v_exp_f32 v58, v226\n"
      "v_exp_f32 v59, v227\n"
      "v_cvt_pk_bf16_f32 v206, v56, v57\n"
      "v_cvt_pk_bf16_f32 v207, v58, v59\n"
      "v_exp_f32 v56, v228\n"
      "v_exp_f32 v57, v229\n"
      "v_exp_f32 v58, v230\n"
      "v_exp_f32 v59, v231\n"
      "v_cvt_pk_bf16_f32 v214, v56, v57\n"
      "v_cvt_pk_bf16_f32 v215, v58, v59\n"
      "s_nop 7\n"
      "s_cmp_lg_u32 s70, 0\n"
      "s_branch L_rare_Ef1_1_ret\n"
      "L_rare_Ef1_3:\n"
      "s_cselect_b32 s70, 1, 0\n"
      "s_mov_b32 s60, 39\n"
      "s_nop 7\n"
      "s_nop 7\n"
      "s_swappc_b64 s[56:57], s[58:59]\n"
      "v_exp_f32 v56, v36\n"
      "v_exp_f32 v57, v37\n"
      "v_exp_f32 v58, v38\n"
      "v_exp_f32 v59, v39\n"
      "v_cvt_pk_bf16_f32 v192, v56, v57\n"
      "v_cvt_pk_bf16_f32 v193, v58, v59\n"
      "v_exp_f32 v56, v40\n"
      "v_exp_f32 v57, v41\n"
      "v_exp_f32 v58, v42\n"
      "v_exp_f32 v59, v43\n"
      "v_cvt_pk_bf16_f32 v200, v56, v57\n"
      "v_cvt_pk_bf16_f32 v201, v58, v59\n"
      "v_exp_f32 v56, v44\n"
      "v_exp_f32 v57, v45\n"
      "v_exp_f32 v58, v46\n"
      "v_exp_f32 v59, v47\n"
      "v_cvt_pk_bf16_f32 v208, v56, v57\n"
      "v_cvt_pk_bf16_f32 v209, v58, v59\n"
      "v_exp_f32 v56, v216\n"
      "v_exp_f32 v57, v217\n"
      "v_exp_f32 v58, v218\n"
      "v_exp_f32 v59, v219\n"
      "v_cvt_pk_bf16_f32 v194, v56, v57\n"
      "v_cvt_pk_bf16_f32 v195, v58, v59\n"
      "v_exp_f32 v56, v220\n"
      "v_exp_f32 v57, v221\n"
      "v_exp_f32 v58, v222\n"
      "v_exp_f32 v59, v223\n"
      "v_cvt_pk_bf16_f32 v202, v56, v57\n"
      "v_cvt_pk_bf16_f32 v203, v58, v59\n"
      "v_exp_f32 v56, v224\n"
      "v_exp_f32 v57, v225\n"
      "v_exp_f32 v58, v226\n"
      "v_exp_f32 v59, v227\n"
      "v_cvt_pk_bf16_f32 v210, v56, v57\n"
      "v_cvt_pk_bf16_f32 v211, v58, v59\n"
      "s_nop 7\n"
      "s_cmp_lg_u32 s70, 0\n"
      "s_branch L_rare_Ef1_3_ret\n"
      "L_rare_Ef0_1:\n"
      "s_cselect_b32 s70, 1, 0\n"
      "s_mov_b32 s60, 47\n"
      "s_nop 7\n"
      "s_nop 7\n"
      "s_swappc_b64 s[56:57], s[58:59]\n"
      "v_exp_f32 v56, v36\n"
      "v_exp_f32 v57, v37\n"
      "v_exp_f32 v58, v38\n"
      "v_exp_f32 v59, v39\n"
      "v_cvt_pk_bf16_f32 v160, v56, v57\n"
      "v_cvt_pk_bf16_f32 v161, v58, v59\n"
      "v_exp_f32 v56, v40\n"
      "v_exp_f32 v57, v41\n"
      "v_exp_f32 v58, v42\n"
      "v_exp_f32 v59, v43\n"
      "v_cvt_pk_bf16_f32 v168, v56, v57\n"
      "v_cvt_pk_bf16_f32 v169, v58, v59\n"
      "v_exp_f32 v56, v44\n"
      "v_exp_f32 v57, v45\n"
      "v_exp_f32 v58, v46\n"
      "v_exp_f32 v59, v47\n"
      "v_cvt_pk_bf16_f32 v176, v56, v57\n"
      "v_cvt_pk_bf16_f32 v177, v58, v59\n"
      "v_exp_f32 v56, v48\n"
      "v_exp_f32 v57, v49\n"
      "v_exp_f32 v58, v50\n"
      "v_exp_f32 v59, v51\n"
      "v_cvt_pk_bf16_f32 v184, v56, v57\n"
      "v_cvt_pk_bf16_f32 v185, v58, v59\n"
      "v_exp_f32 v56, v216\n"
      "v_exp_f32 v57, v217\n"
      "v_exp_f32 v58, v218\n"
      "v_exp_f32 v59, v219\n"
      "v_cvt_pk_bf16_f32 v162, v56, v57\n"
      "v_cvt_pk_bf16_f32 v163, v58, v59\n"
      "v_exp_f32 v56, v220\n"
      "v_exp_f32 v57, v221\n"
      "v_exp_f32 v58, v222\n"
      "v_exp_f32 v59, v223\n"
      "v_cvt_pk_bf16_f32 v170, v56, v57\n"
      "v_cvt_pk_bf16_f32 v171, v58, v59\n"
      "v_exp_f32 v56, v224\n"
      "v_exp_f32 v57, v225\n"
      "v_exp_f32 v58, v226\n"
      "v_exp_f32 v59, v227\n"
      "v_cvt_pk_bf16_f32 v178, v56, v57\n"
      "v_cvt_pk_bf16_f32 v179, v58, v59\n"
      "v_exp_f32 v56, v228\n"
      "v_exp_f32 v57, v229\n"
      "v_exp_f32 v58, v230\n"
      "v_exp_f32 v59, v231\n"
      "v_cvt_pk_bf16_f32 v186, v56, v57\n"
      "v_cvt_pk_bf16_f32 v187, v58, v59\n"
      "s_nop 7\n"
      "s_cmp_lg_u32 s70, 0\n"
      "s_branch L_rare_Ef0_1_ret\n"
      "L_rare_Ef0_3:\n"
      "s_cselect_b32 s70, 1, 0\n"
      "s_mov_b32 s60, 39\n"
      "s_nop 7\n"
      "s_nop 7\n"
      "s_swappc_b64 s[56:57], s[58:59]\n"
      "v_exp_f32 v56, v36\n"
      "v_exp_f32 v57, v37\n"
      "v_exp_f32 v58, v38\n"
      "v_exp_f32 v59, v39\n"
      "v_cvt_pk_bf16_f32 v164, v56, v57\n"
      "v_cvt_pk_bf16_f32 v165, v58, v59\n"
      "v_exp_f32 v56, v40\n"
      "v_exp_f32 v57, v41\n"
      "v_exp_f32 v58, v42\n"
      "v_exp_f32 v59, v43\n"
      "v_cvt_pk_bf16_f32 v172, v56, v57\n"
      "v_cvt_pk_bf16_f32 v173, v58, v59\n"
      "v_exp_f32 v56, v44\n"
      "v_exp_f32 v57, v45\n"
      "v_exp_f32 v58, v46\n"
      "v_exp_f32 v59, v47\n"
      "v_cvt_pk_bf16_f32 v180, v56, v57\n"
      "v_cvt_pk_bf16_f32 v181, v58, v59\n"
      "v_exp_f32 v56, v216\n"
      "v_exp_f32 v57, v217\n"
      "v_exp_f32 v58, v218\n"
      "v_exp_f32 v59, v219\n"
      "v_cvt_pk_bf16_f32 v166, v56, v57\n"
      "v_cvt_pk_bf16_f32 v167, v58, v59\n"
      "v_exp_f32 v56, v220\n"
      "v_exp_f32 v57, v221\n"
      "v_exp_f32 v58, v222\n"
      "v_exp_f32 v59, v223\n"
      "v_cvt_pk_bf16_f32 v174, v56, v57\n"
      "v_cvt_pk_bf16_f32 v175, v58, v59\n"
      "v_exp_f32 v56, v224\n"
      "v_exp_f32 v57, v225\n"
      "v_exp_f32 v58, v226\n"
      "v_exp_f32 v59, v227\n"
      "v_cvt_pk_bf16_f32 v182, v56, v57\n"
      "v_cvt_pk_bf16_f32 v183, v58, v59\n"
      "s_nop 7\n"
      "s_cmp_lg_u32 s70, 0\n"
      "s_branch L_rare_Ef0_3_ret\n"
      "L_rare_Oa_0:\n"
      "s_cselect_b32 s70, 1, 0\n"
      "s_mov_b32 s60, 23\n"
      "s_nop 7\n"
      "s_nop 7\n"
      "s_swappc_b64 s[56:57], s[58:59]\n"
      "v_exp_f32 v56, v36\n"
      "v_exp_f32 v57, v37\n"
      "v_exp_f32 v58, v38\n"
      "v_exp_f32 v59, v39\n"
      "v_cvt_pk_bf16_f32 v160, v56, v57\n"
      "v_cvt_pk_bf16_f32 v161, v58, v59\n"
      "v_exp_f32 v56, v40\n"
      "v_exp_f32 v57, v41\n"
      "v_exp_f32 v58, v42\n"
      "v_exp_f32 v59, v43\n"
      "v_cvt_pk_bf16_f32 v168, v56, v57\n"
      "v_cvt_pk_bf16_f32 v169, v58, v59\n"
      "v_exp_f32 v56, v44\n"
      "v_exp_f32 v57, v45\n"
      "v_exp_f32 v58, v46\n"
      "v_exp_f32 v59, v47\n"
      "v_cvt_pk_bf16_f32 v176, v56, v57\n"
      "v_cvt_pk_bf16_f32 v177, v58, v59\n"
      "s_nop 7\n"
      "s_cmp_lg_u32 s70, 0\n"
      "s_branch L_rare_Oa_0_ret\n"
      "L_rare_Oa_1:\n"
      "s_cselect_b32 s70, 1, 0\n"
      "s_mov_b32 s60, 23\n"
      "s_nop 7\n"
      "s_nop 7\n"
      "v_swap_b32 v36, v216\n"
      "v_swap_b32 v37, v217\n"
      "v_swap_b32 v38, v218\n"
      "v_swap_b32 v39, v219\n"
      "v_swap_b32 v40, v220\n"
      "v_swap_b32 v41, v221\n"
      "v_swap_b32 v42, v222\n"
      "v_swap_b32 v43, v223\n"
      "v_swap_b32 v44, v224\n"
      "v_swap_b32 v45, v225\n"
      "v_swap_b32 v46, v226\n"
      "v_swap_b32 v47, v227\n"
      "v_swap_b32 v48, v228\n"
      "v_swap_b32 v49, v229\n"
      "v_swap_b32 v50, v230\n"
      "v_swap_b32 v51, v231\n"
      "s_swappc_b64 s[56:57], s[58:59]\n"
      "v_exp_f32 v56, v36\n"
      "v_exp_f32 v57, v37\n"
      "v_exp_f32 v58, v38\n"
      "v_exp_f32 v59, v39\n"
      "v_cvt_pk_bf16_f32 v162, v56, v57\n"
      "v_cvt_pk_bf16_f32 v163, v58, v59\n"
      "v_exp_f32 v56, v40\n"
      "v_exp_f32 v57, v41\n"
      "v_exp_f32 v58, v42\n"
      "v_exp_f32 v59, v43\n"
      "v_cvt_pk_bf16_f32 v170, v56, v57\n"
      "v_cvt_pk_bf16_f32 v171, v58, v59\n"
      "v_exp_f32 v56, v44\n"
      "v_exp_f32 v57, v45\n"
      "v_exp_f32 v58, v46\n"
      "v_exp_f32 v59, v47\n"
      "v_cvt_pk_bf16_f32 v178, v56, v57\n"
      "v_cvt_pk_bf16_f32 v179, v58, v59\n"
      "v_swap_b32 v36, v216\n"
      "v_swap_b32 v37, v217\n"
      "v_swap_b32 v38, v218\n"
      "v_swap_b32 v39, v219\n"
      "v_swap_b32 v40, v220\n"
      "v_swap_b32 v41, v221\n"
      "v_swap_b32 v42, v222\n"
      "v_swap_b32 v43, v223\n"
      "v_swap_b32 v44, v224\n"
      "v_swap_b32 v45, v225\n"
      "v_swap_b32 v46, v226\n"
      "v_swap_b32 v47, v227\n"
      "v_swap_b32 v48, v228\n"
      "v_swap_b32 v49, v229\n"
      "v_swap_b32 v50, v230\n"
      "v_swap_b32 v51, v231\n"
      "s_nop 7\n"
      "s_cmp_lg_u32 s70, 0\n"
      "s_branch L_rare_Oa_1_ret\n"
      "L_rare_Oa_2:\n"
      "s_cselect_b32 s70, 1, 0\n"
      "s_mov_b32 s60, 31\n"
      "s_nop 7\n"
      "s_nop 7\n"
      "s_swappc_b64 s[56:57], s[58:59]\n"
      "v_exp_f32 v56, v36\n"
      "v_exp_f32 v57, v37\n"
      "v_exp_f32 v58, v38\n"
      "v_exp_f32 v59, v39\n"
      "v_cvt_pk_bf16_f32 v164, v56, v57\n"
      "v_cvt_pk_bf16_f32 v165, v58, v59\n"
      "v_exp_f32 v56, v40\n"
      "v_exp_f32 v57, v41\n"
      "v_exp_f32 v58, v42\n"
      "v_exp_f32 v59, v43\n"
      "v_cvt_pk_bf16_f32 v172, v56, v57\n"
      "v_cvt_pk_bf16_f32 v173, v58, v59\n"
      "v_exp_f32 v56, v44\n"
      "v_exp_f32 v57, v45\n"
      "v_exp_f32 v58, v46\n"
      "v_exp_f32 v59, v47\n"
      "v_cvt_pk_bf16_f32 v180, v56, v57\n"
      "v_cvt_pk_bf16_f32 v181, v58, v59\n"
      "v_exp_f32 v56, v48\n"
      "v_exp_f32 v57, v49\n"
      "v_exp_f32 v58, v50\n"
      "v_exp_f32 v59, v51\n"
      "v_cvt_pk_bf16_f32 v184, v56, v57\n"
      "v_cvt_pk_bf16_f32 v185, v58, v59\n"
      "s_nop 7\n"
      "s_cmp_lg_u32 s70, 0\n"
      "s_branch L_rare_Oa_2_ret\n"
      "L_rare_Oa_3:\n"
      "s_cselect_b32 s70, 1, 0\n"
      "s_mov_b32 s60, 31\n"
      "s_nop 7\n"
      "s_nop 7\n"
      "v_swap_b32 v36, v216\n"
      "v_swap_b32 v37, v217\n"
      "v_swap_b32 v38, v218\n"
      "v_swap_b32 v39, v219\n"
      "v_swap_b32 v40, v220\n"
      "v_swap_b32 v41, v221\n"
      "v_swap_b32 v42, v222\n"
      "v_swap_b32 v43, v223\n"
      "v_swap_b32 v44, v224\n"
      "v_swap_b32 v45, v225\n"
      "v_swap_b32 v46, v226\n"
      "v_swap_b32 v47, v227\n"
      "v_swap_b32 v48, v228\n"
      "v_swap_b32 v49, v229\n"
      "v_swap_b32 v50, v230\n"
      "v_swap_b32 v51, v231\n"
      "s_swappc_b64 s[56:57], s[58:59]\n"
      "v_exp_f32 v56, v36\n"
      "v_exp_f32 v57, v37\n"
      "v_exp_f32 v58, v38\n"
      "v_exp_f32 v59, v39\n"
      "v_cvt_pk_bf16_f32 v166, v56, v57\n"
      "v_cvt_pk_bf16_f32 v167, v58, v59\n"
      "v_exp_f32 v56, v40\n"
      "v_exp_f32 v57, v41\n"
      "v_exp_f32 v58, v42\n"
      "v_exp_f32 v59, v43\n"
      "v_cvt_pk_bf16_f32 v174, v56, v57\n"
      "v_cvt_pk_bf16_f32 v175, v58, v59\n"
      "v_exp_f32 v56, v44\n"
      "v_exp_f32 v57, v45\n"
      "v_exp_f32 v58, v46\n"
      "v_exp_f32 v59, v47\n"
      "v_cvt_pk_bf16_f32 v182, v56, v57\n"
      "v_cvt_pk_bf16_f32 v183, v58, v59\n"
      "v_exp_f32 v56, v48\n"
      "v_exp_f32 v57, v49\n"
      "v_exp_f32 v58, v50\n"
      "v_exp_f32 v59, v51\n"
      "v_cvt_pk_bf16_f32 v186, v56, v57\n"
      "v_cvt_pk_bf16_f32 v187, v58, v59\n"
      "v_swap_b32 v36, v216\n"
      "v_swap_b32 v37, v217\n"
      "v_swap_b32 v38, v218\n"
      "v_swap_b32 v39, v219\n"
      "v_swap_b32 v40, v220\n"
      "v_swap_b32 v41, v221\n"
      "v_swap_b32 v42, v222\n"
      "v_swap_b32 v43, v223\n"
      "v_swap_b32 v44, v224\n"
      "v_swap_b32 v45, v225\n"
      "v_swap_b32 v46, v226\n"
      "v_swap_b32 v47, v227\n"
      "v_swap_b32 v48, v228\n"
      "v_swap_b32 v49, v229\n"
      "v_swap_b32 v50, v230\n"
      "v_swap_b32 v51, v231\n"
      "s_nop 7\n"
      "s_cmp_lg_u32 s70, 0\n"
      "s_branch L_rare_Oa_3_ret\n"
      "L_rare_Of1_1:\n"
      "s_cselect_b32 s70, 1, 0\n"
      "s_mov_b32 s60, 39\n"
      "s_nop 7\n"
      "s_nop 7\n"
      "s_swappc_b64 s[56:57], s[58:59]\n"
      "v_exp_f32 v56, v36\n"
      "v_exp_f32 v57, v37\n"
      "v_exp_f32 v58, v38\n"
      "v_exp_f32 v59, v39\n"
      "v_cvt_pk_bf16_f32 v188, v56, v57\n"
      "v_cvt_pk_bf16_f32 v189, v58, v59\n"
      "v_exp_f32 v56, v40\n"
      "v_exp_f32 v57, v41\n"
      "v_exp_f32 v58, v42\n"
      "v_exp_f32 v59, v43\n"
      "v_cvt_pk_bf16_f32 v196, v56, v57\n"
      "v_cvt_pk_bf16_f32 v197, v58, v59\n"
      "v_exp_f32 v56, v44\n"
      "v_exp_f32 v57, v45\n"
      "v_exp_f32 v58, v46\n"
      "v_exp_f32 v59, v47\n"
      "v_cvt_pk_bf16_f32 v204, v56, v57\n"
      "v_cvt_pk_bf16_f32 v205, v58, v59\n"
      "v_exp_f32 v56, v216\n"
      "v_exp_f32 v57, v217\n"
      "v_exp_f32 v58, v218\n"
      "v_exp_f32 v59, v219\n"
      "v_cvt_pk_bf16_f32 v190, v56, v57\n"
      "v_cvt_pk_bf16_f32 v191, v58, v59\n"
      "v_exp_f32 v56, v220\n"
      "v_exp_f32 v57, v221\n"
      "v_exp_f32 v58, v222\n"
      "v_exp_f32 v59, v223\n"
      "v_cvt_pk_bf16_f32 v198, v56, v57\n"
      "v_cvt_pk_bf16_f32 v199, v58, v59\n"
      "v_exp_f32 v56, v224\n"
      "v_exp_f32 v57, v225\n"
      "v_exp_f32 v58, v226\n"
      "v_exp_f32 v59, v227\n"
      "v_cvt_pk_bf16_f32 v206, v56, v57\n"
      "v_cvt_pk_bf16_f32 v207, v58, v59\n"
      "s_nop 7\n"
      "s_cmp_lg_u32 s70, 0\n"
      "s_branch L_rare_Of1_1_ret\n"
      "L_rare_Of1_3:\n"
      "s_cselect_b32 s70, 1, 0\n"
      "s_mov_b32 s60, 47\n"
      "s_nop 7\n"
      "s_nop 7\n"
      "s_swappc_b64 s[56:57], s[58:59]\n"
      "v_exp_f32 v56, v36\n"
      "v_exp_f32 v57, v37\n"
      "v_exp_f32 v58, v38\n"
      "v_exp_f32 v59, v39\n"
      "v_cvt_pk_bf16_f32 v192, v56, v57\n"
      "v_cvt_pk_bf16_f32 v193, v58, v59\n"
      "v_exp_f32 v56, v40\n"
      "v_exp_f32 v57, v41\n"
      "v_exp_f32 v58, v42\n"
      "v_exp_f32 v59, v43\n"
      "v_cvt_pk_bf16_f32 v200, v56, v57\n"
      "v_cvt_pk_bf16_f32 v201, v58, v59\n"
      "v_exp_f32 v56, v44\n"
      "v_exp_f32 v57, v45\n"
      "v_exp_f32 v58, v46\n"
      "v_exp_f32 v59, v47\n"
      "v_cvt_pk_bf16_f32 v208, v56, v57\n"
      "v_cvt_pk_bf16_f32 v209, v58, v59\n"
      "v_exp_f32 v56, v48\n"
      "v_exp_f32 v57, v49\n"
      "v_exp_f32 v58, v50\n"
      "v_exp_f32 v59, v51\n"
      "v_cvt_pk_bf16_f32 v212, v56, v57\n"
      "v_cvt_pk_bf16_f32 v213, v58, v59\n"
      "v_exp_f32 v56, v216\n"
      "v_exp_f32 v57, v217\n"
      "v_exp_f32 v58, v218\n"
      "v_exp_f32 v59, v219\n"
      "v_cvt_pk_bf16_f32 v194, v56, v57\n"
      "v_cvt_pk_bf16_f32 v195, v58, v59\n"
      "v_exp_f32 v56, v220\n"
      "v_exp_f32 v57, v221\n"
      "v_exp_f32 v58, v222\n"
      "v_exp_f32 v59, v223\n"
      "v_cvt_pk_bf16_f32 v202, v56, v57\n"
      "v_cvt_pk_bf16_f32 v203, v58, v59\n"
      "v_exp_f32 v56, v224\n"
      "v_exp_f32 v57, v225\n"
      "v_exp_f32 v58, v226\n"
      "v_exp_f32 v59, v227\n"
      "v_cvt_pk_bf16_f32 v210, v56, v57\n"
      "v_cvt_pk_bf16_f32 v211, v58, v59\n"
      "v_exp_f32 v56, v228\n"
      "v_exp_f32 v57, v229\n"
      "v_exp_f32 v58, v230\n"
      "v_exp_f32 v59, v231\n"
      "v_cvt_pk_bf16_f32 v214, v56, v57\n"
      "v_cvt_pk_bf16_f32 v215, v58, v59\n"
      "s_nop 7\n"
      "s_cmp_lg_u32 s70, 0\n"
      "s_branch L_rare_Of1_3_ret\n"
      "L_rare_Of0_1:\n"
      "s_cselect_b32 s70, 1, 0\n"
      "s_mov_b32 s60, 39\n"
      "s_nop 7\n"
      "s_nop 7\n"
      "s_swappc_b64 s[56:57], s[58:59]\n"
      "v_exp_f32 v56, v36\n"
      "v_exp_f32 v57, v37\n"
      "v_exp_f32 v58, v38\n"
      "v_exp_f32 v59, v39\n"
      "v_cvt_pk_bf16_f32 v160, v56, v57\n"
      "v_cvt_pk_bf16_f32 v161, v58, v59\n"
      "v_exp_f32 v56, v40\n"
      "v_exp_f32 v57, v41\n"
      "v_exp_f32 v58, v42\n"
      "v_exp_f32 v59, v43\n"
      "v_cvt_pk_bf16_f32 v168, v56, v57\n"
      "v_cvt_pk_bf16_f32 v169, v58, v59\n"
      "v_exp_f32 v56, v44\n"
      "v_exp_f32 v57, v45\n"
      "v_exp_f32 v58, v46\n"
      "v_exp_f32 v59, v47\n"
      "v_cvt_pk_bf16_f32 v176, v56, v57\n"
      "v_cvt_pk_bf16_f32 v177, v58, v59\n"
      "v_exp_f32 v56, v216\n"
      "v_exp_f32 v57, v217\n"
      "v_exp_f32 v58, v218\n"
      "v_exp_f32 v59, v219\n"
      "v_cvt_pk_bf16_f32 v162, v56, v57\n"
      "v_cvt_pk_bf16_f32 v163, v58, v59\n"
      "v_exp_f32 v56, v220\n"
      "v_exp_f32 v57, v221\n"
      "v_exp_f32 v58, v222\n"
      "v_exp_f32 v59, v223\n"
      "v_cvt_pk_bf16_f32 v170, v56, v57\n"
      "v_cvt_pk_bf16_f32 v171, v58, v59\n"
      "v_exp_f32 v56, v224\n"
      "v_exp_f32 v57, v225\n"
      "v_exp_f32 v58, v226\n"
      "v_exp_f32 v59, v227\n"
      "v_cvt_pk_bf16_f32 v178, v56, v57\n"
      "v_cvt_pk_bf16_f32 v179, v58, v59\n"
      "s_nop 7\n"
      "s_cmp_lg_u32 s70, 0\n"
      "s_branch L_rare_Of0_1_ret\n"
      "L_rare_Of0_3:\n"
      "s_cselect_b32 s70, 1, 0\n"
      "s_mov_b32 s60, 47\n"
      "s_nop 7\n"
      "s_nop 7\n"
      "s_swappc_b64 s[56:57], s[58:59]\n"
      "v_exp_f32 v56, v36\n"
      "v_exp_f32 v57, v37\n"
      "v_exp_f32 v58, v38\n"
      "v_exp_f32 v59, v39\n"
      "v_cvt_pk_bf16_f32 v164, v56, v57\n"
      "v_cvt_pk_bf16_f32 v165, v58, v59\n"
      "v_exp_f32 v56, v40\n"
      "v_exp_f32 v57, v41\n"
      "v_exp_f32 v58, v42\n"
      "v_exp_f32 v59, v43\n"
      "v_cvt_pk_bf16_f32 v172, v56, v57\n"
      "v_cvt_pk_bf16_f32 v173, v58, v59\n"
      "v_exp_f32 v56, v44\n"
      "v_exp_f32 v57, v45\n"
      "v_exp_f32 v58, v46\n"
      "v_exp_f32 v59, v47\n"
      "v_cvt_pk_bf16_f32 v180, v56, v57\n"
      "v_cvt_pk_bf16_f32 v181, v58, v59\n"
      "v_exp_f32 v56, v48\n"
      "v_exp_f32 v57, v49\n"
      "v_exp_f32 v58, v50\n"
      "v_exp_f32 v59, v51\n"
      "v_cvt_pk_bf16_f32 v184, v56, v57\n"
      "v_cvt_pk_bf16_f32 v185, v58, v59\n"
      "v_exp_f32 v56, v216\n"
      "v_exp_f32 v57, v217\n"
      "v_exp_f32 v58, v218\n"
      "v_exp_f32 v59, v219\n"
      "v_cvt_pk_bf16_f32 v166, v56, v57\n"
      "v_cvt_pk_bf16_f32 v167, v58, v59\n"
      "v_exp_f32 v56, v220\n"
      "v_exp_f32 v57, v221\n"
      "v_exp_f32 v58, v222\n"
      "v_exp_f32 v59, v223\n"
      "v_cvt_pk_bf16_f32 v174, v56, v57\n"
      "v_cvt_pk_bf16_f32 v175, v58, v59\n"
      "v_exp_f32 v56, v224\n"
      "v_exp_f32 v57, v225\n"
      "v_exp_f32 v58, v226\n"
      "v_exp_f32 v59, v227\n"
      "v_cvt_pk_bf16_f32 v182, v56, v57\n"
      "v_cvt_pk_bf16_f32 v183, v58, v59\n"
      "v_exp_f32 v56, v228\n"
      "v_exp_f32 v57, v229\n"
      "v_exp_f32 v58, v230\n"
      "v_exp_f32 v59, v231\n"
      "v_cvt_pk_bf16_f32 v186, v56, v57\n"
      "v_cvt_pk_bf16_f32 v187, v58, v59\n"
      "s_nop 7\n"
      "s_cmp_lg_u32 s70, 0\n"
      "s_branch L_rare_Of0_3_ret\n"
      "L_rare_Na_0:\n"
      "s_cselect_b32 s70, 1, 0\n"
      "s_mov_b32 s60, 23\n"
      "s_nop 7\n"
      "s_nop 7\n"
      "s_swappc_b64 s[56:57], s[58:59]\n"
      "v_exp_f32 v56, v36\n"
      "v_exp_f32 v57, v37\n"
      "v_exp_f32 v58, v38\n"
      "v_exp_f32 v59, v39\n"
      "v_cvt_pk_bf16_f32 v160, v56, v57\n"
      "v_cvt_pk_bf16_f32 v161, v58, v59\n"
      "v_exp_f32 v56, v40\n"
      "v_exp_f32 v57, v41\n"
      "v_exp_f32 v58, v42\n"
      "v_exp_f32 v59, v43\n"
      "v_cvt_pk_bf16_f32 v168, v56, v57\n"
      "v_cvt_pk_bf16_f32 v169, v58, v59\n"
      "v_exp_f32 v56, v44\n"
      "v_exp_f32 v57, v45\n"
      "v_exp_f32 v58, v46\n"
      "v_exp_f32 v59, v47\n"
      "v_cvt_pk_bf16_f32 v176, v56, v57\n"
      "v_cvt_pk_bf16_f32 v177, v58, v59\n"
      "s_nop 7\n"
      "s_cmp_lg_u32 s70, 0\n"
      "s_branch L_rare_Na_0_ret\n"
      "L_rare_Na_1:\n"
      "s_cselect_b32 s70, 1, 0\n"
      "s_mov_b32 s60, 23\n"
      "s_nop 7\n"
      "s_nop 7\n"
      "v_swap_b32 v36, v216\n"
      "v_swap_b32 v37, v217\n"
      "v_swap_b32 v38, v218\n"
      "v_swap_b32 v39, v219\n"
      "v_swap_b32 v40, v220\n"
      "v_swap_b32 v41, v221\n"
      "v_swap_b32 v42, v222\n"
      "v_swap_b32 v43, v223\n"
      "v_swap_b32 v44, v224\n"
      "v_swap_b32 v45, v225\n"
      "v_swap_b32 v46, v226\n"
      "v_swap_b32 v47, v227\n"
      "v_swap_b32 v48, v228\n"
      "v_swap_b32 v49, v229\n"
      "v_swap_b32 v50, v230\n"
      "v_swap_b32 v51, v231\n"
      "s_swappc_b64 s[56:57], s[58:59]\n"
      "v_exp_f32 v56, v36\n"
      "v_exp_f32 v57, v37\n"
      "v_exp_f32 v58, v38\n"
      "v_exp_f32 v59, v39\n"
      "v_cvt_pk_bf16_f32 v162, v56, v57\n"
      "v_cvt_pk_bf16_f32 v163, v58, v59\n"
      "v_exp_f32 v56, v40\n"
      "v_exp_f32 v57, v41\n"
      "v_exp_f32 v58, v42\n"
      "v_exp_f32 v59, v43\n"
      "v_cvt_pk_bf16_f32 v170, v56, v57\n"
      "v_cvt_pk_bf16_f32 v171, v58, v59\n"
      "v_exp_f32 v56, v44\n"
      "v_exp_f32 v57, v45\n"
      "v_exp_f32 v58, v46\n"
      "v_exp_f32 v59, v47\n"
      "v_cvt_pk_bf16_f32 v178, v56, v57\n"
      "v_cvt_pk_bf16_f32 v179, v58, v59\n"
      "v_swap_b32 v36, v216\n"
      "v_swap_b32 v37, v217\n"
      "v_swap_b32 v38, v218\n"
      "v_swap_b32 v39, v219\n"
      "v_swap_b32 v40, v220\n"
      "v_swap_b32 v41, v221\n"
      "v_swap_b32 v42, v222\n"
      "v_swap_b32 v43, v223\n"
      "v_swap_b32 v44, v224\n"
      "v_swap_b32 v45, v225\n"
      "v_swap_b32 v46, v226\n"
      "v_swap_b32 v47, v227\n"
      "v_swap_b32 v48, v228\n"
      "v_swap_b32 v49, v229\n"
      "v_swap_b32 v50, v230\n"
      "v_swap_b32 v51, v231\n"
      "s_nop 7\n"
      "s_cmp_lg_u32 s70, 0\n"
      "s_branch L_rare_Na_1_ret\n"
      "L_rare_Na_2:\n"
      "s_cselect_b32 s70, 1, 0\n"
      "s_mov_b32 s60, 23\n"
      "s_nop 7\n"
      "s_nop 7\n"
      "s_swappc_b64 s[56:57], s[58:59]\n"
      "v_exp_f32 v56, v36\n"
      "v_exp_f32 v57, v37\n"
      "v_exp_f32 v58, v38\n"
      "v_exp_f32 v59, v39\n"
      "v_cvt_pk_bf16_f32 v164, v56, v57\n"
      "v_cvt_pk_bf16_f32 v165, v58, v59\n"
      "v_exp_f32 v56, v40\n"
      "v_exp_f32 v57, v41\n"
      "v_exp_f32 v58, v42\n"
      "v_exp_f32 v59, v43\n"
      "v_cvt_pk_bf16_f32 v172, v56, v57\n"
      "v_cvt_pk_bf16_f32 v173, v58, v59\n"
      "v_exp_f32 v56, v44\n"
      "v_exp_f32 v57, v45\n"
      "v_exp_f32 v58, v46\n"
      "v_exp_f32 v59, v47\n"
      "v_cvt_pk_bf16_f32 v180, v56, v57\n"
      "v_cvt_pk_bf16_f32 v181, v58, v59\n"
      "s_nop 7\n"
      "s_cmp_lg_u32 s70, 0\n"
      "s_branch L_rare_Na_2_ret\n"
      "L_rare_Na_3:\n"
      "s_cselect_b32 s70, 1, 0\n"
      "s_mov_b32 s60, 23\n"
      "s_nop 7\n"
      "s_nop 7\n"
      "v_swap_b32 v36, v216\n"
      "v_swap_b32 v37, v217\n"
      "v_swap_b32 v38, v218\n"
      "v_swap_b32 v39, v219\n"
      "v_swap_b32 v40, v220\n"
      "v_swap_b32 v41, v221\n"
      "v_swap_b32 v42, v222\n"
      "v_swap_b32 v43, v223\n"
      "v_swap_b32 v44, v224\n"
      "v_swap_b32 v45, v225\n"
      "v_swap_b32 v46, v226\n"
      "v_swap_b32 v47, v227\n"
      "v_swap_b32 v48, v228\n"
      "v_swap_b32 v49, v229\n"
      "v_swap_b32 v50, v230\n"
      "v_swap_b32 v51, v231\n"
      "s_swappc_b64 s[56:57], s[58:59]\n"
      "v_exp_f32 v56, v36\n"
      "v_exp_f32 v57, v37\n"
      "v_exp_f32 v58, v38\n"
      "v_exp_f32 v59, v39\n"
      "v_cvt_pk_bf16_f32 v166, v56, v57\n"
      "v_cvt_pk_bf16_f32 v167, v58, v59\n"
      "v_exp_f32 v56, v40\n"
      "v_exp_f32 v57, v41\n"
      "v_exp_f32 v58, v42\n"
      "v_exp_f32 v59, v43\n"
      "v_cvt_pk_bf16_f32 v174, v56, v57\n"
      "v_cvt_pk_bf16_f32 v175, v58, v59\n"
      "v_exp_f32 v56, v44\n"
      "v_exp_f32 v57, v45\n"
      "v_exp_f32 v58, v46\n"
      "v_exp_f32 v59, v47\n"
      "v_cvt_pk_bf16_f32 v182, v56, v57\n"
      "v_cvt_pk_bf16_f32 v183, v58, v59\n"
      "v_swap_b32 v36, v216\n"
      "v_swap_b32 v37, v217\n"
      "v_swap_b32 v38, v218\n"
      "v_swap_b32 v39, v219\n"
      "v_swap_b32 v40, v220\n"
      "v_swap_b32 v41, v221\n"
      "v_swap_b32 v42, v222\n"
      "v_swap_b32 v43, v223\n"
      "v_swap_b32 v44, v224\n"
      "v_swap_b32 v45, v225\n"
      "v_swap_b32 v46, v226\n"
      "v_swap_b32 v47, v227\n"
      "v_swap_b32 v48, v228\n"
      "v_swap_b32 v49, v229\n"
      "v_swap_b32 v50, v230\n"
      "v_swap_b32 v51, v231\n"
      "s_nop 7\n"
      "s_cmp_lg_u32 s70, 0\n"
      "s_branch L_rare_Na_3_ret\n"
      "L_rare_Nf1_1:\n"
      "s_cselect_b32 s70, 1, 0\n"
      "s_mov_b32 s60, 39\n"
      "s_nop 7\n"
      "s_nop 7\n"
      "s_swappc_b64 s[56:57], s[58:59]\n"
      "v_exp_f32 v56, v36\n"
      "v_exp_f32 v57, v37\n"
      "v_exp_f32 v58, v38\n"
      "v_exp_f32 v59, v39\n"
      "v_cvt_pk_bf16_f32 v188, v56, v57\n"
      "v_cvt_pk_bf16_f32 v189, v58, v59\n"
      "v_exp_f32 v56, v40\n"
      "v_exp_f32 v57, v41\n"
      "v_exp_f32 v58, v42\n"
      "v_exp_f32 v59, v43\n"
      "v_cvt_pk_bf16_f32 v196, v56, v57\n"
      "v_cvt_pk_bf16_f32 v197, v58, v59\n"
      "v_exp_f32 v56, v44\n"
      "v_exp_f32 v57, v45\n"
      "v_exp_f32 v58, v46\n"
      "v_exp_f32 v59, v47\n"
      "v_cvt_pk_bf16_f32 v204, v56, v57\n"
      "v_cvt_pk_bf16_f32 v205, v58, v59\n"
      "v_exp_f32 v56, v216\n"
      "v_exp_f32 v57, v217\n"
      "v_exp_f32 v58, v218\n"
      "v_exp_f32 v59, v219\n"
      "v_cvt_pk_bf16_f32 v190, v56, v57\n"
      "v_cvt_pk_bf16_f32 v191, v58, v59\n"
      "v_exp_f32 v56, v220\n"
      "v_exp_f32 v57, v221\n"
      "v_exp_f32 v58, v222\n"
      "v_exp_f32 v59, v223\n"
      "v_cvt_pk_bf16_f32 v198, v56, v57\n"
      "v_cvt_pk_bf16_f32 v199, v58, v59\n"
      "v_exp_f32 v56, v224\n"
      "v_exp_f32 v57, v225\n"
      "v_exp_f32 v58, v226\n"
      "v_exp_f32 v59, v227\n"
      "v_cvt_pk_bf16_f32 v206, v56, v57\n"
      "v_cvt_pk_bf16_f32 v207, v58, v59\n"
      "s_nop 7\n"
      "s_cmp_lg_u32 s70, 0\n"
      "s_branch L_rare_Nf1_1_ret\n"
      "L_rare_Nf1_3:\n"
      "s_cselect_b32 s70, 1, 0\n"
      "s_mov_b32 s60, 39\n"
      "s_nop 7\n"
      "s_nop 7\n"
      "s_swappc_b64 s[56:57], s[58:59]\n"
      "v_exp_f32 v56, v36\n"
      "v_exp_f32 v57, v37\n"
      "v_exp_f32 v58, v38\n"
      "v_exp_f32 v59, v39\n"
      "v_cvt_pk_bf16_f32 v192, v56, v57\n"
      "v_cvt_pk_bf16_f32 v193, v58, v59\n"
      "v_exp_f32 v56, v40\n"
      "v_exp_f32 v57, v41\n"
      "v_exp_f32 v58, v42\n"
      "v_exp_f32 v59, v43\n"
      "v_cvt_pk_bf16_f32 v200, v56, v57\n"
      "v_cvt_pk_bf16_f32 v201, v58, v59\n"
      "v_exp_f32 v56, v44\n"
      "v_exp_f32 v57, v45\n"
      "v_exp_f32 v58, v46\n"
      "v_exp_f32 v59, v47\n"
      "v_cvt_pk_bf16_f32 v208, v56, v57\n"
      "v_cvt_pk_bf16_f32 v209, v58, v59\n"
      "v_exp_f32 v56, v216\n"
      "v_exp_f32 v57, v217\n"
      "v_exp_f32 v58, v218\n"
      "v_exp_f32 v59, v219\n"
      "v_cvt_pk_bf16_f32 v194, v56, v57\n"
      "v_cvt_pk_bf16_f32 v195, v58, v59\n"
      "v_exp_f32 v56, v220\n"
      "v_exp_f32 v57, v221\n"
      "v_exp_f32 v58, v222\n"
      "v_exp_f32 v59, v223\n"
      "v_cvt_pk_bf16_f32 v202, v56, v57\n"
      "v_cvt_pk_bf16_f32 v203, v58, v59\n"
      "v_exp_f32 v56, v224\n"
      "v_exp_f32 v57, v225\n"
      "v_exp_f32 v58, v226\n"
      "v_exp_f32 v59, v227\n"
      "v_cvt_pk_bf16_f32 v210, v56, v57\n"
      "v_cvt_pk_bf16_f32 v211, v58, v59\n"
      "s_nop 7\n"
      "s_cmp_lg_u32 s70, 0\n"
      "s_branch L_rare_Nf1_3_ret\n"
      "L_rare_Nf0_1:\n"
      "s_cselect_b32 s70, 1, 0\n"
      "s_mov_b32 s60, 39\n"
      "s_nop 7\n"
      "s_nop 7\n"
      "s_swappc_b64 s[56:57], s[58:59]\n"
      "v_exp_f32 v56, v36\n"
      "v_exp_f32 v57, v37\n"
      "v_exp_f32 v58, v38\n"
      "v_exp_f32 v59, v39\n"
      "v_cvt_pk_bf16_f32 v160, v56, v57\n"
      "v_cvt_pk_bf16_f32 v161, v58, v59\n"
      "v_exp_f32 v56, v40\n"
      "v_exp_f32 v57, v41\n"
      "v_exp_f32 v58, v42\n"
      "v_exp_f32 v59, v43\n"
      "v_cvt_pk_bf16_f32 v168, v56, v57\n"
      "v_cvt_pk_bf16_f32 v169, v58, v59\n"
      "v_exp_f32 v56, v44\n"
      "v_exp_f32 v57, v45\n"
      "v_exp_f32 v58, v46\n"
      "v_exp_f32 v59, v47\n"
      "v_cvt_pk_bf16_f32 v176, v56, v57\n"
      "v_cvt_pk_bf16_f32 v177, v58, v59\n"
      "v_exp_f32 v56, v216\n"
      "v_exp_f32 v57, v217\n"
      "v_exp_f32 v58, v218\n"
      "v_exp_f32 v59, v219\n"
      "v_cvt_pk_bf16_f32 v162, v56, v57\n"
      "v_cvt_pk_bf16_f32 v163, v58, v59\n"
      "v_exp_f32 v56, v220\n"
      "v_exp_f32 v57, v221\n"
      "v_exp_f32 v58, v222\n"
      "v_exp_f32 v59, v223\n"
      "v_cvt_pk_bf16_f32 v170, v56, v57\n"
      "v_cvt_pk_bf16_f32 v171, v58, v59\n"
      "v_exp_f32 v56, v224\n"
      "v_exp_f32 v57, v225\n"
      "v_exp_f32 v58, v226\n"
      "v_exp_f32 v59, v227\n"
      "v_cvt_pk_bf16_f32 v178, v56, v57\n"
      "v_cvt_pk_bf16_f32 v179, v58, v59\n"
      "s_nop 7\n"
      "s_cmp_lg_u32 s70, 0\n"
      "s_branch L_rare_Nf0_1_ret\n"
      "L_rare_Nf0_3:\n"
      "s_cselect_b32 s70, 1, 0\n"
      "s_mov_b32 s60, 39\n"
      "s_nop 7\n"
      "s_nop 7\n"
      "s_swappc_b64 s[56:57], s[58:59]\n"
      "v_exp_f32 v56, v36\n"
      "v_exp_f32 v57, v37\n"
      "v_exp_f32 v58, v38\n"
      "v_exp_f32 v59, v39\n"
      "v_cvt_pk_bf16_f32 v164, v56, v57\n"
      "v_cvt_pk_bf16_f32 v165, v58, v59\n"
      "v_exp_f32 v56, v40\n"
      "v_exp_f32 v57, v41\n"
      "v_exp_f32 v58, v42\n"
      "v_exp_f32 v59, v43\n"
      "v_cvt_pk_bf16_f32 v172, v56, v57\n"
      "v_cvt_pk_bf16_f32 v173, v58, v59\n"
      "v_exp_f32 v56, v44\n"
      "v_exp_f32 v57, v45\n"
      "v_exp_f32 v58, v46\n"
      "v_exp_f32 v59, v47\n"
      "v_cvt_pk_bf16_f32 v180, v56, v57\n"
      "v_cvt_pk_bf16_f32 v181, v58, v59\n"
      "v_exp_f32 v56, v216\n"
      "v_exp_f32 v57, v217\n"
      "v_exp_f32 v58, v218\n"
      "v_exp_f32 v59, v219\n"
      "v_cvt_pk_bf16_f32 v166, v56, v57\n"
      "v_cvt_pk_bf16_f32 v167, v58, v59\n"
      "v_exp_f32 v56, v220\n"
      "v_exp_f32 v57, v221\n"
      "v_exp_f32 v58, v222\n"
      "v_exp_f32 v59, v223\n"
      "v_cvt_pk_bf16_f32 v174, v56, v57\n"
      "v_cvt_pk_bf16_f32 v175, v58, v59\n"
      "v_exp_f32 v56, v224\n"
      "v_exp_f32 v57, v225\n"
      "v_exp_f32 v58, v226\n"
      "v_exp_f32 v59, v227\n"
      "v_cvt_pk_bf16_f32 v182, v56, v57\n"
      "v_cvt_pk_bf16_f32 v183, v58, v59\n"
      "s_nop 7\n"
      "s_cmp_lg_u32 s70, 0\n"
      "s_branch L_rare_Nf0_3_ret\n"
      "L_hook_E:\n"
      "s_add_u32 s66, s52, 165888\n"
      "s_add_u32 s67, s53, 165888\n"
      "s_add_u32 s68, s54, 165888\n"
      "s_add_u32 s69, s55, 165888\n"
      "s_mov_b32 m0, s87\n"
      "s_add_u32 s63, s69, s88\n"
      "buffer_load_dwordx4 v1, s[28:31], s63 offen offset:0 nt lds\n"
      "buffer_load_dwordx4 v2, s[28:31], s63 offen offset:1024 nt lds\n"
      "buffer_load_dwordx4 v3, s[28:31], s63 offen offset:2048 nt lds\n"
      "s_mov_b32 s84, s36\n"
      "s_mov_b32 s85, s38\n"
      "s_mov_b32 s20, s28\n"
      "s_mov_b32 s24, s28\n"
      "s_mov_b32 s21, s29\n"
      "s_mov_b32 s25, s29\n"
      "s_mov_b32 s22, s30\n"
      "s_mov_b32 s26, s30\n"
      "s_mov_b32 s36, s67\n"
      "s_add_u32 s37, s67, 3072\n"
      "s_mov_b32 s38, s68\n"
      "s_add_u32 s39, s68, 3072\n"
      "s_branch L_hook_E_ret\n"
      "L_hook_O:\n"
      "s_add_u32 s66, s52, 165888\n"
      "s_add_u32 s67, s53, 165888\n"
      "s_add_u32 s68, s54, 165888\n"
      "s_add_u32 s69, s55, 165888\n"
      "s_mov_b32 m0, s87\n"
      "s_add_u32 s63, s69, s88\n"
      "buffer_load_dwordx4 v1, s[28:31], s63 offen offset:0 nt lds\n"
      "buffer_load_dwordx4 v2, s[28:31], s63 offen offset:1024 nt lds\n"
      "buffer_load_dwordx4 v3, s[28:31], s63 offen offset:2048 nt lds\n"
      "s_mov_b32 s84, s36\n"
      "s_mov_b32 s85, s38\n"
      "s_mov_b32 s20, s28\n"
      "s_mov_b32 s24, s28\n"
      "s_mov_b32 s21, s29\n"
      "s_mov_b32 s25, s29\n"
      "s_mov_b32 s22, s30\n"
      "s_mov_b32 s26, s30\n"
      "s_mov_b32 s36, s67\n"
      "s_add_u32 s37, s67, 3072\n"
      "s_mov_b32 s38, s68\n"
      "s_add_u32 s39, s68, 3072\n"
      "s_branch L_hook_O_ret\n"
      "L_hook_N:\n"
      "s_add_u32 s66, s52, 165888\n"
      "s_add_u32 s67, s53, 165888\n"
      "s_add_u32 s68, s54, 165888\n"
      "s_add_u32 s69, s55, 165888\n"
      "s_mov_b32 s84, s36\n"
      "s_mov_b32 s85, s38\n"
      "s_mov_b32 s20, s28\n"
      "s_mov_b32 s24, s28\n"
      "s_mov_b32 s21, s29\n"
      "s_mov_b32 s25, s29\n"
      "s_mov_b32 s22, s30\n"
      "s_mov_b32 s26, s30\n"
      "s_mov_b32 s36, s67\n"
      "s_add_u32 s37, s67, 3072\n"
      "s_mov_b32 s38, s68\n"
      "s_add_u32 s39, s68, 3072\n"
      "s_branch L_hook_N_ret\n"
      "L_rare_common:\n"
      "s_nop 7\n"
      "s_nop 7\n"
      "s_lshl_b32 s63, s61, 9\n"
      "s_add_u32 s63, s63, s65\n"
      "s_add_u32 s63, s63, 161792\n"
      "v_and_b32 v62, 15, v0\n"
      "v_lshl_add_u32 v62, v62, 2, s63\n"
      "v_and_b32 v63, 48, v0\n"
      "v_add_u32 v63, s63, v63\n"
      "L_rare_t0:\n"
      "s_bitcmp0_b32 s60, 0\n"
      "s_cbranch_scc1 L_rare_t1\n"
      "v_max3_f32 v52, v36, v37, v38\n"
      "v_max_f32 v52, v52, v39\n"
      "s_bitcmp0_b32 s60, 5\n"
      "s_cbranch_scc1 L_rare_s0\n"
      "v_max3_f32 v52, v52, v216, v217\n"
      "v_max3_f32 v52, v52, v218, v219\n"
      "L_rare_s0:\n"
      "v_mov_b32 v53, v52\n"
      "s_nop 1\n"
      "v_permlane32_swap_b32 v52, v53\n"
      "v_max_f32 v52, v52, v53\n"
      "v_mov_b32 v53, v52\n"
      "s_nop 1\n"
      "v_permlane16_swap_b32 v52, v53\n"
      "v_max_f32 v52, v52, v53\n"
      "v_add_f32 v52, 0x41800000, v52\n"
      "v_ceil_f32 v52, v52\n"
      "v_max_f32 v52, 0, v52\n"
      "v_min_f32 v53, 0x447a0000, v52\n"
      "v_cvt_i32_f32 v53, v53\n"
      "v_sub_u32 v53, 0, v53\n"
      "v_ldexp_f32 v54, 1.0, v53\n"
      "v_sub_f32 v18, v18, v52\n"
      "v_sub_f32 v36, v36, v52\n"
      "v_sub_f32 v37, v37, v52\n"
      "v_sub_f32 v38, v38, v52\n"
      "v_sub_f32 v39, v39, v52\n"
      "s_cbranch_scc1 L_rare_u0\n"
      "v_sub_f32 v216, v216, v52\n"
      "v_sub_f32 v217, v217, v52\n"
      "v_sub_f32 v218, v218, v52\n"
      "v_sub_f32 v219, v219, v52\n"
      "L_rare_u0:\n"
      "v_lshlrev_b32 v56, 16, v160\n"
      "v_and_b32 v57, 0xffff0000, v160\n"
      "v_mul_f32 v56, v56, v54\n"
      "v_mul_f32 v57, v57, v54\n"
      "v_cvt_pk_bf16_f32 v160, v56, v57\n"
      "v_lshlrev_b32 v56, 16, v161\n"
      "v_and_b32 v57, 0xffff0000, v161\n"
      "v_mul_f32 v56, v56, v54\n"
      "v_mul_f32 v57, v57, v54\n"
      "v_cvt_pk_bf16_f32 v161, v56, v57\n"
      "v_lshlrev_b32 v56, 16, v162\n"
      "v_and_b32 v57, 0xffff0000, v162\n"
      "v_mul_f32 v56, v56, v54\n"
      "v_mul_f32 v57, v57, v54\n"
      "v_cvt_pk_bf16_f32 v162, v56, v57\n"
      "v_lshlrev_b32 v56, 16, v163\n"
      "v_and_b32 v57, 0xffff0000, v163\n"
      "v_mul_f32 v56, v56, v54\n"
      "v_mul_f32 v57, v57, v54\n"
      "v_cvt_pk_bf16_f32 v163, v56, v57\n"
      "v_lshlrev_b32 v56, 16, v164\n"
      "v_and_b32 v57, 0xffff0000, v164\n"
      "v_mul_f32 v56, v56, v54\n"
      "v_mul_f32 v57, v57, v54\n"
      "v_cvt_pk_bf16_f32 v164, v56, v57\n"
      "v_lshlrev_b32 v56, 16, v165\n"
      "v_and_b32 v57, 0xffff0000, v165\n"
      "v_mul_f32 v56, v56, v54\n"
      "v_mul_f32 v57, v57, v54\n"
      "v_cvt_pk_bf16_f32 v165, v56, v57\n"
      "v_lshlrev_b32 v56, 16, v166\n"
      "v_and_b32 v57, 0xffff0000, v166\n"
      "v_mul_f32 v56, v56, v54\n"
      "v_mul_f32 v57, v57, v54\n"
      "v_cvt_pk_bf16_f32 v166, v56, v57\n"
      "v_lshlrev_b32 v56, 16, v167\n"
      "v_and_b32 v57, 0xffff0000, v167\n"
      "v_mul_f32 v56, v56, v54\n"
      "v_mul_f32 v57, v57, v54\n"
      "v_cvt_pk_bf16_f32 v167, v56, v57\n"
      "s_bitcmp1_b32 s60, 4\n"
      "s_cbranch_scc1 L_rare_t1\n"
      "ds_write_b32 v62, v54 offset:0\n"
      "ds_read_b128 v[28:31], v63 offset:0\n"
      "v_lshlrev_b32 v56, 16, v188\n"
      "v_and_b32 v57, 0xffff0000, v188\n"
      "v_mul_f32 v56, v56, v54\n"
      "v_mul_f32 v57, v57, v54\n"
      "v_cvt_pk_bf16_f32 v188, v56, v57\n"
      "v_lshlrev_b32 v56, 16, v189\n"
      "v_and_b32 v57, 0xffff0000, v189\n"
      "v_mul_f32 v56, v56, v54\n"
      "v_mul_f32 v57, v57, v54\n"
      "v_cvt_pk_bf16_f32 v189, v56, v57\n"
      "v_lshlrev_b32 v56, 16, v190\n"
      "v_and_b32 v57, 0xffff0000, v190\n"
      "v_mul_f32 v56, v56, v54\n"
      "v_mul_f32 v57, v57, v54\n"
      "v_cvt_pk_bf16_f32 v190, v56, v57\n"
      "v_lshlrev_b32 v56, 16, v191\n"
      "v_and_b32 v57, 0xffff0000, v191\n"
      "v_mul_f32 v56, v56, v54\n"
      "v_mul_f32 v57, v57, v54\n"
      "v_cvt_pk_bf16_f32 v191, v56, v57\n"
      "v_lshlrev_b32 v56, 16, v192\n"
      "v_and_b32 v57, 0xffff0000, v192\n"
      "v_mul_f32 v56, v56, v54\n"
      "v_mul_f32 v57, v57, v54\n"
      "v_cvt_pk_bf16_f32 v192, v56, v57\n"
      "v_lshlrev_b32 v56, 16, v193\n"
      "v_and_b32 v57, 0xffff0000, v193\n"
      "v_mul_f32 v56, v56, v54\n"
      "v_mul_f32 v57, v57, v54\n"
      "v_cvt_pk_bf16_f32 v193, v56, v57\n"
      "v_lshlrev_b32 v56, 16, v194\n"
      "v_and_b32 v57, 0xffff0000, v194\n"
      "v_mul_f32 v56, v56, v54\n"
      "v_mul_f32 v57, v57, v54\n"
      "v_cvt_pk_bf16_f32 v194, v56, v57\n"
      "v_lshlrev_b32 v56, 16, v195\n"
      "v_and_b32 v57, 0xffff0000, v195\n"
      "v_mul_f32 v56, v56, v54\n"
      "v_mul_f32 v57, v57, v54\n"
      "v_cvt_pk_bf16_f32 v195, v56, v57\n"
      "s_waitcnt lgkmcnt(0)\n"
      "v_mul_f32 v64, v64, v28\n"
      "v_mul_f32 v65, v65, v29\n"
      "v_mul_f32 v66, v66, v30\n"
      "v_mul_f32 v67, v67, v31\n"
      "v_mul_f32 v68, v68, v28\n"
      "v_mul_f32 v69, v69, v29\n"
      "v_mul_f32 v70, v70, v30\n"
      "v_mul_f32 v71, v71, v31\n"
      "v_mul_f32 v72, v72, v28\n"
      "v_mul_f32 v73, v73, v29\n"
      "v_mul_f32 v74, v74, v30\n"
      "v_mul_f32 v75, v75, v31\n"
      "v_mul_f32 v76, v76, v28\n"
      "v_mul_f32 v77, v77, v29\n"
      "v_mul_f32 v78, v78, v30\n"
      "v_mul_f32 v79, v79, v31\n"
      "v_mul_f32 v80, v80, v28\n"
      "v_mul_f32 v81, v81, v29\n"
      "v_mul_f32 v82, v82, v30\n"
      "v_mul_f32 v83, v83, v31\n"
      "v_mul_f32 v84, v84, v28\n"
      "v_mul_f32 v85, v85, v29\n"
      "v_mul_f32 v86, v86, v30\n"
      "v_mul_f32 v87, v87, v31\n"
      "v_mul_f32 v88, v88, v28\n"
      "v_mul_f32 v89, v89, v29\n"
      "v_mul_f32 v90, v90, v30\n"
      "v_mul_f32 v91, v91, v31\n"
      "v_mul_f32 v92, v92, v28\n"
      "v_mul_f32 v93, v93, v29\n"
      "v_mul_f32 v94, v94, v30\n"
      "v_mul_f32 v95, v95, v31\n"
      "v_mul_f32 v96, v96, v28\n"
      "v_mul_f32 v97, v97, v29\n"
      "v_mul_f32 v98, v98, v30\n"
      "v_mul_f32 v99, v99, v31\n"
      "v_mul_f32 v100, v100, v28\n"
      "v_mul_f32 v101, v101, v29\n"
      "v_mul_f32 v102, v102, v30\n"
      "v_mul_f32 v103, v103, v31\n"
      "v_mul_f32 v104, v104, v28\n"
      "v_mul_f32 v105, v105, v29\n"
      "v_mul_f32 v106, v106, v30\n"
      "v_mul_f32 v107, v107, v31\n"
      "v_mul_f32 v108, v108, v28\n"
      "v_mul_f32 v109, v109, v29\n"
      "v_mul_f32 v110, v110, v30\n"
      "v_mul_f32 v111, v111, v31\n"
      "v_accvgpr_read_b32 v56, a192\n"
      "v_mul_f32 v56, v56, v54\n"
      "v_accvgpr_write_b32 a192, v56\n"
      "v_accvgpr_read_b32 v57, a193\n"
      "v_mul_f32 v57, v57, v54\n"
      "v_accvgpr_write_b32 a193, v57\n"
      "v_accvgpr_read_b32 v58, a194\n"
      "v_mul_f32 v58, v58, v54\n"
      "v_accvgpr_write_b32 a194, v58\n"
      "v_accvgpr_read_b32 v59, a195\n"
      "v_mul_f32 v59, v59, v54\n"
      "v_accvgpr_write_b32 a195, v59\n"
      "L_rare_t1:\n"
      "s_bitcmp0_b32 s60, 1\n"
      "s_cbranch_scc1 L_rare_t2\n"
      "v_max3_f32 v52, v40, v41, v42\n"
      "v_max_f32 v52, v52, v43\n"
      "s_bitcmp0_b32 s60, 5\n"
      "s_cbranch_scc1 L_rare_s1\n"
      "v_max3_f32 v52, v52, v220, v221\n"
      "v_max3_f32 v52, v52, v222, v223\n"
      "L_rare_s1:\n"
      "v_mov_b32 v53, v52\n"
      "s_nop 1\n"
      "v_permlane32_swap_b32 v52, v53\n"
      "v_max_f32 v52, v52, v53\n"
      "v_mov_b32 v53, v52\n"
      "s_nop 1\n"
      "v_permlane16_swap_b32 v52, v53\n"
      "v_max_f32 v52, v52, v53\n"
      "v_add_f32 v52, 0x41800000, v52\n"
      "v_ceil_f32 v52, v52\n"
      "v_max_f32 v52, 0, v52\n"
      "v_min_f32 v53, 0x447a0000, v52\n"
      "v_cvt_i32_f32 v53, v53\n"
      "v_sub_u32 v53, 0, v53\n"
      "v_ldexp_f32 v54, 1.0, v53\n"
      "v_sub_f32 v19, v19, v52\n"
      "v_sub_f32 v40, v40, v52\n"
      "v_sub_f32 v41, v41, v52\n"
      "v_sub_f32 v42, v42, v52\n"
      "v_sub_f32 v43, v43, v52\n"
      "s_cbranch_scc1 L_rare_u1\n"
      "v_sub_f32 v220, v220, v52\n"
      "v_sub_f32 v221, v221, v52\n"
      "v_sub_f32 v222, v222, v52\n"
      "v_sub_f32 v223, v223, v52\n"
      "L_rare_u1:\n"
      "v_lshlrev_b32 v56, 16, v168\n"
      "v_and_b32 v57, 0xffff0000, v168\n"
      "v_mul_f32 v56, v56, v54\n"
      "v_mul_f32 v57, v57, v54\n"
      "v_cvt_pk_bf16_f32 v168, v56, v57\n"
      "v_lshlrev_b32 v56, 16, v169\n"
      "v_and_b32 v57, 0xffff0000, v169\n"
      "v_mul_f32 v56, v56, v54\n"
      "v_mul_f32 v57, v57, v54\n"
      "v_cvt_pk_bf16_f32 v169, v56, v57\n"
      "v_lshlrev_b32 v56, 16, v170\n"
      "v_and_b32 v57, 0xffff0000, v170\n"
      "v_mul_f32 v56, v56, v54\n"
      "v_mul_f32 v57, v57, v54\n"
      "v_cvt_pk_bf16_f32 v170, v56, v57\n"
      "v_lshlrev_b32 v56, 16, v171\n"
      "v_and_b32 v57, 0xffff0000, v171\n"
      "v_mul_f32 v56, v56, v54\n"
      "v_mul_f32 v57, v57, v54\n"
      "v_cvt_pk_bf16_f32 v171, v56, v57\n"
      "v_lshlrev_b32 v56, 16, v172\n"
      "v_and_b32 v57, 0xffff0000, v172\n"
      "v_mul_f32 v56, v56, v54\n"
      "v_mul_f32 v57, v57, v54\n"
      "v_cvt_pk_bf16_f32 v172, v56, v57\n"
      "v_lshlrev_b32 v56, 16, v173\n"
      "v_and_b32 v57, 0xffff0000, v173\n"
      "v_mul_f32 v56, v56, v54\n"
      "v_mul_f32 v57, v57, v54\n"
      "v_cvt_pk_bf16_f32 v173, v56, v57\n"
      "v_lshlrev_b32 v56, 16, v174\n"
      "v_and_b32 v57, 0xffff0000, v174\n"
      "v_mul_f32 v56, v56, v54\n"
      "v_mul_f32 v57, v57, v54\n"
      "v_cvt_pk_bf16_f32 v174, v56, v57\n"
      "v_lshlrev_b32 v56, 16, v175\n"
      "v_and_b32 v57, 0xffff0000, v175\n"
      "v_mul_f32 v56, v56, v54\n"
      "v_mul_f32 v57, v57, v54\n"
      "v_cvt_pk_bf16_f32 v175, v56, v57\n"
      "s_bitcmp1_b32 s60, 4\n"
      "s_cbranch_scc1 L_rare_t2\n"
      "ds_write_b32 v62, v54 offset:0\n"
      "ds_read_b128 v[28:31], v63 offset:0\n"
      "v_lshlrev_b32 v56, 16, v196\n"
      "v_and_b32 v57, 0xffff0000, v196\n"
      "v_mul_f32 v56, v56, v54\n"
      "v_mul_f32 v57, v57, v54\n"
      "v_cvt_pk_bf16_f32 v196, v56, v57\n"
      "v_lshlrev_b32 v56, 16, v197\n"
      "v_and_b32 v57, 0xffff0000, v197\n"
      "v_mul_f32 v56, v56, v54\n"
      "v_mul_f32 v57, v57, v54\n"
      "v_cvt_pk_bf16_f32 v197, v56, v57\n"
      "v_lshlrev_b32 v56, 16, v198\n"
      "v_and_b32 v57, 0xffff0000, v198\n"
      "v_mul_f32 v56, v56, v54\n"
      "v_mul_f32 v57, v57, v54\n"
      "v_cvt_pk_bf16_f32 v198, v56, v57\n"
      "v_lshlrev_b32 v56, 16, v199\n"
      "v_and_b32 v57, 0xffff0000, v199\n"
      "v_mul_f32 v56, v56, v54\n"
      "v_mul_f32 v57, v57, v54\n"
      "v_cvt_pk_bf16_f32 v199, v56, v57\n"
      "v_lshlrev_b32 v56, 16, v200\n"
      "v_and_b32 v57, 0xffff0000, v200\n"
      "v_mul_f32 v56, v56, v54\n"
      "v_mul_f32 v57, v57, v54\n"
      "v_cvt_pk_bf16_f32 v200, v56, v57\n"
      "v_lshlrev_b32 v56, 16, v201\n"
      "v_and_b32 v57, 0xffff0000, v201\n"
      "v_mul_f32 v56, v56, v54\n"
      "v_mul_f32 v57, v57, v54\n"
      "v_cvt_pk_bf16_f32 v201, v56, v57\n"
      "v_lshlrev_b32 v56, 16, v202\n"
      "v_and_b32 v57, 0xffff0000, v202\n"
      "v_mul_f32 v56, v56, v54\n"
      "v_mul_f32 v57, v57, v54\n"
      "v_cvt_pk_bf16_f32 v202, v56, v57\n"
      "v_lshlrev_b32 v56, 16, v203\n"
      "v_and_b32 v57, 0xffff0000, v203\n"
      "v_mul_f32 v56, v56, v54\n"
      "v_mul_f32 v57, v57, v54\n"
      "v_cvt_pk_bf16_f32 v203, v56, v57\n"
      "s_waitcnt lgkmcnt(0)\n"
      "v_mul_f32 v112, v112, v28\n"
      "v_mul_f32 v113, v113, v29\n"
      "v_mul_f32 v114, v114, v30\n"
      "v_mul_f32 v115, v115, v31\n"
      "v_mul_f32 v116, v116, v28\n"
      "v_mul_f32 v117, v117, v29\n"
      "v_mul_f32 v118, v118, v30\n"
      "v_mul_f32 v119, v119, v31\n"
      "v_mul_f32 v120, v120, v28\n"
      "v_mul_f32 v121, v121, v29\n"
      "v_mul_f32 v122, v122, v30\n"
      "v_mul_f32 v123, v123, v31\n"
      "v_mul_f32 v124, v124, v28\n"
      "v_mul_f32 v125, v125, v29\n"
      "v_mul_f32 v126, v126, v30\n"
      "v_mul_f32 v127, v127, v31\n"
      "v_mul_f32 v128, v128, v28\n"
      "v_mul_f32 v129, v129, v29\n"
      "v_mul_f32 v130, v130, v30\n"
      "v_mul_f32 v131, v131, v31\n"
      "v_mul_f32 v132, v132, v28\n"
      "v_mul_f32 v133, v133, v29\n"
      "v_mul_f32 v134, v134, v30\n"
      "v_mul_f32 v135, v135, v31\n"
      "v_mul_f32 v136, v136, v28\n"
      "v_mul_f32 v137, v137, v29\n"
      "v_mul_f32 v138, v138, v30\n"
      "v_mul_f32 v139, v139, v31\n"
      "v_mul_f32 v140, v140, v28\n"
      "v_mul_f32 v141, v141, v29\n"
      "v_mul_f32 v142, v142, v30\n"
      "v_mul_f32 v143, v143, v31\n"
      "v_mul_f32 v144, v144, v28\n"
      "v_mul_f32 v145, v145, v29\n"
      "v_mul_f32 v146, v146, v30\n"
      "v_mul_f32 v147, v147, v31\n"
      "v_mul_f32 v148, v148, v28\n"
      "v_mul_f32 v149, v149, v29\n"
      "v_mul_f32 v150, v150, v30\n"
      "v_mul_f32 v151, v151, v31\n"
      "v_mul_f32 v152, v152, v28\n"
      "v_mul_f32 v153, v153, v29\n"
      "v_mul_f32 v154, v154, v30\n"
      "v_mul_f32 v155, v155, v31\n"
      "v_mul_f32 v156, v156, v28\n"
      "v_mul_f32 v157, v157, v29\n"
      "v_mul_f32 v158, v158, v30\n"
      "v_mul_f32 v159, v159, v31\n"
      "v_accvgpr_read_b32 v56, a196\n"
      "v_mul_f32 v56, v56, v54\n"
      "v_accvgpr_write_b32 a196, v56\n"
      "v_accvgpr_read_b32 v57, a197\n"
      "v_mul_f32 v57, v57, v54\n"
      "v_accvgpr_write_b32 a197, v57\n"
      "v_accvgpr_read_b32 v58, a198\n"
      "v_mul_f32 v58, v58, v54\n"
      "v_accvgpr_write_b32 a198, v58\n"
      "v_accvgpr_read_b32 v59, a199\n"
      "v_mul_f32 v59, v59, v54\n"
      "v_accvgpr_write_b32 a199, v59\n"
      "L_rare_t2:\n"
      "s_bitcmp0_b32 s60, 2\n"
      "s_cbranch_scc1 L_rare_t3\n"
      "v_max3_f32 v52, v44, v45, v46\n"
      "v_max_f32 v52, v52, v47\n"
      "s_bitcmp0_b32 s60, 5\n"
      "s_cbranch_scc1 L_rare_s2\n"
      "v_max3_f32 v52, v52, v224, v225\n"
      "v_max3_f32 v52, v52, v226, v227\n"
      "L_rare_s2:\n"
      "v_mov_b32 v53, v52\n"
      "s_nop 1\n"
      "v_permlane32_swap_b32 v52, v53\n"
      "v_max_f32 v52, v52, v53\n"
      "v_mov_b32 v53, v52\n"
      "s_nop 1\n"
      "v_permlane16_swap_b32 v52, v53\n"
      "v_max_f32 v52, v52, v53\n"
      "v_add_f32 v52, 0x41800000, v52\n"
      "v_ceil_f32 v52, v52\n"
      "v_max_f32 v52, 0, v52\n"
      "v_min_f32 v53, 0x447a0000, v52\n"
      "v_cvt_i32_f32 v53, v53\n"
      "v_sub_u32 v53, 0, v53\n"
      "v_ldexp_f32 v54, 1.0, v53\n"
      "v_sub_f32 v20, v20, v52\n"
      "v_sub_f32 v44, v44, v52\n"
      "v_sub_f32 v45, v45, v52\n"
      "v_sub_f32 v46, v46, v52\n"
      "v_sub_f32 v47, v47, v52\n"
      "s_cbranch_scc1 L_rare_u2\n"
      "v_sub_f32 v224, v224, v52\n"
      "v_sub_f32 v225, v225, v52\n"
      "v_sub_f32 v226, v226, v52\n"
      "v_sub_f32 v227, v227, v52\n"
      "L_rare_u2:\n"
      "v_lshlrev_b32 v56, 16, v176\n"
      "v_and_b32 v57, 0xffff0000, v176\n"
      "v_mul_f32 v56, v56, v54\n"
      "v_mul_f32 v57, v57, v54\n"
      "v_cvt_pk_bf16_f32 v176, v56, v57\n"
      "v_lshlrev_b32 v56, 16, v177\n"
      "v_and_b32 v57, 0xffff0000, v177\n"
      "v_mul_f32 v56, v56, v54\n"
      "v_mul_f32 v57, v57, v54\n"
      "v_cvt_pk_bf16_f32 v177, v56, v57\n"
      "v_lshlrev_b32 v56, 16, v178\n"
      "v_and_b32 v57, 0xffff0000, v178\n"
      "v_mul_f32 v56, v56, v54\n"
      "v_mul_f32 v57, v57, v54\n"
      "v_cvt_pk_bf16_f32 v178, v56, v57\n"
      "v_lshlrev_b32 v56, 16, v179\n"
      "v_and_b32 v57, 0xffff0000, v179\n"
      "v_mul_f32 v56, v56, v54\n"
      "v_mul_f32 v57, v57, v54\n"
      "v_cvt_pk_bf16_f32 v179, v56, v57\n"
      "v_lshlrev_b32 v56, 16, v180\n"
      "v_and_b32 v57, 0xffff0000, v180\n"
      "v_mul_f32 v56, v56, v54\n"
      "v_mul_f32 v57, v57, v54\n"
      "v_cvt_pk_bf16_f32 v180, v56, v57\n"
      "v_lshlrev_b32 v56, 16, v181\n"
      "v_and_b32 v57, 0xffff0000, v181\n"
      "v_mul_f32 v56, v56, v54\n"
      "v_mul_f32 v57, v57, v54\n"
      "v_cvt_pk_bf16_f32 v181, v56, v57\n"
      "v_lshlrev_b32 v56, 16, v182\n"
      "v_and_b32 v57, 0xffff0000, v182\n"
      "v_mul_f32 v56, v56, v54\n"
      "v_mul_f32 v57, v57, v54\n"
      "v_cvt_pk_bf16_f32 v182, v56, v57\n"
      "v_lshlrev_b32 v56, 16, v183\n"
      "v_and_b32 v57, 0xffff0000, v183\n"
      "v_mul_f32 v56, v56, v54\n"
      "v_mul_f32 v57, v57, v54\n"
      "v_cvt_pk_bf16_f32 v183, v56, v57\n"
      "s_bitcmp1_b32 s60, 4\n"
      "s_cbranch_scc1 L_rare_t3\n"
      "ds_write_b32 v62, v54 offset:0\n"
      "ds_read_b128 v[28:31], v63 offset:0\n"
      "v_lshlrev_b32 v56, 16, v204\n"
      "v_and_b32 v57, 0xffff0000, v204\n"
      "v_mul_f32 v56, v56, v54\n"
      "v_mul_f32 v57, v57, v54\n"
      "v_cvt_pk_bf16_f32 v204, v56, v57\n"
      "v_lshlrev_b32 v56, 16, v205\n"
      "v_and_b32 v57, 0xffff0000, v205\n"
      "v_mul_f32 v56, v56, v54\n"
      "v_mul_f32 v57, v57, v54\n"
      "v_cvt_pk_bf16_f32 v205, v56, v57\n"
      "v_lshlrev_b32 v56, 16, v206\n"
      "v_and_b32 v57, 0xffff0000, v206\n"
      "v_mul_f32 v56, v56, v54\n"
      "v_mul_f32 v57, v57, v54\n"
      "v_cvt_pk_bf16_f32 v206, v56, v57\n"
      "v_lshlrev_b32 v56, 16, v207\n"
      "v_and_b32 v57, 0xffff0000, v207\n"
      "v_mul_f32 v56, v56, v54\n"
      "v_mul_f32 v57, v57, v54\n"
      "v_cvt_pk_bf16_f32 v207, v56, v57\n"
      "v_lshlrev_b32 v56, 16, v208\n"
      "v_and_b32 v57, 0xffff0000, v208\n"
      "v_mul_f32 v56, v56, v54\n"
      "v_mul_f32 v57, v57, v54\n"
      "v_cvt_pk_bf16_f32 v208, v56, v57\n"
      "v_lshlrev_b32 v56, 16, v209\n"
      "v_and_b32 v57, 0xffff0000, v209\n"
      "v_mul_f32 v56, v56, v54\n"
      "v_mul_f32 v57, v57, v54\n"
      "v_cvt_pk_bf16_f32 v209, v56, v57\n"
      "v_lshlrev_b32 v56, 16, v210\n"
      "v_and_b32 v57, 0xffff0000, v210\n"
      "v_mul_f32 v56, v56, v54\n"
      "v_mul_f32 v57, v57, v54\n"
      "v_cvt_pk_bf16_f32 v210, v56, v57\n"
      "v_lshlrev_b32 v56, 16, v211\n"
      "v_and_b32 v57, 0xffff0000, v211\n"
      "v_mul_f32 v56, v56, v54\n"
      "v_mul_f32 v57, v57, v54\n"
      "v_cvt_pk_bf16_f32 v211, v56, v57\n"
      "s_waitcnt lgkmcnt(0)\n"
      "v_accvgpr_read_b32 v56, a96\n"
      "v_mul_f32 v56, v56, v28\n"
      "v_accvgpr_write_b32 a96, v56\n"
      "v_accvgpr_read_b32 v57, a97\n"
      "v_mul_f32 v57, v57, v29\n"
      "v_accvgpr_write_b32 a97, v57\n"
      "v_accvgpr_read_b32 v58, a98\n"
      "v_mul_f32 v58, v58, v30\n"
      "v_accvgpr_write_b32 a98, v58\n"
      "v_accvgpr_read_b32 v59, a99\n"
      "v_mul_f32 v59, v59, v31\n"
      "v_accvgpr_write_b32 a99, v59\n"
      "v_accvgpr_read_b32 v56, a100\n"
      "v_mul_f32 v56, v56, v28\n"
      "v_accvgpr_write_b32 a100, v56\n"
      "v_accvgpr_read_b32 v57, a101\n"
      "v_mul_f32 v57, v57, v29\n"
      "v_accvgpr_write_b32 a101, v57\n"
      "v_accvgpr_read_b32 v58, a102\n"
      "v_mul_f32 v58, v58, v30\n"
      "v_accvgpr_write_b32 a102, v58\n"
      "v_accvgpr_read_b32 v59, a103\n"
      "v_mul_f32 v59, v59, v31\n"
      "v_accvgpr_write_b32 a103, v59\n"
      "v_accvgpr_read_b32 v56, a104\n"
      "v_mul_f32 v56, v56, v28\n"
      "v_accvgpr_write_b32 a104, v56\n"
      "v_accvgpr_read_b32 v57, a105\n"
      "v_mul_f32 v57, v57, v29\n"
      "v_accvgpr_write_b32 a105, v57\n"
      "v_accvgpr_read_b32 v58, a106\n"
      "v_mul_f32 v58, v58, v30\n"
      "v_accvgpr_write_b32 a106, v58\n"
      "v_accvgpr_read_b32 v59, a107\n"
      "v_mul_f32 v59, v59, v31\n"
      "v_accvgpr_write_b32 a107, v59\n"
      "v_accvgpr_read_b32 v56, a108\n"
      "v_mul_f32 v56, v56, v28\n"
      "v_accvgpr_write_b32 a108, v56\n"
      "v_accvgpr_read_b32 v57, a109\n"
      "v_mul_f32 v57, v57, v29\n"
      "v_accvgpr_write_b32 a109, v57\n"
      "v_accvgpr_read_b32 v58, a110\n"
      "v_mul_f32 v58, v58, v30\n"
      "v_accvgpr_write_b32 a110, v58\n"
      "v_accvgpr_read_b32 v59, a111\n"
      "v_mul_f32 v59, v59, v31\n"
      "v_accvgpr_write_b32 a111, v59\n"
      "v_accvgpr_read_b32 v56, a112\n"
      "v_mul_f32 v56, v56, v28\n"
      "v_accvgpr_write_b32 a112, v56\n"
      "v_accvgpr_read_b32 v57, a113\n"
      "v_mul_f32 v57, v57, v29\n"
      "v_accvgpr_write_b32 a113, v57\n"
      "v_accvgpr_read_b32 v58, a114\n"
      "v_mul_f32 v58, v58, v30\n"
      "v_accvgpr_write_b32 a114, v58\n"
      "v_accvgpr_read_b32 v59, a115\n"
      "v_mul_f32 v59, v59, v31\n"
      "v_accvgpr_write_b32 a115, v59\n"
      "v_accvgpr_read_b32 v56, a116\n"
      "v_mul_f32 v56, v56, v28\n"
      "v_accvgpr_write_b32 a116, v56\n"
      "v_accvgpr_read_b32 v57, a117\n"
      "v_mul_f32 v57, v57, v29\n"
      "v_accvgpr_write_b32 a117, v57\n"
      "v_accvgpr_read_b32 v58, a118\n"
      "v_mul_f32 v58, v58, v30\n"
      "v_accvgpr_write_b32 a118, v58\n"
      "v_accvgpr_read_b32 v59, a119\n"
      "v_mul_f32 v59, v59, v31\n"
      "v_accvgpr_write_b32 a119, v59\n"
      "v_accvgpr_read_b32 v56, a120\n"
      "v_mul_f32 v56, v56, v28\n"
      "v_accvgpr_write_b32 a120, v56\n"
      "v_accvgpr_read_b32 v57, a121\n"
      "v_mul_f32 v57, v57, v29\n"
      "v_accvgpr_write_b32 a121, v57\n"
      "v_accvgpr_read_b32 v58, a122\n"
      "v_mul_f32 v58, v58, v30\n"
      "v_accvgpr_write_b32 a122, v58\n"
      "v_accvgpr_read_b32 v59, a123\n"
      "v_mul_f32 v59, v59, v31\n"
      "v_accvgpr_write_b32 a123, v59\n"
      "v_accvgpr_read_b32 v56, a124\n"
      "v_mul_f32 v56, v56, v28\n"
      "v_accvgpr_write_b32 a124, v56\n"
      "v_accvgpr_read_b32 v57, a125\n"
      "v_mul_f32 v57, v57, v29\n"
      "v_accvgpr_write_b32 a125, v57\n"
      "v_accvgpr_read_b32 v58, a126\n"
      "v_mul_f32 v58, v58, v30\n"
      "v_accvgpr_write_b32 a126, v58\n"
      "v_accvgpr_read_b32 v59, a127\n"
      "v_mul_f32 v59, v59, v31\n"
      "v_accvgpr_write_b32 a127, v59\n"
      "v_accvgpr_read_b32 v56, a128\n"
      "v_mul_f32 v56, v56, v28\n"
      "v_accvgpr_write_b32 a128, v56\n"
      "v_accvgpr_read_b32 v57, a129\n"
      "v_mul_f32 v57, v57, v29\n"
      "v_accvgpr_write_b32 a129, v57\n"
      "v_accvgpr_read_b32 v58, a130\n"
      "v_mul_f32 v58, v58, v30\n"
      "v_accvgpr_write_b32 a130, v58\n"
      "v_accvgpr_read_b32 v59, a131\n"
      "v_mul_f32 v59, v59, v31\n"
      "v_accvgpr_write_b32 a131, v59\n"
      "v_accvgpr_read_b32 v56, a132\n"
      "v_mul_f32 v56, v56, v28\n"
      "v_accvgpr_write_b32 a132, v56\n"
      "v_accvgpr_read_b32 v57, a133\n"
      "v_mul_f32 v57, v57, v29\n"
      "v_accvgpr_write_b32 a133, v57\n"
      "v_accvgpr_read_b32 v58, a134\n"
      "v_mul_f32 v58, v58, v30\n"
      "v_accvgpr_write_b32 a134, v58\n"
      "v_accvgpr_read_b32 v59, a135\n"
      "v_mul_f32 v59, v59, v31\n"
      "v_accvgpr_write_b32 a135, v59\n"
      "v_accvgpr_read_b32 v56, a136\n"
      "v_mul_f32 v56, v56, v28\n"
      "v_accvgpr_write_b32 a136, v56\n"
      "v_accvgpr_read_b32 v57, a137\n"
      "v_mul_f32 v57, v57, v29\n"
      "v_accvgpr_write_b32 a137, v57\n"
      "v_accvgpr_read_b32 v58, a138\n"
      "v_mul_f32 v58, v58, v30\n"
      "v_accvgpr_write_b32 a138, v58\n"
      "v_accvgpr_read_b32 v59, a139\n"
      "v_mul_f32 v59, v59, v31\n"
      "v_accvgpr_write_b32 a139, v59\n"
      "v_accvgpr_read_b32 v56, a140\n"
      "v_mul_f32 v56, v56, v28\n"
      "v_accvgpr_write_b32 a140, v56\n"
      "v_accvgpr_read_b32 v57, a141\n"
      "v_mul_f32 v57, v57, v29\n"
      "v_accvgpr_write_b32 a141, v57\n"
      "v_accvgpr_read_b32 v58, a142\n"
      "v_mul_f32 v58, v58, v30\n"
      "v_accvgpr_write_b32 a142, v58\n"
      "v_accvgpr_read_b32 v59, a143\n"
      "v_mul_f32 v59, v59, v31\n"
      "v_accvgpr_write_b32 a143, v59\n"
      "v_accvgpr_read_b32 v56, a200\n"
      "v_mul_f32 v56, v56, v54\n"
      "v_accvgpr_write_b32 a200, v56\n"
      "v_accvgpr_read_b32 v57, a201\n"
      "v_mul_f32 v57, v57, v54\n"
      "v_accvgpr_write_b32 a201, v57\n"
      "v_accvgpr_read_b32 v58, a202\n"
      "v_mul_f32 v58, v58, v54\n"
      "v_accvgpr_write_b32 a202, v58\n"
      "v_accvgpr_read_b32 v59, a203\n"
      "v_mul_f32 v59, v59, v54\n"
      "v_accvgpr_write_b32 a203, v59\n"
      "L_rare_t3:\n"
      "s_bitcmp0_b32 s60, 3\n"
      "s_cbranch_scc1 L_rare_t4\n"
      "v_max3_f32 v52, v48, v49, v50\n"
      "v_max_f32 v52, v52, v51\n"
      "s_bitcmp0_b32 s60, 5\n"
      "s_cbranch_scc1 L_rare_s3\n"
      "v_max3_f32 v52, v52, v228, v229\n"
      "v_max3_f32 v52, v52, v230, v231\n"
      "L_rare_s3:\n"
      "v_mov_b32 v53, v52\n"
      "s_nop 1\n"
      "v_permlane32_swap_b32 v52, v53\n"
      "v_max_f32 v52, v52, v53\n"
      "v_mov_b32 v53, v52\n"
      "s_nop 1\n"
      "v_permlane16_swap_b32 v52, v53\n"
      "v_max_f32 v52, v52, v53\n"
      "v_add_f32 v52, 0x41800000, v52\n"
      "v_ceil_f32 v52, v52\n"
      "v_max_f32 v52, 0, v52\n"
      "v_min_f32 v53, 0x447a0000, v52\n"
      "v_cvt_i32_f32 v53, v53\n"
      "v_sub_u32 v53, 0, v53\n"
      "v_ldexp_f32 v54, 1.0, v53\n"
      "v_sub_f32 v21, v21, v52\n"
      "v_sub_f32 v48, v48, v52\n"
      "v_sub_f32 v49, v49, v52\n"
      "v_sub_f32 v50, v50, v52\n"
      "v_sub_f32 v51, v51, v52\n"
      "s_cbranch_scc1 L_rare_u3\n"
      "v_sub_f32 v228, v228, v52\n"
      "v_sub_f32 v229, v229, v52\n"
      "v_sub_f32 v230, v230, v52\n"
      "v_sub_f32 v231, v231, v52\n"
      "L_rare_u3:\n"
      "v_lshlrev_b32 v56, 16, v184\n"
      "v_and_b32 v57, 0xffff0000, v184\n"
      "v_mul_f32 v56, v56, v54\n"
      "v_mul_f32 v57, v57, v54\n"
      "v_cvt_pk_bf16_f32 v184, v56, v57\n"
      "v_lshlrev_b32 v56, 16, v185\n"
      "v_and_b32 v57, 0xffff0000, v185\n"
      "v_mul_f32 v56, v56, v54\n"
      "v_mul_f32 v57, v57, v54\n"
      "v_cvt_pk_bf16_f32 v185, v56, v57\n"
      "v_lshlrev_b32 v56, 16, v186\n"
      "v_and_b32 v57, 0xffff0000, v186\n"
      "v_mul_f32 v56, v56, v54\n"
      "v_mul_f32 v57, v57, v54\n"
      "v_cvt_pk_bf16_f32 v186, v56, v57\n"
      "v_lshlrev_b32 v56, 16, v187\n"
      "v_and_b32 v57, 0xffff0000, v187\n"
      "v_mul_f32 v56, v56, v54\n"
      "v_mul_f32 v57, v57, v54\n"
      "v_cvt_pk_bf16_f32 v187, v56, v57\n"
      "s_bitcmp1_b32 s60, 4\n"
      "s_cbranch_scc1 L_rare_t4\n"
      "ds_write_b32 v62, v54 offset:0\n"
      "ds_read_b128 v[28:31], v63 offset:0\n"
      "v_lshlrev_b32 v56, 16, v212\n"
      "v_and_b32 v57, 0xffff0000, v212\n"
      "v_mul_f32 v56, v56, v54\n"
      "v_mul_f32 v57, v57, v54\n"
      "v_cvt_pk_bf16_f32 v212, v56, v57\n"
      "v_lshlrev_b32 v56, 16, v213\n"
      "v_and_b32 v57, 0xffff0000, v213\n"
      "v_mul_f32 v56, v56, v54\n"
      "v_mul_f32 v57, v57, v54\n"
      "v_cvt_pk_bf16_f32 v213, v56, v57\n"
      "v_lshlrev_b32 v56, 16, v214\n"
      "v_and_b32 v57, 0xffff0000, v214\n"
      "v_mul_f32 v56, v56, v54\n"
      "v_mul_f32 v57, v57, v54\n"
      "v_cvt_pk_bf16_f32 v214, v56, v57\n"
      "v_lshlrev_b32 v56, 16, v215\n"
      "v_and_b32 v57, 0xffff0000, v215\n"
      "v_mul_f32 v56, v56, v54\n"
      "v_mul_f32 v57, v57, v54\n"
      "v_cvt_pk_bf16_f32 v215, v56, v57\n"
      "s_waitcnt lgkmcnt(0)\n"
      "v_accvgpr_read_b32 v56, a144\n"
      "v_mul_f32 v56, v56, v28\n"
      "v_accvgpr_write_b32 a144, v56\n"
      "v_accvgpr_read_b32 v57, a145\n"
      "v_mul_f32 v57, v57, v29\n"
      "v_accvgpr_write_b32 a145, v57\n"
      "v_accvgpr_read_b32 v58, a146\n"
      "v_mul_f32 v58, v58, v30\n"
      "v_accvgpr_write_b32 a146, v58\n"
      "v_accvgpr_read_b32 v59, a147\n"
      "v_mul_f32 v59, v59, v31\n"
      "v_accvgpr_write_b32 a147, v59\n"
      "v_accvgpr_read_b32 v56, a148\n"
      "v_mul_f32 v56, v56, v28\n"
      "v_accvgpr_write_b32 a148, v56\n"
      "v_accvgpr_read_b32 v57, a149\n"
      "v_mul_f32 v57, v57, v29\n"
      "v_accvgpr_write_b32 a149, v57\n"
      "v_accvgpr_read_b32 v58, a150\n"
      "v_mul_f32 v58, v58, v30\n"
      "v_accvgpr_write_b32 a150, v58\n"
      "v_accvgpr_read_b32 v59, a151\n"
      "v_mul_f32 v59, v59, v31\n"
      "v_accvgpr_write_b32 a151, v59\n"
      "v_accvgpr_read_b32 v56, a152\n"
      "v_mul_f32 v56, v56, v28\n"
      "v_accvgpr_write_b32 a152, v56\n"
      "v_accvgpr_read_b32 v57, a153\n"
      "v_mul_f32 v57, v57, v29\n"
      "v_accvgpr_write_b32 a153, v57\n"
      "v_accvgpr_read_b32 v58, a154\n"
      "v_mul_f32 v58, v58, v30\n"
      "v_accvgpr_write_b32 a154, v58\n"
      "v_accvgpr_read_b32 v59, a155\n"
      "v_mul_f32 v59, v59, v31\n"
      "v_accvgpr_write_b32 a155, v59\n"
      "v_accvgpr_read_b32 v56, a156\n"
      "v_mul_f32 v56, v56, v28\n"
      "v_accvgpr_write_b32 a156, v56\n"
      "v_accvgpr_read_b32 v57, a157\n"
      "v_mul_f32 v57, v57, v29\n"
      "v_accvgpr_write_b32 a157, v57\n"
      "v_accvgpr_read_b32 v58, a158\n"
      "v_mul_f32 v58, v58, v30\n"
      "v_accvgpr_write_b32 a158, v58\n"
      "v_accvgpr_read_b32 v59, a159\n"
      "v_mul_f32 v59, v59, v31\n"
      "v_accvgpr_write_b32 a159, v59\n"
      "v_accvgpr_read_b32 v56, a160\n"
      "v_mul_f32 v56, v56, v28\n"
      "v_accvgpr_write_b32 a160, v56\n"
      "v_accvgpr_read_b32 v57, a161\n"
      "v_mul_f32 v57, v57, v29\n"
      "v_accvgpr_write_b32 a161, v57\n"
      "v_accvgpr_read_b32 v58, a162\n"
      "v_mul_f32 v58, v58, v30\n"
      "v_accvgpr_write_b32 a162, v58\n"
      "v_accvgpr_read_b32 v59, a163\n"
      "v_mul_f32 v59, v59, v31\n"
      "v_accvgpr_write_b32 a163, v59\n"
      "v_accvgpr_read_b32 v56, a164\n"
      "v_mul_f32 v56, v56, v28\n"
      "v_accvgpr_write_b32 a164, v56\n"
      "v_accvgpr_read_b32 v57, a165\n"
      "v_mul_f32 v57, v57, v29\n"
      "v_accvgpr_write_b32 a165, v57\n"
      "v_accvgpr_read_b32 v58, a166\n"
      "v_mul_f32 v58, v58, v30\n"
      "v_accvgpr_write_b32 a166, v58\n"
      "v_accvgpr_read_b32 v59, a167\n"
      "v_mul_f32 v59, v59, v31\n"
      "v_accvgpr_write_b32 a167, v59\n"
      "v_accvgpr_read_b32 v56, a168\n"
      "v_mul_f32 v56, v56, v28\n"
      "v_accvgpr_write_b32 a168, v56\n"
      "v_accvgpr_read_b32 v57, a169\n"
      "v_mul_f32 v57, v57, v29\n"
      "v_accvgpr_write_b32 a169, v57\n"
      "v_accvgpr_read_b32 v58, a170\n"
      "v_mul_f32 v58, v58, v30\n"
      "v_accvgpr_write_b32 a170, v58\n"
      "v_accvgpr_read_b32 v59, a171\n"
      "v_mul_f32 v59, v59, v31\n"
      "v_accvgpr_write_b32 a171, v59\n"
      "v_accvgpr_read_b32 v56, a172\n"
      "v_mul_f32 v56, v56, v28\n"
      "v_accvgpr_write_b32 a172, v56\n"
      "v_accvgpr_read_b32 v57, a173\n"
      "v_mul_f32 v57, v57, v29\n"
      "v_accvgpr_write_b32 a173, v57\n"
      "v_accvgpr_read_b32 v58, a174\n"
      "v_mul_f32 v58, v58, v30\n"
      "v_accvgpr_write_b32 a174, v58\n"
      "v_accvgpr_read_b32 v59, a175\n"
      "v_mul_f32 v59, v59, v31\n"
      "v_accvgpr_write_b32 a175, v59\n"
      "v_accvgpr_read_b32 v56, a176\n"
      "v_mul_f32 v56, v56, v28\n"
      "v_accvgpr_write_b32 a176, v56\n"
      "v_accvgpr_read_b32 v57, a177\n"
      "v_mul_f32 v57, v57, v29\n"
      "v_accvgpr_write_b32 a177, v57\n"
      "v_accvgpr_read_b32 v58, a178\n"
      "v_mul_f32 v58, v58, v30\n"
      "v_accvgpr_write_b32 a178, v58\n"
      "v_accvgpr_read_b32 v59, a179\n"
      "v_mul_f32 v59, v59, v31\n"
      "v_accvgpr_write_b32 a179, v59\n"
      "v_accvgpr_read_b32 v56, a180\n"
      "v_mul_f32 v56, v56, v28\n"
      "v_accvgpr_write_b32 a180, v56\n"
      "v_accvgpr_read_b32 v57, a181\n"
      "v_mul_f32 v57, v57, v29\n"
      "v_accvgpr_write_b32 a181, v57\n"
      "v_accvgpr_read_b32 v58, a182\n"
      "v_mul_f32 v58, v58, v30\n"
      "v_accvgpr_write_b32 a182, v58\n"
      "v_accvgpr_read_b32 v59, a183\n"
      "v_mul_f32 v59, v59, v31\n"
      "v_accvgpr_write_b32 a183, v59\n"
      "v_accvgpr_read_b32 v56, a184\n"
      "v_mul_f32 v56, v56, v28\n"
      "v_accvgpr_write_b32 a184, v56\n"
      "v_accvgpr_read_b32 v57, a185\n"
      "v_mul_f32 v57, v57, v29\n"
      "v_accvgpr_write_b32 a185, v57\n"
      "v_accvgpr_read_b32 v58, a186\n"
      "v_mul_f32 v58, v58, v30\n"
      "v_accvgpr_write_b32 a186, v58\n"
      "v_accvgpr_read_b32 v59, a187\n"
      "v_mul_f32 v59, v59, v31\n"
      "v_accvgpr_write_b32 a187, v59\n"
      "v_accvgpr_read_b32 v56, a188\n"
      "v_mul_f32 v56, v56, v28\n"
      "v_accvgpr_write_b32 a188, v56\n"
      "v_accvgpr_read_b32 v57, a189\n"
      "v_mul_f32 v57, v57, v29\n"
      "v_accvgpr_write_b32 a189, v57\n"
      "v_accvgpr_read_b32 v58, a190\n"
      "v_mul_f32 v58, v58, v30\n"
      "v_accvgpr_write_b32 a190, v58\n"
      "v_accvgpr_read_b32 v59, a191\n"
      "v_mul_f32 v59, v59, v31\n"
      "v_accvgpr_write_b32 a191, v59\n"
      "v_accvgpr_read_b32 v56, a204\n"
      "v_mul_f32 v56, v56, v54\n"
      "v_accvgpr_write_b32 a204, v56\n"
      "v_accvgpr_read_b32 v57, a205\n"
      "v_mul_f32 v57, v57, v54\n"
      "v_accvgpr_write_b32 a205, v57\n"
      "v_accvgpr_read_b32 v58, a206\n"
      "v_mul_f32 v58, v58, v54\n"
      "v_accvgpr_write_b32 a206, v58\n"
      "v_accvgpr_read_b32 v59, a207\n"
      "v_mul_f32 v59, v59, v54\n"
      "v_accvgpr_write_b32 a207, v59\n"
      "L_rare_t4:\n"
      "s_setpc_b64 s[56:57]\n"
      "L_end:\n"
      :
      : "s"(__builtin_amdgcn_kernarg_segment_ptr()), "s"(blockIdx.x), "s"(wave), "s"((unsigned)(size_t)lds)
      : "memory", "vcc", "scc", "s4", "s5", "s6", "s7", "s8", "s9", "s10", "s11", "s12", "s13", "s14", "s15", "s16", "s20", "s21", "s22", "s23", "s24", "s25", "s26", "s27", "s28", "s29", "s30", "s31", "s34", "s35", "s36", "s37", "s38", "s39", "s40", "s41", "s42", "s43", "s44", "s45", "s46", "s47", "s48", "s49", "s50", "s51", "s52", "s53", "s54", "s55", "s56", "s57", "s58", "s59", "s60", "s61", "s62", "s63", "s64", "s65", "s66", "s67", "s68", "s69", "s70", "s71", "s72", "s73", "s74", "s75", "s76", "s77", "s78", "s79", "s80", "s81", "s82", "s83", "s84", "s85", "s86", "s87", "s88", "s89", "s90", "s91", "s92", "s93", "s94", "s95", "s96", "s97", "s98", "s99", "v0", "v1", "v2", "v3", "v4", "v5", "v6", "v7", "v8", "v9", "v10", "v11", "v12", "v13", "v14", "v15", "v16", "v17", "v18", "v19", "v20", "v21", "v22", "v23", "v24", "v25", "v26", "v27", "v28", "v29", "v30", "v31", "v32", "v33", "v34", "v35", "v36", "v37", "v38", "v39", "v40", "v41", "v42", "v43", "v44", "v45", "v46", "v47", "v48", "v49", "v50", "v51", "v52", "v53", "v54", "v55", "v56", "v57", "v58", "v59", "v60", "v61", "v62", "v63", "v64", "v65", "v66", "v67", "v68", "v69", "v70", "v71", "v72", "v73", "v74", "v75", "v76", "v77", "v78", "v79", "v80", "v81", "v82", "v83", "v84", "v85", "v86", "v87", "v88", "v89", "v90", "v91", "v92", "v93", "v94", "v95", "v96", "v97", "v98", "v99", "v100", "v101", "v102", "v103", "v104", "v105", "v106", "v107", "v108", "v109", "v110", "v111", "v112", "v113", "v114", "v115", "v116", "v117", "v118", "v119", "v120", "v121", "v122", "v123", "v124", "v125", "v126", "v127", "v128", "v129", "v130", "v131", "v132", "v133", "v134", "v135", "v136", "v137", "v138", "v139", "v140", "v141", "v142", "v143", "v144", "v145", "v146", "v147", "v148", "v149", "v150", "v151", "v152", "v153", "v154", "v155", "v156", "v157", "v158", "v159", "v160", "v161", "v162", "v163", "v164", "v165", "v166", "v167", "v168", "v169", "v170", "v171", "v172", "v173", "v174", "v175", "v176", "v177", "v178", "v179", "v180", "v181", "v182", "v183", "v184", "v185", "v186", "v187", "v188", "v189", "v190", "v191", "v192", "v193", "v194", "v195", "v196", "v197", "v198", "v199", "v200", "v201", "v202", "v203", "v204", "v205", "v206", "v207", "v208", "v209", "v210", "v211", "v212", "v213", "v214", "v215", "v216", "v217", "v218", "v219", "v220", "v221", "v222", "v223", "v224", "v225", "v226", "v227", "v228", "v229", "v230", "v231", "v232", "v233", "v234", "v235", "v236", "v237", "v238", "v239", "v240", "v241", "v242", "v243", "v244", "v245", "v246", "v247", "v248", "v249", "v250", "v251", "v252", "v253", "v254", "v255", "a0", "a1", "a2", "a3", "a4", "a5", "a6", "a7", "a8", "a9", "a10", "a11", "a12", "a13", "a14", "a15", "a16", "a17", "a18", "a19", "a20", "a21", "a22", "a23", "a24", "a25", "a26", "a27", "a28", "a29", "a30", "a31", "a32", "a33", "a34", "a35", "a36", "a37", "a38", "a39", "a40", "a41", "a42", "a43", "a44", "a45", "a46", "a47", "a48", "a49", "a50", "a51", "a52", "a53", "a54", "a55", "a56", "a57", "a58", "a59", "a60", "a61", "a62", "a63", "a64", "a65", "a66", "a67", "a68", "a69", "a70", "a71", "a72", "a73", "a74", "a75", "a76", "a77", "a78", "a79", "a80", "a81", "a82", "a83", "a84", "a85", "a86", "a87", "a88", "a89", "a90", "a91", "a92", "a93", "a94", "a95", "a96", "a97", "a98", "a99", "a100", "a101", "a102", "a103", "a104", "a105", "a106", "a107", "a108", "a109", "a110", "a111", "a112", "a113", "a114", "a115", "a116", "a117", "a118", "a119", "a120", "a121", "a122", "a123", "a124", "a125", "a126", "a127", "a128", "a129", "a130", "a131", "a132", "a133", "a134", "a135", "a136", "a137", "a138", "a139", "a140", "a141", "a142", "a143", "a144", "a145", "a146", "a147", "a148", "a149", "a150", "a151", "a152", "a153", "a154", "a155", "a156", "a157", "a158", "a159", "a160", "a161", "a162", "a163", "a164", "a165", "a166", "a167", "a168", "a169", "a170", "a171", "a172", "a173", "a174", "a175", "a176", "a177", "a178", "a179", "a180", "a181", "a182", "a183", "a184", "a185", "a186", "a187", "a188", "a189", "a190", "a191", "a192", "a193", "a194", "a195", "a196", "a197", "a198", "a199", "a200", "a201", "a202", "a203", "a204", "a205", "a206", "a207", "a208", "a209", "a210", "a211", "a212", "a213", "a214", "a215", "a216", "a217", "a218", "a219", "a220", "a221", "a222", "a223", "a224", "a225", "a226", "a227", "a228", "a229", "a230", "a231", "a232", "a233", "a234", "a235", "a236", "a237", "a238", "a239", "a240", "a241", "a242", "a243", "a244", "a245", "a246", "a247", "a248", "a249", "a250", "a251", "a252", "a253", "a254", "a255");
}

extern "C" void kernel_launch(void* const* d_in, const int* in_sizes, int n_in, void* d_out, int out_size, void* d_ws, size_t ws_size,
                              hipStream_t stream) {
  attn_fwd_d192<<<dim3(256), dim3(256), 0, stream>>>(d_in[0], d_in[1], d_in[2], d_out);
}
```
